```python
import torch

def _softmax(x, axis=-1):
    return torch.softmax(x, dim=axis)
def _gelu(x):
    return torch.nn.functional.gelu(x, approximate="tanh")
def _silu(x):
    return torch.nn.functional.silu(x)
def _sigmoid(x):
    return torch.sigmoid(x)
def _layernorm(x, axis=-1, eps=1e-5):
    m = x.mean(dim=axis, keepdim=True)
    v = x.var(dim=axis, keepdim=True, unbiased=False)
    return (x - m) / torch.sqrt(v + eps)
def _scatter_add(tgt, idx, src, axis=0):
    return tgt.clone().index_add_(axis, idx, src)
def _segment_sum(src, seg, n):
    out = torch.zeros((n,) + src.shape[1:], dtype=src.dtype)
    return out.index_add_(0, seg, src)
def _pad(x, pw):
    flat = tuple(v for pair in reversed(pw) for v in pair)
    return torch.nn.functional.pad(x, flat)

kernel_name = 'gated_mix__gqa_block_cached__dense_activation_squared_relu__decode'

def setup_inputs():

    _w = (lambda a: (lambda t: t.to(torch.bfloat16) if t.is_floating_point() else t)(torch.as_tensor(a)))
    def _A_setup():
        B, Sq, D = 8, 1, 512
        Hq, Hkv, Sk, Dh = 32, 8, 4096, 128
        assert Hq % Hkv == 0, (Hq, Hkv)
        return {
            "x": _w((lambda *s: torch.randn(*s, dtype=torch.bfloat16) * 0.05)(B, Sq, D)),
            "k_cache": _w((lambda *s: torch.randn(*s, dtype=torch.bfloat16) * 6.4)(B, Hkv, Sk, Dh)),
            "v_cache": _w((lambda *s: torch.randn(*s, dtype=torch.bfloat16) * 0.4)(B, Hkv, Sk, Dh)),
            "wq": _w((lambda *s: torch.randn(*s, dtype=torch.bfloat16) * 6.4)(D, Hq * Dh) * (D ** -0.5)),
            "wo": _w((lambda *s: torch.randn(*s, dtype=torch.bfloat16) * 0.4)(Hq * Dh, D) * ((Hq * Dh) ** -0.5)),
        }
    _A_in = _A_setup()

    def _B_setup():
        B, S, D, F = 8, 1, 512, 1792
        return {
            "x": _w((lambda *s: torch.randn(*s, dtype=torch.bfloat16) * 0.05)(B, S, D)),
            "w1": _w((lambda *s: torch.randn(*s, dtype=torch.bfloat16) * 0.05)(D, F)),
            "b1": _w((lambda *s: torch.randn(*s, dtype=torch.bfloat16) * 0.05)(F)),
            "w2": _w((lambda *s: torch.randn(*s, dtype=torch.bfloat16) * 0.05)(F, D)),
            "b2": _w((lambda *s: torch.randn(*s, dtype=torch.bfloat16) * 0.05)(D)),
        }
    _B_in = _B_setup()

    return {
        "A_x": _A_in["x"],
        "A_k_cache": _A_in["k_cache"],
        "A_v_cache": _A_in["v_cache"],
        "A_wq": _A_in["wq"],
        "A_wo": _A_in["wo"],
        "B_x": _B_in["x"],
        "B_w1": _B_in["w1"],
        "B_b1": _B_in["b1"],
        "B_w2": _B_in["w2"],
        "B_b2": _B_in["b2"],
        "gate_w": _w((lambda *s: torch.randn(*s, dtype=torch.bfloat16) * 0.05)(512) * 16.0),
    }

def reference(A_x, A_k_cache, A_v_cache, A_wq, A_wo, B_x, B_w1, B_b1, B_w2, B_b2, gate_w):
    def _A_ref(x, k_cache, v_cache, wq, wo):
        B, Sq, D = x.shape
        Hq, Dh = 32, 128
        Hkv = k_cache.shape[1]
        rep = Hq // Hkv
        q = (lambda x, *d: x.permute(*d))((x @ wq).reshape(B, Sq, Hq, Dh), 0, 2, 1, 3)
        k = (lambda x, n, axis: torch.repeat_interleave(x, n, dim=axis))(k_cache, rep, axis=1)
        v = (lambda x, n, axis: torch.repeat_interleave(x, n, dim=axis))(v_cache, rep, axis=1)
        scores = torch.einsum("bhqd,bhkd->bhqk", q, k) * (Dh ** -0.5)
        p = _softmax(scores, axis=-1)
        o = torch.einsum("bhqk,bhkd->bhqd", p, v)
        return (lambda x, *d: x.permute(*d))(o, 0, 2, 1, 3).reshape(B, Sq, Hq * Dh) @ wo

    def _B_ref(x, w1, b1, w2, b2):
        h = torch.einsum("bsd,df->bsf", x, w1) + b1
        zero = torch.zeros_like(h)
        r = torch.where(h > zero, h, zero)
        h = r * r
        out = torch.einsum("bsf,fd->bsd", h, w2) + b2
        return out

    x = A_x
    g = _sigmoid((x * gate_w).sum(-1, keepdims=True))
    a = _A_ref(x, A_k_cache, A_v_cache, A_wq, A_wo)
    b = _B_ref(x, B_w1, B_b1, B_w2, B_b2)
    return g * a + (1 - g) * b
```

```cpp
#include <hip/hip_runtime.h>
#include <hip/hip_bf16.h>
#include <stdint.h>

typedef unsigned v4u __attribute__((ext_vector_type(4)));
typedef unsigned v2u __attribute__((ext_vector_type(2)));
typedef float f32x4 __attribute__((ext_vector_type(4)));
typedef __bf16 bf16x8 __attribute__((ext_vector_type(8)));
typedef __bf16 bf16x2 __attribute__((ext_vector_type(2)));
typedef short s16x4 __attribute__((ext_vector_type(4)));
#define GAS __attribute__((address_space(1)))
#define LAS __attribute__((address_space(3)))

constexpr int NB = 8, DM = 512, HQ = 32, HKV = 8, SK = 4096, DH = 128, FF = 1792;

__device__ __forceinline__ f32x4 mfma32(v4u a, v4u b, f32x4 c) {
  return __builtin_amdgcn_mfma_f32_16x16x32_bf16(__builtin_bit_cast(bf16x8, a), __builtin_bit_cast(bf16x8, b), c, 0, 0, 0);
}
__device__ __forceinline__ f32x4 mfma16(v2u a, v2u b, f32x4 c) {
  return __builtin_amdgcn_mfma_f32_16x16x16bf16_1k(__builtin_bit_cast(s16x4, a), __builtin_bit_cast(s16x4, b), c, 0, 0, 0);
}
__device__ __forceinline__ unsigned cvtpk(float lo, float hi) {
  bf16x2 r = {(__bf16)lo, (__bf16)hi};
  return __builtin_bit_cast(unsigned, r);
}
__device__ __forceinline__ float bf_lo(unsigned u) { return __builtin_bit_cast(float, u << 16); }
__device__ __forceinline__ float bf_hi(unsigned u) { return __builtin_bit_cast(float, u & 0xffff0000u); }
__device__ __forceinline__ float rbf(float x) { return (float)(__bf16)x; }
template <int CTRL> __device__ __forceinline__ float dpp(float x) {
  return __builtin_bit_cast(float, __builtin_amdgcn_mov_dpp(__builtin_bit_cast(int, x), CTRL, 0xf, 0xf, true));
}
template <int CTRL> __device__ __forceinline__ unsigned dppu(unsigned x) {
  return (unsigned)__builtin_amdgcn_mov_dpp((int)x, CTRL, 0xf, 0xf, true);
}
__device__ __forceinline__ float xrow_max(float x) {
  auto s = __builtin_amdgcn_permlane16_swap(__float_as_uint(x), __float_as_uint(x), false, false);
  x = fmaxf(__uint_as_float(s[0]), __uint_as_float(s[1]));
  auto t = __builtin_amdgcn_permlane32_swap(__float_as_uint(x), __float_as_uint(x), false, false);
  return fmaxf(__uint_as_float(t[0]), __uint_as_float(t[1]));
}
__device__ __forceinline__ float xrow_sum(float x) {
  auto s = __builtin_amdgcn_permlane16_swap(__float_as_uint(x), __float_as_uint(x), false, false);
  x = __uint_as_float(s[0]) + __uint_as_float(s[1]);
  auto t = __builtin_amdgcn_permlane32_swap(__float_as_uint(x), __float_as_uint(x), false, false);
  return __uint_as_float(t[0]) + __uint_as_float(t[1]);
}
__device__ __forceinline__ v2u tr_read(const LAS void* p) {
  return __builtin_bit_cast(v2u, __builtin_amdgcn_ds_read_tr16_b64_v4i16((LAS s16x4*)p));
}

constexpr size_t WS_Q = 0;
constexpr size_t WS_H = 65536;
constexpr size_t WS_P = 131072;
constexpr size_t WS_LSE = WS_P + 32 * 8 * 4 * 128 * 2;
constexpr size_t WS_O = WS_LSE + 4096;

__global__ void __launch_bounds__(64) k_pre(const unsigned short* x, const unsigned short* wq, const unsigned short* w1,
                                            const unsigned short* b1, unsigned char* ws) {
  __shared__ __attribute__((aligned(16))) unsigned char lds[16384];
  const int l = threadIdx.x, blk = blockIdx.x;
  const bool isq = blk < 256;
  const int tile = isq ? blk : blk - 256;
  const unsigned short* W = isq ? wq : w1;
  const int ld = isq ? 4096 : FF;
  const int n0 = tile * 16;
  v4u w[16], xa[16];
#pragma unroll
  for (int i = 0; i < 16; ++i) w[i] = *(const v4u*)(W + (size_t)(32 * i + (l >> 1)) * ld + n0 + 8 * (l & 1));
#pragma unroll
  for (int i = 0; i < 16; ++i) xa[i] = *(const v4u*)(x + (l & 7) * DM + 32 * i + 8 * (l >> 4));
#pragma unroll
  for (int i = 0; i < 16; ++i) *(v4u*)(lds + i * 1024 + l * 16) = w[i];
  __syncthreads();
  f32x4 acc = {0.f, 0.f, 0.f, 0.f};
  const int g = l >> 4, qq = (l & 15) >> 2, p = l & 3;
#pragma unroll
  for (int i = 0; i < 16; ++i) {
    const LAS unsigned char* base = (const LAS unsigned char*)lds + (32 * i + 8 * g + qq) * 32 + p * 8;
    v2u t0 = tr_read(base), t1 = tr_read(base + 4 * 32);
    v4u wf = {t0.x, t0.y, t1.x, t1.y};
    acc = mfma32(wf, xa[i], acc);
  }
  if ((l & 15) < 8) {
    const int b = l & 7, n = n0 + 4 * g;
    if (isq) {
      v2u o = {cvtpk(acc[0], acc[1]), cvtpk(acc[2], acc[3])};
      *(v2u*)(ws + WS_Q + ((size_t)b * 4096 + n) * 2) = o;
    } else {
      const v2u bb = *(const v2u*)(b1 + n);
      float h[4];
      h[0] = rbf(rbf(acc[0]) + bf_lo(bb.x)); h[1] = rbf(rbf(acc[1]) + bf_hi(bb.x));
      h[2] = rbf(rbf(acc[2]) + bf_lo(bb.y)); h[3] = rbf(rbf(acc[3]) + bf_hi(bb.y));
#pragma unroll
      for (int i = 0; i < 4; ++i) { float r = h[i] > 0.f ? h[i] : 0.f; h[i] = r * r; }
      v2u o = {cvtpk(h[0], h[1]), cvtpk(h[2], h[3])};
      *(v2u*)(ws + WS_H + ((size_t)b * FF + n) * 2) = o;
    }
  }
}

#define KV_NT 1
__device__ __forceinline__ v4u ldkv(const unsigned char* p) {
  return __builtin_nontemporal_load((const GAS v4u*)p);
}

__global__ void __launch_bounds__(512, 2) k_attn(const unsigned char* kc, const unsigned char* vc, unsigned char* ws) {
  __shared__ __attribute__((aligned(16))) float lds[8 * 4 * 128 + 64];
  const int tid = threadIdx.x, l = tid & 63, w = __builtin_amdgcn_readfirstlane(tid >> 6);
  const int c = blockIdx.x, kvh = c & 7, i32 = c >> 3, b = i32 >> 2, sp = i32 & 3;
  const size_t base = ((size_t)(b * HKV + kvh) * SK + sp * 1024 + w * 128) * 256;
  const unsigned char* kp = kc + base + ((l >> 2) & 3) * 256 + (l >> 4) * 64 + (l & 3) * 16;
  const unsigned char* vp = vc + base + l * 16;
  v4u kv[32];
  const v4u qf = *(const v4u*)(ws + WS_Q + ((size_t)b * 4096 + kvh * 512 + ((l >> 2) & 3) * 128 + (l >> 4) * 32 + (l & 3) * 8) * 2);
  __builtin_amdgcn_sched_barrier(0);
#pragma unroll
  for (int i = 0; i < 32; ++i) kv[i] = ldkv(kp + i * 1024);
  __builtin_amdgcn_sched_barrier(0);
  constexpr float C2 = 0.088388347648318440f * 1.4426950408889634f;
  float sc[8];
  const int s = l & 3;
#pragma unroll
  for (int i = 0; i < 32; ++i) {
    f32x4 d = mfma32(kv[i], qf, f32x4{0.f, 0.f, 0.f, 0.f});
    kv[i] = ldkv(vp + i * 1024);
    float t = (dpp<0x00>(d[0]) + dpp<0x55>(d[1])) + (dpp<0xAA>(d[2]) + dpp<0xFF>(d[3]));
    if ((i & 3) == 0) sc[i >> 2] = t; else sc[i >> 2] = (s == (i & 3)) ? t : sc[i >> 2];
    __builtin_amdgcn_sched_barrier(0);
  }
  float mx = sc[0];
#pragma unroll
  for (int m = 1; m < 8; ++m) mx = fmaxf(mx, sc[m]);
  mx = fmaxf(mx, dpp<0xB1>(mx)); mx = fmaxf(mx, dpp<0x4E>(mx)); mx = xrow_max(mx);
  const float mneg = -mx * C2;
  float lsum = 0.f;
  unsigned pk[8];
#pragma unroll
  for (int m = 0; m < 8; ++m) { float p = __builtin_amdgcn_exp2f(fmaf(sc[m], C2, mneg)); pk[m] = cvtpk(p, p); lsum += bf_lo(pk[m]); }
  lsum += dpp<0xB1>(lsum); lsum += dpp<0x4E>(lsum); lsum = xrow_sum(lsum);
  const unsigned half = (l & 1) ? 0xffff0000u : 0x0000ffffu;
  const unsigned m0 = (l & 2) ? 0u : half, m1 = (l & 2) ? half : 0u;
  f32x4 o0 = {0.f, 0.f, 0.f, 0.f}, o1 = {0.f, 0.f, 0.f, 0.f};
#pragma unroll
  for (int i = 0; i < 32; ++i) {
    unsigned pb = (i & 3) == 0 ? dppu<0x00>(pk[i >> 2]) : (i & 3) == 1 ? dppu<0x55>(pk[i >> 2]) : (i & 3) == 2 ? dppu<0xAA>(pk[i >> 2]) : dppu<0xFF>(pk[i >> 2]);
    v2u a = {pb & m0, pb & m1};
    o0 = mfma16(a, v2u{kv[i].x, kv[i].y}, o0);
    o1 = mfma16(a, v2u{kv[i].z, kv[i].w}, o1);
  }
  *(f32x4*)(lds + (w * 4 + (l >> 4)) * 128 + (l & 15) * 8) = o0;
  *(f32x4*)(lds + (w * 4 + (l >> 4)) * 128 + (l & 15) * 8 + 4) = o1;
  if (l < 16 && (l & 3) == 0) { lds[4096 + (l >> 2) * 16 + w * 2] = mx * C2; lds[4096 + (l >> 2) * 16 + w * 2 + 1] = lsum; }
  __syncthreads();
  if (tid < 256) {
    const int h = tid >> 6, d = (tid & 63) * 2;
    float mw[8], lw[8], M = -INFINITY;
#pragma unroll
    for (int j = 0; j < 8; ++j) { mw[j] = lds[4096 + h * 16 + j * 2]; lw[j] = lds[4096 + h * 16 + j * 2 + 1]; M = fmaxf(M, mw[j]); }
    float L = 0.f, a0 = 0.f, a1 = 0.f;
#pragma unroll
    for (int j = 0; j < 8; ++j) {
      const float e = __builtin_amdgcn_exp2f(mw[j] - M);
      L = fmaf(e, lw[j], L);
      const float2 ov = *(const float2*)(lds + (j * 4 + h) * 128 + d);
      a0 = fmaf(e, ov.x, a0); a1 = fmaf(e, ov.y, a1);
    }
    const float inv = 1.f / L;
    const int hh = kvh * 4 + h;
    *(unsigned*)(ws + WS_P + (((size_t)(hh * 8 + b) * 4 + sp) * 128 + d) * 2) = cvtpk(a0 * inv, a1 * inv);
    if ((tid & 63) == 0) *(float*)(ws + WS_LSE + ((hh * 8 + b) * 4 + sp) * 4) = M + __builtin_amdgcn_logf(L);
  }
}

__global__ void __launch_bounds__(256) k_post(const unsigned short* wo, const unsigned short* w2, unsigned char* ws) {
  __shared__ __attribute__((aligned(16))) unsigned char lds[16384];
  const int tid = threadIdx.x, l = tid & 63, w = tid >> 6, blk = blockIdx.x;
  const int h = blk >> 3, n = blk & 7;
  const bool iso = h < 32;
  const unsigned short* W = iso ? wo + (size_t)h * 128 * DM : w2 + (size_t)(h - 32) * 128 * DM;
#pragma unroll
  for (int i = 0; i < 4; ++i) {
    const int e = i * 256 + tid, row = e >> 3, ch = e & 7;
    *(v4u*)(lds + e * 16) = *(const v4u*)(W + (size_t)row * DM + n * 64 + ch * 8);
  }
  __syncthreads();
  const int g = l >> 4, qq = (l & 15) >> 2, p = l & 3;
  v4u wf[4];
#pragma unroll
  for (int k = 0; k < 4; ++k) {
    const LAS unsigned char* base = (const LAS unsigned char*)lds + (32 * k + 8 * g + qq) * 128 + w * 32 + p * 8;
    v2u t0 = tr_read(base), t1 = tr_read(base + 4 * 128);
    wf[k] = v4u{t0.x, t0.y, t1.x, t1.y};
  }
  const int b = l & 7, sl = (l >> 3) & 1;
  f32x4 r;
  if (iso) {
    const f32x4 ls = *(const f32x4*)(ws + WS_LSE + (h * 8 + b) * 16);
    const float M = fmaxf(fmaxf(ls[0], ls[1]), fmaxf(ls[2], ls[3]));
    const float e0 = __builtin_amdgcn_exp2f(ls[0] - M), e1 = __builtin_amdgcn_exp2f(ls[1] - M), e2 = __builtin_amdgcn_exp2f(ls[2] - M), e3 = __builtin_amdgcn_exp2f(ls[3] - M);
    const float inv = 1.f / ((e0 + e1) + (e2 + e3));
    const float wa = (sl ? e1 : e0) * inv, wb = (sl ? e3 : e2) * inv;
    f32x4 da = {0.f, 0.f, 0.f, 0.f}, db = {0.f, 0.f, 0.f, 0.f};
#pragma unroll
    for (int k = 0; k < 4; ++k) {
      const v4u pa = *(const v4u*)(ws + WS_P + (((size_t)(h * 8 + b) * 4 + sl) * 128 + 32 * k + 8 * g) * 2);
      const v4u pb = *(const v4u*)(ws + WS_P + (((size_t)(h * 8 + b) * 4 + 2 + sl) * 128 + 32 * k + 8 * g) * 2);
      da = mfma32(wf[k], pa, da);
      db = mfma32(wf[k], pb, db);
    }
#pragma unroll
    for (int j = 0; j < 4; ++j) { float t = da[j] * wa + db[j] * wb; r[j] = t + dpp<0x128>(t); }
  } else {
    f32x4 da = {0.f, 0.f, 0.f, 0.f};
#pragma unroll
    for (int k = 0; k < 4; ++k) {
      const v4u pa = *(const v4u*)(ws + WS_H + ((size_t)b * FF + (h - 32) * 128 + 32 * k + 8 * g) * 2);
      da = mfma32(wf[k], pa, da);
    }
    r = da;
  }
  if (sl == 0) {
    const int rr = b * 32 + n * 4 + w;
    *(f32x4*)(ws + WS_O + ((size_t)(rr * 46 + h) * 16 + 4 * g) * 4) = r;
  }
}

__global__ void __launch_bounds__(64) k_fin(const unsigned short* x, const unsigned short* gw, const unsigned short* b2,
                                            const unsigned char* ws, unsigned short* out) {
  const int l = threadIdx.x, r = blockIdx.x, b = r >> 5, ct = r & 31;
  const int hh = l >> 2, q4 = l & 3;
  const f32x4 a0 = *(const f32x4*)(ws + WS_O + ((size_t)(r * 46 + hh) * 16 + 4 * q4) * 4);
  const f32x4 a1 = *(const f32x4*)(ws + WS_O + ((size_t)(r * 46 + 16 + hh) * 16 + 4 * q4) * 4);
  f32x4 f = {0.f, 0.f, 0.f, 0.f};
  if (hh < 14) f = *(const f32x4*)(ws + WS_O + ((size_t)(r * 46 + 32 + hh) * 16 + 4 * q4) * 4);
  const v4u xv = *(const v4u*)(x + b * DM + l * 8), gv = *(const v4u*)(gw + l * 8);
  float gs = 0.f;
#pragma unroll
  for (int j = 0; j < 4; ++j) { gs += rbf(bf_lo(xv[j]) * bf_lo(gv[j])); gs += rbf(bf_hi(xv[j]) * bf_hi(gv[j])); }
  gs += dpp<0xB1>(gs); gs += dpp<0x4E>(gs); gs += dpp<0x124>(gs); gs += dpp<0x128>(gs); gs = xrow_sum(gs);
  gs = rbf(gs);
  const float g = rbf(1.f / (1.f + __expf(-gs)));
  const float og = rbf(1.f - g);
  f32x4 a = a0 + a1;
#pragma unroll
  for (int j = 0; j < 4; ++j) {
    float t = a[j]; t += dpp<0x124>(t); t += dpp<0x128>(t); a[j] = xrow_sum(t);
    float u = f[j]; u += dpp<0x124>(u); u += dpp<0x128>(u); f[j] = xrow_sum(u);
  }
  if (l < 4) {
    const int n = ct * 16 + 4 * l;
    const v2u bb = *(const v2u*)(b2 + n);
    const float bias[4] = {bf_lo(bb.x), bf_hi(bb.x), bf_lo(bb.y), bf_hi(bb.y)};
    float o[4];
#pragma unroll
    for (int j = 0; j < 4; ++j) { const float av = rbf(a[j]), bv = rbf(rbf(f[j]) + bias[j]); o[j] = rbf(g * av) + rbf(og * bv); }
    *(v2u*)(out + b * DM + n) = v2u{cvtpk(o[0], o[1]), cvtpk(o[2], o[3])};
  }
}

extern "C" void kernel_launch(void* const* d_in, const int* in_sizes, int n_in, void* d_out, int out_size, void* d_ws, size_t ws_size,
                              hipStream_t stream) {
  const unsigned short* x = (const unsigned short*)d_in[0];
  const unsigned char* kc = (const unsigned char*)d_in[1];
  const unsigned char* vc = (const unsigned char*)d_in[2];
  const unsigned short* wq = (const unsigned short*)d_in[3];
  const unsigned short* wo = (const unsigned short*)d_in[4];
  const unsigned short* w1 = (const unsigned short*)d_in[6];
  const unsigned short* b1 = (const unsigned short*)d_in[7];
  const unsigned short* w2 = (const unsigned short*)d_in[8];
  const unsigned short* b2 = (const unsigned short*)d_in[9];
  const unsigned short* gw = (const unsigned short*)d_in[10];
  unsigned char* ws = (unsigned char*)d_ws;
  k_pre<<<256 + 112, 64, 0, stream>>>(x, wq, w1, b1, ws);
  k_attn<<<256, 512, 0, stream>>>(kc, vc, ws);
  k_post<<<46 * 8, 256, 0, stream>>>(wo, w2, ws);
  k_fin<<<256, 64, 0, stream>>>(x, gw, b2, ws, (unsigned short*)d_out);
}
```

```cpp
#include <hip/hip_runtime.h>
#include <hip/hip_bf16.h>
#include <stdint.h>

typedef unsigned v4u __attribute__((ext_vector_type(4)));
typedef unsigned v2u __attribute__((ext_vector_type(2)));
typedef float f32x4 __attribute__((ext_vector_type(4)));
typedef float f32x2 __attribute__((ext_vector_type(2)));
typedef __bf16 bf16x8 __attribute__((ext_vector_type(8)));
typedef __bf16 bf16x2 __attribute__((ext_vector_type(2)));
typedef short s16x4 __attribute__((ext_vector_type(4)));
typedef unsigned long long u64;
#define GAS __attribute__((address_space(1)))
#define LAS __attribute__((address_space(3)))

constexpr int DM = 512, HKV = 8, SK = 4096, FF = 1792;
constexpr int AUX_SC1 = 16;
constexpr unsigned SENT = 0xFFFFFFFFu;
#define ROT 11
#define PACE 0
#define SBAR 1
#define QD 16
#define HD 0
#define WNT 1
#define OSTR 64
#define TSLEEP 0
#define DT 480
#define T0 150
#define LAG 6
constexpr int NWAVE = 10;

constexpr int WS_E = 0;
constexpr int WS_QP = 4096;
constexpr int WS_HP = WS_QP + 8 * 4 * 4096 * 8;
constexpr int WS_P = WS_HP + 8 * 4 * FF * 8;
constexpr int WS_LSE = WS_P + 32 * 8 * 4 * 256;
constexpr int WS_O = WS_LSE + 1024 * 128;
constexpr int WS_OF = WS_O + 256 * 32 * OSTR;
constexpr int WS_END = WS_OF + 256 * 16 * OSTR;
constexpr int WS_ST = 8 << 20;
constexpr int L_WO = 0, L_WQ = 16384, L_WF = 32768, L_MRG = 49152, L_ML = L_MRG + 16384, L_Q = L_ML + 256, L_H = L_Q + 1024, L_FIN = L_H + 2048,
              L_FLAG = L_FIN + 128, L_END = L_FLAG + 16;
constexpr int L_PG = L_WQ;

__device__ __forceinline__ f32x4 mfma32(v4u a, v4u b, f32x4 c) {
  return __builtin_amdgcn_mfma_f32_16x16x32_bf16(__builtin_bit_cast(bf16x8, a), __builtin_bit_cast(bf16x8, b), c, 0, 0, 0);
}
__device__ __forceinline__ f32x4 mfma16(v2u a, v2u b, f32x4 c) {
  return __builtin_amdgcn_mfma_f32_16x16x16bf16_1k(__builtin_bit_cast(s16x4, a), __builtin_bit_cast(s16x4, b), c, 0, 0, 0);
}
__device__ __forceinline__ unsigned cvtpk(float lo, float hi) { bf16x2 r = {(__bf16)lo, (__bf16)hi}; return __builtin_bit_cast(unsigned, r); }
__device__ __forceinline__ float bf_lo(unsigned u) { return __builtin_bit_cast(float, u << 16); }
__device__ __forceinline__ float bf_hi(unsigned u) { return __builtin_bit_cast(float, u & 0xffff0000u); }
__device__ __forceinline__ float rbf(float x) { return (float)(__bf16)x; }
template <int CTRL> __device__ __forceinline__ float dpp(float x) { return __builtin_bit_cast(float, __builtin_amdgcn_mov_dpp(__builtin_bit_cast(int, x), CTRL, 0xf, 0xf, true)); }
template <int CTRL> __device__ __forceinline__ unsigned dppu(unsigned x) { return (unsigned)__builtin_amdgcn_mov_dpp((int)x, CTRL, 0xf, 0xf, true); }
__device__ __forceinline__ float xrow_max(float x) {
  auto s = __builtin_amdgcn_permlane16_swap(__float_as_uint(x), __float_as_uint(x), false, false);
  x = fmaxf(__uint_as_float(s[0]), __uint_as_float(s[1]));
  auto t = __builtin_amdgcn_permlane32_swap(__float_as_uint(x), __float_as_uint(x), false, false);
  return fmaxf(__uint_as_float(t[0]), __uint_as_float(t[1]));
}
__device__ __forceinline__ float xrow_sum(float x) {
  auto s = __builtin_amdgcn_permlane16_swap(__float_as_uint(x), __float_as_uint(x), false, false);
  x = __uint_as_float(s[0]) + __uint_as_float(s[1]);
  auto t = __builtin_amdgcn_permlane32_swap(__float_as_uint(x), __float_as_uint(x), false, false);
  return __uint_as_float(t[0]) + __uint_as_float(t[1]);
}
__device__ __forceinline__ v2u tr_read(const LAS unsigned char* p) { return __builtin_bit_cast(v2u, __builtin_amdgcn_ds_read_tr16_b64_v4i16((LAS s16x4*)p)); }
__device__ __forceinline__ v4u make_srd(const void* p, unsigned bytes) {
  const u64 a = (u64)p;
  return v4u{(unsigned)a, (unsigned)(a >> 32) & 0xffffu, bytes, 0x00020000u};
}
__device__ __forceinline__ void dma_tile(const void* src, unsigned ldb, unsigned lds_addr, int l) {
  const v4u srd = make_srd(src, 127u * ldb + 128u);
  const unsigned vo = (unsigned)(l >> 3) * ldb + (unsigned)(l & 7) * 16u;
  unsigned keep;
  asm volatile("s_mov_b32 %0, m0" : "=s"(keep));
#pragma unroll
  for (int i = 0; i < 16; ++i) {
    asm volatile("s_mov_b32 m0, %0\n\ts_nop 0\n\tbuffer_load_dwordx4 %1, %2, %3 offen nt lds" :: "s"(lds_addr + i * 1024), "v"(vo), "s"(srd), "s"(i * 8 * ldb) : "memory");
  }
  asm volatile("s_mov_b32 m0, %0" :: "s"(keep));
}
__device__ __forceinline__ v4u tile_frag(const LAS unsigned char* tile, int l, int k, int ct) {
  const LAS unsigned char* p = tile + (8 * (l >> 4) + ((l & 15) >> 2)) * 128 + (l & 3) * 8 + k * 4096 + ct * 32;
  const v2u t0 = tr_read(p), t1 = tr_read(p + 512);
  return v4u{t0.x, t0.y, t1.x, t1.y};
}
#define DRAIN() asm volatile("s_waitcnt vmcnt(0)" ::: "memory")
#define LDSWAIT() asm volatile("s_waitcnt lgkmcnt(0)" ::: "memory")
__device__ __forceinline__ u64 read_tag(const unsigned char* ws) {
  u64 t;
  asm volatile("s_load_dwordx2 %0, %1, 0x0\n\ts_waitcnt lgkmcnt(0)" : "=s"(t) : "s"(ws) : "memory");
  t += 0x9E3779B97F4A7C15ull;
  t = (t ^ (t >> 30)) * 0xBF58476D1CE4E5B9ull; t = (t ^ (t >> 27)) * 0x94D049BB133111EBull;
  return t ^ (t >> 31);
}
__device__ __forceinline__ unsigned now_ticks() {
  u64 t;
  asm volatile("s_memrealtime %0\n\ts_waitcnt lgkmcnt(0)" : "=s"(t) :: "memory");
  return (unsigned)t;
}
#define WAIT_TICK(base, delta) \
  asm volatile("s_branch 2f\n1:\n\ts_sleep 1\n2:\n\ts_memrealtime vcc\n\ts_waitcnt lgkmcnt(0)\n\ts_sub_u32 vcc_lo, vcc_lo, %0\n\ts_cmpk_lt_i32 vcc_lo, %1\n\ts_cbranch_scc1 1b" \
               :: "s"(base), "n"(delta) : "scc", "vcc", "memory")
__device__ __forceinline__ unsigned umax4(v4u v) { return max(max(v.x, v.y), max(v.z, v.w)); }
__device__ __forceinline__ unsigned nosent(unsigned x) { return x < 0xFFFFFFFEu ? x : 0xFFFFFFFEu; }
__device__ __forceinline__ v4u nosent4(f32x4 d) { return v4u{nosent(__float_as_uint(d[0])), nosent(__float_as_uint(d[1])), nosent(__float_as_uint(d[2])), nosent(__float_as_uint(d[3]))}; }
__device__ __forceinline__ f32x4 sel4(bool c, f32x4 a, f32x4 b) { return f32x4{c ? a[0] : b[0], c ? a[1] : b[1], c ? a[2] : b[2], c ? a[3] : b[3]}; }
constexpr int SPIN_CAP = 1 << 18;
#define LD_SC1(off) __builtin_amdgcn_raw_buffer_load_b128(rws, (off), 0, AUX_SC1)
#define ST_SC1(v, off) __builtin_amdgcn_raw_buffer_store_b128((v), rws, (off), 0, AUX_SC1)

#define STAMP(k) do { } while (0)
#define FLUSH() do { } while (0)

__device__ __forceinline__ void wait_lds_flag(const LAS unsigned char* p, u64 tag) {
#pragma nounroll
  for (int spin = 0; spin < SPIN_CAP * 16; ++spin) { if (*(volatile const LAS u64*)p == tag) break; asm volatile("s_sleep 2" ::: "memory"); }
  asm volatile("" ::: "memory");
}

__device__ __forceinline__ void proj_issue(const unsigned short* x, const unsigned short* W, int ld, int kq, int n0, unsigned tile_addr, int l, v4u (&xa)[4]) {
  dma_tile(W + kq * 128 * ld + n0, (unsigned)ld * 2u, tile_addr, l);
#pragma unroll
  for (int k = 0; k < 4; ++k) xa[k] = *(const GAS v4u*)(x + (l & 7) * DM + kq * 128 + 32 * k + 8 * (l >> 4));
}
__device__ __forceinline__ void proj_finish(int ld, int kq, int n0, const LAS unsigned char* tile, __amdgpu_buffer_rsrc_t rws, int dst, u64 tag, int l, const v4u (&xa)[4]) {
  const unsigned tl = (unsigned)tag & 0xffu, th = (unsigned)(tag >> 32);
  const int off = dst + (((l & 7) * 4 + kq) * ld + n0 + 4 * (l >> 4) + ((l >> 3) & 1) * 2) * 8;
#pragma unroll
  for (int ct = 0; ct < 4; ++ct) {
    f32x4 d = {0.f, 0.f, 0.f, 0.f};
#pragma unroll
    for (int k = 0; k < 4; ++k) d = mfma32(tile_frag(tile, l, k, ct), xa[k], d);
    const unsigned v0 = __float_as_uint((l & 8) ? d[2] : d[0]), v1 = __float_as_uint((l & 8) ? d[3] : d[1]);
    ST_SC1((v4u{(v0 & 0xffffff00u) | tl, th, (v1 & 0xffffff00u) | tl, th}), off + ct * 128);
  }
}
__device__ __forceinline__ f32x2 gran_sum(const v4u* t, u64 tag, unsigned& bad) {
  const unsigned tl = (unsigned)tag & 0xffu, th = (unsigned)(tag >> 32);
  float s0[4], s1[4];
#pragma unroll
  for (int k = 0; k < 4; ++k) {
    bad |= (((t[k].x ^ tl) | (t[k].z ^ tl)) & 0xffu) | (t[k].y ^ th) | (t[k].w ^ th);
    s0[k] = __uint_as_float(t[k].x & 0xffffff00u); s1[k] = __uint_as_float(t[k].z & 0xffffff00u);
  }
  return f32x2{(s0[0] + s0[1]) + (s0[2] + s0[3]), (s1[0] + s1[1]) + (s1[2] + s1[3])};
}

struct Args {
  const unsigned short *x, *wq, *wo, *w1, *b1, *w2, *b2, *gw;
  const unsigned char *kc, *vc;
  unsigned char* ws;
  unsigned short* out;
};

__global__ void __launch_bounds__(NWAVE * 64) fused_decode(Args a) {
  __shared__ __attribute__((aligned(16))) unsigned char smem[L_END];
  LAS unsigned char* L = (LAS unsigned char*)smem;
  const unsigned lbase = (unsigned)(uintptr_t)L;
  const int tid = threadIdx.x, l = tid & 63, w = __builtin_amdgcn_readfirstlane(tid >> 6);
  const int c = blockIdx.x, kvh = c & 7, i32 = c >> 3, b = i32 >> 2, sp = i32 & 3;
  unsigned char* ws = a.ws;
  STAMP(0);
  const __amdgpu_buffer_rsrc_t rws = __builtin_amdgcn_make_buffer_rsrc((void*)ws, 0, WS_END, 0x00020000);
  const int g = l >> 4;
  const int oh = kvh * 4 + (i32 >> 3), on = i32 & 7;
  const bool ffn1 = c < 112, ffn2 = c >= 112 && c < 224;
  const int fc = (c - 112) >> 3;

  if (w == 0) {
    const v4u sv = {SENT, SENT, SENT, SENT};
    ST_SC1(sv, WS_P + (((kvh * 4 + g) * 8 + b) * 4 + sp) * 256 + (l & 15) * 16);
    if (l < 32) ST_SC1(sv, WS_LSE + (((kvh * 4 + (l >> 3)) * 8 + b) * 4 + sp) * 128 + (l & 7) * 16);
#pragma unroll
    for (int j = 0; j < 2; ++j) {
      const int p = j * 16 + (l >> 2);
      ST_SC1(sv, WS_O + (((p >> 2) * 32 + on * 4 + (p & 3)) * 32 + oh) * OSTR + (l & 3) * 16);
      if (ffn2) ST_SC1(sv, WS_OF + (((p >> 2) * 32 + kvh * 4 + (p & 3)) * 16 + fc) * OSTR + (l & 3) * 16);
    }
    v4u xa[4];
    proj_issue(a.x, a.wq, 4096, i32 >> 3, kvh * 512 + (i32 & 7) * 64, lbase + L_WQ, l, xa);
    if (SBAR) { __builtin_amdgcn_sched_barrier(0); __builtin_amdgcn_s_barrier(); __builtin_amdgcn_sched_barrier(0); }
    const u64 tag = read_tag(ws);
    DRAIN();
    STAMP(5);
    proj_finish(4096, i32 >> 3, kvh * 512 + (i32 & 7) * 64, L + L_WQ, rws, WS_QP, tag, l, xa);
    STAMP(1);
    if (QD) asm volatile("s_sleep %0" :: "n"(QD) : "memory");
#pragma nounroll
    for (int spin = 0; spin < SPIN_CAP; ++spin) {
      v4u t[16];
#pragma unroll
      for (int i = 0; i < 16; ++i) t[i] = LD_SC1(WS_QP + ((b * 4 + (i & 3)) * 4096 + kvh * 512 + (i >> 2) * 128 + l * 2) * 8);
      unsigned bad = 0;
#pragma unroll
      for (int j = 0; j < 4; ++j) { const f32x2 q = gran_sum(t + 4 * j, tag, bad); *(LAS unsigned*)(L + L_Q + j * 256 + l * 4) = cvtpk(q.x, q.y); }
      if (__builtin_amdgcn_ballot_w64(bad != 0) == 0) break;
      asm volatile("s_sleep 4" ::: "memory");
    }
    LDSWAIT();
    if (l == 0) *(volatile LAS u64*)(L + L_FLAG) = tag;
    STAMP(3);
    {
      const int rn = (c & 31) >> 2;
      const int pc = l < 32 ? ((l & 3) * 8 + rn) * 8 + (l >> 2) : l < 46 ? 112 + (l - 32) * 8 + rn : c;
      const int coff = WS_QP + ((pc >> 6) * 4096 + (pc & 7) * 512 + ((pc >> 3) & 7) * 64) * 8;
#pragma nounroll
      for (int spin = 0; spin < SPIN_CAP; ++spin) {
        const v2u f = __builtin_amdgcn_raw_buffer_load_b64(rws, coff, 0, AUX_SC1);
        if (__builtin_amdgcn_ballot_w64((((f.x ^ (unsigned)tag) & 0xffu) | (f.y ^ (unsigned)(tag >> 32))) != 0) == 0) break;
        asm volatile("s_sleep 16" ::: "memory");
      }
      asm volatile("" ::: "memory");
    }
    const int rb = c >> 5, ct = c & 31;
    const v4u xv = *(const GAS v4u*)(a.x + rb * DM + l * 8), gv = *(const GAS v4u*)(a.gw + l * 8);
    const v2u bb = *(const GAS v2u*)(a.b2 + ct * 16 + 4 * (l & 3));
    float gs = 0.f;
#pragma unroll
    for (int j = 0; j < 4; ++j) { gs += rbf(bf_lo(xv[j]) * bf_lo(gv[j])); gs += rbf(bf_hi(xv[j]) * bf_hi(gv[j])); }
    gs += dpp<0xB1>(gs); gs += dpp<0x4E>(gs); gs += dpp<0x124>(gs); gs += dpp<0x128>(gs); gs = xrow_sum(gs);
    gs = rbf(gs);
    const float gt = rbf(1.f / (1.f + __expf(-gs))), og = rbf(1.f - gt);
    v4u r2;
#pragma nounroll
    for (int spin = 0; spin < SPIN_CAP; ++spin) {
      r2 = LD_SC1(l < 56 ? WS_OF + (c * 16 + (l >> 2)) * OSTR + (l & 3) * 16 : WS_END);
      if (__builtin_amdgcn_ballot_w64(umax4(r2) == SENT) == 0) break;
      asm volatile("s_sleep 16" ::: "memory");
    }
    f32x4 fv = __builtin_bit_cast(f32x4, r2);
    const float bias[4] = {bf_lo(bb.x), bf_hi(bb.x), bf_lo(bb.y), bf_hi(bb.y)};
#pragma unroll
    for (int j = 0; j < 4; ++j) { float u = fv[j]; u += dpp<0x124>(u); u += dpp<0x128>(u); fv[j] = rbf(og * rbf(rbf(xrow_sum(u)) + bias[j])); }
    if (l < 4) *(LAS f32x4*)(L + L_FIN + l * 16) = fv;
    if (l == 0) *(LAS float*)(L + L_FIN + 64) = gt;
    LDSWAIT();
    STAMP(4);
    FLUSH();
    return;
  }
  if (w == 1) {
    if (ffn1) {
      v4u xa[4];
      proj_issue(a.x, a.w1, FF, c & 3, (c >> 2) * 64, lbase + L_WF, l, xa);
      if (SBAR) { __builtin_amdgcn_sched_barrier(0); __builtin_amdgcn_s_barrier(); __builtin_amdgcn_sched_barrier(0); }
      const u64 tag = read_tag(ws);
      DRAIN();
      proj_finish(FF, c & 3, (c >> 2) * 64, L + L_WF, rws, WS_HP, tag, l, xa);
      STAMP(1);
    } else if (ffn2) {
      dma_tile(a.w2 + fc * 128 * DM + kvh * 64, DM * 2, lbase + L_WF, l);
      const unsigned bb = *(const GAS unsigned*)(a.b1 + fc * 128 + l * 2);
      if (SBAR) { __builtin_amdgcn_sched_barrier(0); __builtin_amdgcn_s_barrier(); __builtin_amdgcn_sched_barrier(0); }
      const u64 tag = read_tag(ws);
      wait_lds_flag(L + L_FLAG, tag);
      if (HD) asm volatile("s_sleep %0" :: "n"(HD) : "memory");
#pragma nounroll
      for (int hb2 = 0; hb2 < 2; ++hb2) {
#pragma nounroll
        for (int spin = 0; spin < SPIN_CAP; ++spin) {
          v4u t[16];
#pragma unroll
          for (int i = 0; i < 16; ++i) t[i] = LD_SC1(WS_HP + (((hb2 * 4 + (i >> 2)) * 4 + (i & 3)) * FF + fc * 128 + l * 2) * 8);
          unsigned bad = 0;
#pragma unroll
          for (int j = 0; j < 4; ++j) {
            const f32x2 z = gran_sum(t + 4 * j, tag, bad);
            const float h0 = rbf(rbf(z.x) + bf_lo(bb)), h1 = rbf(rbf(z.y) + bf_hi(bb));
            const float r0 = h0 > 0.f ? h0 : 0.f, r1 = h1 > 0.f ? h1 : 0.f;
            *(LAS unsigned*)(L + L_H + (hb2 * 4 + j) * 256 + l * 4) = cvtpk(r0 * r0, r1 * r1);
          }
          if (__builtin_amdgcn_ballot_w64(bad != 0) == 0) break;
          asm volatile("s_sleep 8" ::: "memory");
        }
      }
      STAMP(1);
      DRAIN();
      LDSWAIT();
      v4u hb[4];
#pragma unroll
      for (int k = 0; k < 4; ++k) hb[k] = *(const LAS v4u*)(L + L_H + (l & 7) * 256 + k * 64 + g * 16);
      f32x4 d[4];
#pragma unroll
      for (int ct = 0; ct < 4; ++ct) {
        d[ct] = f32x4{0.f, 0.f, 0.f, 0.f};
#pragma unroll
        for (int k = 0; k < 4; ++k) d[ct] = mfma32(tile_frag(L + L_WF, l, k, ct), hb[k], d[ct]);
      }
      const int off = WS_OF + (((l & 7) * 32 + kvh * 4 + ((l >> 3) & 1)) * 16 + fc) * OSTR + g * 16;
      ST_SC1(nosent4(sel4(l & 8, d[1], d[0])), off);
      ST_SC1(nosent4(sel4(l & 8, d[3], d[2])), off + 32 * OSTR);
    } else if (SBAR) __builtin_amdgcn_s_barrier();
    dma_tile(a.wo + oh * 128 * DM + on * 64, DM * 2, lbase + L_WO, l);
    DRAIN();
    STAMP(2);
    FLUSH();
    return;
  }

  const int sw = w - 2;
  const unsigned tb = now_ticks() + T0 + ((sw * (DT / 8)) >> 4);
#define PACE_TO(j) WAIT_TICK(tb, ((j) * DT) >> 4)
  if (SBAR) __builtin_amdgcn_s_barrier();
  const size_t base = ((size_t)(b * HKV + kvh) * SK + sp * 1024 + sw * 128) * 256;
  const v4u rk = make_srd(a.kc + base, 32768), rv = make_srd(a.vc + base, 32768);
  const int ko = ((l >> 2) & 3) * 256 + g * 64 + (l & 3) * 16, vo = l * 16;
  const int rot = ROT == 0 ? 0 : ROT == 5 ? 4 * (i32 & 7) : ROT == 7 ? i32 : ROT == 10 ? (8 * sw + i32) & 31 : (4 * sw + i32) & 31;
#define OFF(i) ((((i) + rot) & 31) * 1024)
#define LOAD_V(i) asm volatile("buffer_load_dwordx4 %0, %1, %2, %3 offen nt" : "+v"(kv[i]) : "v"(vo), "s"(rv), "s"(OFF(i)) : "memory")
  v4u kv[32];
  __builtin_amdgcn_sched_barrier(0);
#pragma unroll
  for (int i = 0; i < 32; ++i) {
    PACE_TO(i);
    asm volatile("buffer_load_dwordx4 %0, %1, %2, %3 offen nt" : "=v"(kv[i]) : "v"(ko), "s"(rk), "s"(OFF(i)) : "memory");
    __builtin_amdgcn_sched_barrier(0);
  }
  STAMP(1);
  const u64 tag = read_tag(ws);
  wait_lds_flag(L + L_FLAG, tag);
  STAMP(2);
  const v4u qf = *(const LAS v4u*)(L + L_Q + ((l >> 2) & 3) * 256 + g * 64 + (l & 3) * 16);
  __builtin_amdgcn_sched_barrier(0);
  constexpr float C2 = 0.088388347648318440f * 1.4426950408889634f;
  float sc[8];
  const int s = l & 3;
#pragma unroll
  for (int m = 0; m < 8; ++m) {
    asm volatile("s_waitcnt vmcnt(%4)" : "+v"(kv[4 * m]), "+v"(kv[4 * m + 1]), "+v"(kv[4 * m + 2]), "+v"(kv[4 * m + 3]) : "n"(28 - 4 * m + m) : "memory");
    f32x4 d[4];
#pragma unroll
    for (int r = 0; r < 4; ++r) d[r] = mfma32(kv[4 * m + r], qf, f32x4{0.f, 0.f, 0.f, 0.f});
    float t[4];
#pragma unroll
    for (int r = 0; r < 4; ++r) { t[r] = dpp<0x00>(d[r][0]); t[r] = dpp<0x55>(d[r][1]) + t[r]; t[r] = dpp<0xAA>(d[r][2]) + t[r]; t[r] = dpp<0xFF>(d[r][3]) + t[r]; }
    sc[m] = (s & 2) ? ((s & 1) ? t[3] : t[2]) : ((s & 1) ? t[1] : t[0]);
    asm volatile("" : "+v"(sc[m]));
    __builtin_amdgcn_sched_barrier(0);
    PACE_TO(32 + m);
    LOAD_V(m);
    __builtin_amdgcn_sched_barrier(0);
  }
  STAMP(3);
  float mx = sc[0];
#pragma unroll
  for (int m = 1; m < 8; ++m) mx = fmaxf(mx, sc[m]);
  mx = fmaxf(mx, dpp<0xB1>(mx)); mx = fmaxf(mx, dpp<0x4E>(mx)); mx = xrow_max(mx);
  const float mneg = -mx * C2;
  float lsum = 0.f;
  unsigned pk[8];
#pragma unroll
  for (int m = 0; m < 8; ++m) { const float p = __builtin_amdgcn_exp2f(fmaf(sc[m], C2, mneg)); pk[m] = cvtpk(p, p); lsum += bf_lo(pk[m]); }
  lsum += dpp<0xB1>(lsum); lsum += dpp<0x4E>(lsum); lsum = xrow_sum(lsum);
  if (l < 16 && (l & 3) == 0) *(LAS f32x2*)(L + L_ML + (l >> 2) * 64 + sw * 8) = f32x2{mx * C2, lsum};
  const unsigned half = (l & 1) ? 0xffff0000u : 0x0000ffffu;
  const unsigned m0 = (l & 2) ? 0u : half, m1 = (l & 2) ? half : 0u;
  f32x4 o0 = {0.f, 0.f, 0.f, 0.f}, o1 = {0.f, 0.f, 0.f, 0.f};
#pragma unroll
  for (int j = 8; j < 40; ++j) {
    if (j < 32) { PACE_TO(32 + j); LOAD_V(j); __builtin_amdgcn_sched_barrier(0); }
    if (j == 31) STAMP(11);
    const int i = j - 8;
    const unsigned pb = (i & 3) == 0 ? dppu<0x00>(pk[i >> 2]) : (i & 3) == 1 ? dppu<0x55>(pk[i >> 2]) : (i & 3) == 2 ? dppu<0xAA>(pk[i >> 2]) : dppu<0xFF>(pk[i >> 2]);
    const v2u av = {pb & m0, pb & m1};
    asm volatile("s_waitcnt vmcnt(%1)" : "+v"(kv[i]) : "n"((j < 32 ? j : 31) - i) : "memory");
    o0 = mfma16(av, v2u{kv[i].x, kv[i].y}, o0);
    o1 = mfma16(av, v2u{kv[i].z, kv[i].w}, o1);
    __builtin_amdgcn_sched_barrier(0);
  }
  *(LAS f32x4*)(L + L_MRG + (sw * 4 + g) * 512 + (l & 15) * 32) = o0;
  *(LAS f32x4*)(L + L_MRG + (sw * 4 + g) * 512 + (l & 15) * 32 + 16) = o1;
  STAMP(4);
  LDSWAIT();
  __builtin_amdgcn_s_barrier();
  asm volatile("" ::: "memory");
  if (sw < 4) {
    STAMP(5);
    f32x4 ml[4];
#pragma unroll
    for (int j = 0; j < 4; ++j) ml[j] = *(const LAS f32x4*)(L + L_ML + sw * 64 + j * 16);
    f32x2 ov[8];
#pragma unroll
    for (int j = 0; j < 8; ++j) ov[j] = *(const LAS f32x2*)(L + L_MRG + (j * 4 + sw) * 512 + l * 8);
    const float M = fmaxf(fmaxf(fmaxf(ml[0][0], ml[0][2]), fmaxf(ml[1][0], ml[1][2])), fmaxf(fmaxf(ml[2][0], ml[2][2]), fmaxf(ml[3][0], ml[3][2])));
    float Ls = 0.f, a0 = 0.f, a1 = 0.f;
#pragma unroll
    for (int j = 0; j < 8; ++j) {
      const float e = __builtin_amdgcn_exp2f(ml[j >> 1][(j & 1) * 2] - M);
      Ls = fmaf(e, ml[j >> 1][(j & 1) * 2 + 1], Ls);
      a0 = fmaf(e, ov[j].x, a0); a1 = fmaf(e, ov[j].y, a1);
    }
    const float inv = __builtin_amdgcn_rcpf(Ls);
    const unsigned pv = nosent(cvtpk(a0 * inv, a1 * inv));
    const v4u pq = {dppu<0x00>(pv), dppu<0x55>(pv), dppu<0xAA>(pv), dppu<0xFF>(pv)};
    const int prec = ((kvh * 4 + sw) * 8 + b) * 4 + sp;
    const unsigned lse = nosent(__float_as_uint(M + __builtin_amdgcn_logf(Ls)));
    const bool isl = (l & 3) == 1;
    if ((l & 3) == 0 || (isl && l < 32))
      ST_SC1((v4u{isl ? lse : pq.x, isl ? lse : pq.y, isl ? lse : pq.z, isl ? lse : pq.w}), isl ? WS_LSE + prec * 128 + (l >> 2) * 16 : WS_P + prec * 256 + l * 4);
    STAMP(6);
    v4u wf[4];
#pragma unroll
    for (int k = 0; k < 4; ++k) wf[k] = tile_frag(L + L_WO, l, k, sw);
    const int pb = l & 7, sl = (l >> 3) & 1;
    v4u p0, p1;
    unsigned lv;
    {
      const int poff = WS_P + (oh * 8 + 2 * sw) * 1024 + l * 16, loff = WS_LSE + ((oh * 8 + 2 * sw) * 4 + (l & 7)) * 128;
      bool need = true;
#pragma nounroll
      for (int spin = 0; spin < SPIN_CAP && need; ++spin) {
        p0 = LD_SC1(poff); p1 = LD_SC1(poff + 1024); lv = __builtin_amdgcn_raw_buffer_load_b32(rws, loff, 0, AUX_SC1);
        need = max(max(umax4(p0), umax4(p1)), lv) == SENT;
        if (TSLEEP) asm volatile("s_sleep %0" :: "n"(TSLEEP) : "memory"); else asm volatile("" ::: "memory");
      }
    }
    *(LAS v4u*)(L + L_PG + (8 * sw + g) * 272 + (l & 15) * 16) = p0;
    *(LAS v4u*)(L + L_PG + (8 * sw + 4 + g) * 272 + (l & 15) * 16) = p1;
    {
      const float ls = __uint_as_float(lv);
      float M2 = fmaxf(ls, dpp<0xB1>(ls)); M2 = fmaxf(M2, dpp<0x4E>(M2));
      const float e = __builtin_amdgcn_exp2f(ls - M2);
      float es = e + dpp<0xB1>(e); es = es + dpp<0x4E>(es);
      if (l < 8) *(LAS float*)(L + L_PG + (8 * sw + l) * 272 + 256) = e * __builtin_amdgcn_rcpf(es);
    }
    STAMP(7);
    LDSWAIT();
    __builtin_amdgcn_s_barrier();
    asm volatile("" ::: "memory");
    const float wa = *(const LAS float*)(L + L_PG + (pb * 4 + sl) * 272 + 256), wb = *(const LAS float*)(L + L_PG + (pb * 4 + 2 + sl) * 272 + 256);
    f32x4 da = {0.f, 0.f, 0.f, 0.f}, db = {0.f, 0.f, 0.f, 0.f};
#pragma unroll
    for (int k = 0; k < 4; ++k) {
      const v4u pa = *(const LAS v4u*)(L + L_PG + (pb * 4 + sl) * 272 + k * 64 + g * 16), pc = *(const LAS v4u*)(L + L_PG + (pb * 4 + 2 + sl) * 272 + k * 64 + g * 16);
      da = mfma32(wf[k], pa, da); db = mfma32(wf[k], pc, db);
    }
    f32x4 r;
#pragma unroll
    for (int j = 0; j < 4; ++j) { const float t = da[j] * wa + db[j] * wb; r[j] = t + dpp<0x128>(t); }
    if (sl == 0) ST_SC1(nosent4(r), WS_O + ((pb * 32 + on * 4 + sw) * 32 + oh) * OSTR + g * 16);
    STAMP(8);
    if (sw == 0) {
      const f32x4 fv = *(const LAS f32x4*)(L + L_FIN + (l & 3) * 16);
      const float gt = *(const LAS float*)(L + L_FIN + 64);
      v4u r0, r1;
      bool need = true;
#pragma nounroll
      for (int spin = 0; spin < SPIN_CAP && need; ++spin) {
        r0 = LD_SC1(WS_O + (c * 32 + (l >> 2)) * OSTR + (l & 3) * 16); r1 = LD_SC1(WS_O + (c * 32 + 16 + (l >> 2)) * OSTR + (l & 3) * 16);
        need = max(umax4(r0), umax4(r1)) == SENT;
        if (TSLEEP) asm volatile("s_sleep %0" :: "n"(TSLEEP) : "memory"); else asm volatile("" ::: "memory");
      }
      STAMP(9);
      f32x4 av = __builtin_bit_cast(f32x4, r0) + __builtin_bit_cast(f32x4, r1);
#pragma unroll
      for (int j = 0; j < 4; ++j) { float t = av[j]; t += dpp<0x124>(t); t += dpp<0x128>(t); av[j] = rbf(gt * rbf(xrow_sum(t))) + fv[j]; }
      if (l < 4) *(GAS v2u*)(a.out + (c >> 5) * DM + (c & 31) * 16 + 4 * l) = v2u{cvtpk(av[0], av[1]), cvtpk(av[2], av[3])};
      if (c == 0 && l == 0) *(GAS u64*)(ws + WS_E) = tag;
      STAMP(10);
    }
  }
  FLUSH();
}

extern "C" void kernel_launch(void* const* d_in, const int* in_sizes, int n_in, void* d_out, int out_size, void* d_ws, size_t ws_size,
                              hipStream_t stream) {
  Args a;
  a.x = (const unsigned short*)d_in[0];
  a.kc = (const unsigned char*)d_in[1];
  a.vc = (const unsigned char*)d_in[2];
  a.wq = (const unsigned short*)d_in[3];
  a.wo = (const unsigned short*)d_in[4];
  a.w1 = (const unsigned short*)d_in[6];
  a.b1 = (const unsigned short*)d_in[7];
  a.w2 = (const unsigned short*)d_in[8];
  a.b2 = (const unsigned short*)d_in[9];
  a.gw = (const unsigned short*)d_in[10];
  a.ws = (unsigned char*)d_ws;
  a.out = (unsigned short*)d_out;
  fused_decode<<<256, NWAVE * 64, 0, stream>>>(a);
}
```

```cpp
#define PDELAY1 12
#define PDELAY2 12
#define DT 485
#define T0 150
#define WIN 5
#define OFULL 1
#define WSOFF 0
#include <hip/hip_runtime.h>
#include <hip/hip_bf16.h>
#include <stdint.h>

typedef unsigned v4u __attribute__((ext_vector_type(4)));
typedef unsigned v2u __attribute__((ext_vector_type(2)));
typedef float f32x4 __attribute__((ext_vector_type(4)));
typedef float f32x2 __attribute__((ext_vector_type(2)));
typedef __bf16 bf16x8 __attribute__((ext_vector_type(8)));
typedef __bf16 bf16x2 __attribute__((ext_vector_type(2)));
typedef short s16x4 __attribute__((ext_vector_type(4)));
typedef unsigned long long u64;
#define GAS __attribute__((address_space(1)))
#define LAS __attribute__((address_space(3)))

constexpr int DM = 512, HKV = 8, SK = 4096, FF = 1792;
constexpr int AUX_SC1 = 16;
constexpr unsigned SENT = 0xFFFFFFFFu;
#define TQ 560
#define TH 560
#define TF 1200
#define GQ 25
#define GH 25
#define SHIFT 1
constexpr int J0 = ((TQ - T0) * 16) / DT;
constexpr int slot_tick(int j) { return (j * DT + (SHIFT ? (j <= J0 ? 0 : j - J0 < 8 ? j - J0 : 8) * (DT / 8) : 0)) >> 4; }
#define PD 1
#define PGAP 4
#define RS 0
constexpr int NWAVE = 10;

constexpr int WS_E = 0;
constexpr int WS_F = 2048;
constexpr int WS_QP = 4096;
constexpr int WS_HP = WS_QP + 8 * 8 * 2 * 512 * 8;
constexpr int WS_P = WS_HP + 14 * 8 * 2 * 128 * 8;
constexpr int WS_LSE = WS_P + 32 * 8 * 4 * 256;
constexpr int WS_O = WS_LSE + 256 * 128;
constexpr int WS_OF = WS_O + 256 * 2048;
constexpr int WS_END = WS_OF + 256 * 1024;
constexpr int WS_ST = 8 << 20;
constexpr int L_WO = 0, L_WT = 16384, L_MRG = 49152, L_ML = L_MRG + 16384, L_CF = L_ML + 512, L_Q = L_CF + 128, L_H = L_Q + 1024, L_FIN = L_H + 2048,
              L_FLAG = L_FIN + 128, L_END = L_FLAG + 16;
constexpr int L_PG = L_WT;

__device__ __forceinline__ f32x4 mfma32(v4u a, v4u b, f32x4 c) {
  return __builtin_amdgcn_mfma_f32_16x16x32_bf16(__builtin_bit_cast(bf16x8, a), __builtin_bit_cast(bf16x8, b), c, 0, 0, 0);
}
__device__ __forceinline__ f32x4 mfma16(v2u a, v2u b, f32x4 c) {
  return __builtin_amdgcn_mfma_f32_16x16x16bf16_1k(__builtin_bit_cast(s16x4, a), __builtin_bit_cast(s16x4, b), c, 0, 0, 0);
}
__device__ __forceinline__ unsigned cvtpk(float lo, float hi) { bf16x2 r = {(__bf16)lo, (__bf16)hi}; return __builtin_bit_cast(unsigned, r); }
__device__ __forceinline__ float bf_lo(unsigned u) { return __builtin_bit_cast(float, u << 16); }
__device__ __forceinline__ float bf_hi(unsigned u) { return __builtin_bit_cast(float, u & 0xffff0000u); }
__device__ __forceinline__ float rbf(float x) { return (float)(__bf16)x; }
template <int CTRL> __device__ __forceinline__ float dpp(float x) { return __builtin_bit_cast(float, __builtin_amdgcn_mov_dpp(__builtin_bit_cast(int, x), CTRL, 0xf, 0xf, true)); }
template <int CTRL> __device__ __forceinline__ unsigned dppu(unsigned x) { return (unsigned)__builtin_amdgcn_mov_dpp((int)x, CTRL, 0xf, 0xf, true); }
__device__ __forceinline__ float xrow_max(float x) {
  auto s = __builtin_amdgcn_permlane16_swap(__float_as_uint(x), __float_as_uint(x), false, false);
  x = fmaxf(__uint_as_float(s[0]), __uint_as_float(s[1]));
  auto t = __builtin_amdgcn_permlane32_swap(__float_as_uint(x), __float_as_uint(x), false, false);
  return fmaxf(__uint_as_float(t[0]), __uint_as_float(t[1]));
}
__device__ __forceinline__ float xrow_sum(float x) {
  auto s = __builtin_amdgcn_permlane16_swap(__float_as_uint(x), __float_as_uint(x), false, false);
  x = __uint_as_float(s[0]) + __uint_as_float(s[1]);
  auto t = __builtin_amdgcn_permlane32_swap(__float_as_uint(x), __float_as_uint(x), false, false);
  return __uint_as_float(t[0]) + __uint_as_float(t[1]);
}
__device__ __forceinline__ v2u tr_read(const LAS unsigned char* p) { return __builtin_bit_cast(v2u, __builtin_amdgcn_ds_read_tr16_b64_v4i16((LAS s16x4*)p)); }
__device__ __forceinline__ v4u make_srd(const void* p, unsigned bytes) {
  const u64 a = (u64)p;
  return v4u{(unsigned)a, (unsigned)(a >> 32) & 0xffffu, bytes, 0x00020000u};
}
__device__ __forceinline__ void dma_rows(v4u srd, unsigned voff, unsigned soff, unsigned lds_addr) {
  unsigned keep;
  asm volatile("s_mov_b32 %0, m0\n\ts_mov_b32 m0, %1\n\ts_nop 0\n\tbuffer_load_dwordx4 %2, %3, %4 offen nt lds\n\ts_mov_b32 m0, %0"
               : "=&s"(keep) : "s"(lds_addr), "v"(voff), "s"(srd), "s"(soff) : "memory");
}
template <int ROWS> __device__ __forceinline__ void dma_tile(const void* src, unsigned ldb, unsigned lds_addr, int l) {
  const v4u srd = make_srd(src, (ROWS - 1) * ldb + 128u);
  const unsigned vo = (unsigned)(l >> 3) * ldb + (unsigned)(l & 7) * 16u;
#pragma unroll 4
  for (int i = 0; i < ROWS / 8; ++i) dma_rows(srd, vo, i * 8 * ldb, lds_addr + i * 1024);
}
__device__ __forceinline__ v4u tile_frag(const LAS unsigned char* tile, int l, int k, int ct) {
  const LAS unsigned char* p = tile + (8 * (l >> 4) + ((l & 15) >> 2)) * 128 + (l & 3) * 8 + k * 4096 + ct * 32;
  const v2u t0 = tr_read(p), t1 = tr_read(p + 512);
  return v4u{t0.x, t0.y, t1.x, t1.y};
}
#define DRAIN() asm volatile("s_waitcnt vmcnt(0)" ::: "memory")
#define LDSWAIT() asm volatile("s_waitcnt lgkmcnt(0)" ::: "memory")
#define WGBAR() do { __builtin_amdgcn_sched_barrier(0); __builtin_amdgcn_s_barrier(); __builtin_amdgcn_sched_barrier(0); asm volatile("" ::: "memory"); } while (0)
__device__ __forceinline__ u64 read_tag(const unsigned char* ws) {
  u64 t;
  asm volatile("s_load_dwordx2 %0, %1, 0x0\n\ts_waitcnt lgkmcnt(0)" : "=s"(t) : "s"(ws) : "memory");
  t += 0x9E3779B97F4A7C15ull;
  t = (t ^ (t >> 30)) * 0xBF58476D1CE4E5B9ull; t = (t ^ (t >> 27)) * 0x94D049BB133111EBull;
  return t ^ (t >> 31);
}
__device__ __forceinline__ unsigned now_ticks() {
  u64 t;
  asm volatile("s_memrealtime %0\n\ts_waitcnt lgkmcnt(0)" : "=s"(t) :: "memory");
  return (unsigned)t;
}
#define WAIT_TICK(base, delta) \
  asm volatile("s_branch 2f\n1:\n\ts_sleep 1\n2:\n\ts_memrealtime vcc\n\ts_waitcnt lgkmcnt(0)\n\ts_sub_u32 vcc_lo, vcc_lo, %0\n\ts_cmpk_lt_i32 vcc_lo, %1\n\ts_cbranch_scc1 1b" \
               :: "s"(base), "n"(delta) : "scc", "vcc", "memory")
__device__ __forceinline__ unsigned umax4(v4u v) { return max(max(v.x, v.y), max(v.z, v.w)); }
__device__ __forceinline__ unsigned nosent(unsigned x) { return x < 0xFFFFFFFEu ? x : 0xFFFFFFFEu; }
__device__ __forceinline__ v4u nosent4(f32x4 d) { return v4u{nosent(__float_as_uint(d[0])), nosent(__float_as_uint(d[1])), nosent(__float_as_uint(d[2])), nosent(__float_as_uint(d[3]))}; }
__device__ __forceinline__ f32x4 sel4(bool c, f32x4 a, f32x4 b) { return f32x4{c ? a[0] : b[0], c ? a[1] : b[1], c ? a[2] : b[2], c ? a[3] : b[3]}; }
constexpr int SPIN_CAP = 1 << 18;
#define LD_SC1(off) __builtin_amdgcn_raw_buffer_load_b128(rws, (off), 0, AUX_SC1)
#define ST_SC1(v, off) __builtin_amdgcn_raw_buffer_store_b128((v), rws, (off), 0, AUX_SC1)

#define STAMP(k) do { } while (0)
#define FLUSH() do { } while (0)

__device__ __forceinline__ void wait_lds_flag(const LAS unsigned char* p, u64 tag) {
#pragma nounroll
  for (int spin = 0; spin < SPIN_CAP * 16; ++spin) { if (*(volatile const LAS u64*)p == tag) break; asm volatile("s_sleep 2" ::: "memory"); }
  asm volatile("" ::: "memory");
}
__device__ __forceinline__ f32x2 gran_sum(v4u t0, v4u t1, u64 tag, unsigned& bad) {
  const unsigned tl = (unsigned)tag & 0xffu, th = (unsigned)(tag >> 32);
  bad |= (((t0.x ^ tl) | (t0.z ^ tl) | (t1.x ^ tl) | (t1.z ^ tl)) & 0xffu) | (t0.y ^ th) | (t0.w ^ th) | (t1.y ^ th) | (t1.w ^ th);
  return f32x2{__uint_as_float(t0.x & 0xffffff00u) + __uint_as_float(t1.x & 0xffffff00u), __uint_as_float(t0.z & 0xffffff00u) + __uint_as_float(t1.z & 0xffffff00u)};
}

__device__ __forceinline__ void poll2(__amdgpu_buffer_rsrc_t rws, int o0, int o1, v4u& r0, v4u& r1) {
  bool need = true;
#pragma nounroll
  for (int spin = 0; spin < SPIN_CAP && need; ++spin) {
    r0 = LD_SC1(o0); r1 = LD_SC1(o1);
    need = max(umax4(r0), umax4(r1)) == SENT;
    if (RS && __builtin_amdgcn_ballot_w64(need) != 0) asm volatile("s_sleep %0" :: "n"(RS) : "memory");
    asm volatile("" ::: "memory");
  }
}

struct Args {
  const unsigned short *x, *wq, *wo, *w1, *b1, *w2, *b2, *gw;
  const unsigned char *kc, *vc;
  unsigned char* ws;
  unsigned short* out;
};

__global__ void __launch_bounds__(NWAVE * 64) fused_decode(Args a) {
  __shared__ __attribute__((aligned(16))) unsigned char smem[L_END];
  LAS unsigned char* L = (LAS unsigned char*)smem;
  const unsigned lbase = (unsigned)(uintptr_t)L;
  const int tid = threadIdx.x, l = tid & 63, w = __builtin_amdgcn_readfirstlane(tid >> 6);
  const int c = blockIdx.x, xr = c >> 3, sp = xr & 3, kvh = xr >> 2, b = (c - kvh) & 7, i32 = b * 4 + sp;
  unsigned char* ws = a.ws;
  STAMP(0);
  const unsigned t_in = now_ticks();
  const __amdgpu_buffer_rsrc_t rws = __builtin_amdgcn_make_buffer_rsrc((void*)ws, 0, WS_END, 0x00020000);
  const int g = l >> 4;
  const int oh = kvh * 4 + (i32 >> 3), on = i32 & 7;
  const bool ffn2 = xr >= 23 && xr < 30;
  const int f2 = (c & 7) * 7 + xr - 23, fc = f2 >> 2, fn = f2 & 3;

  if (w == 0) {
    if (xr < 23) {
      const bool isq = xr < 16;
      const int t = isq ? ((xr >> 1) * 8 + ((c + xr) & 7)) * 2 + (xr & 1) : (c & 7) * 7 + xr - 16;
      const int cb = t >> 1, kh = t & 1;
      const unsigned ldb = isq ? 8192u : (unsigned)FF * 2u;
      dma_tile<256>((const unsigned char*)(isq ? a.wq : a.w1) + (size_t)kh * 256 * ldb + cb * 128, ldb, lbase + L_WT, l);
      v4u xa[8];
#pragma unroll
      for (int k = 0; k < 8; ++k) xa[k] = *(const GAS v4u*)(a.x + (l & 7) * DM + kh * 256 + 32 * k + 8 * g);
      WGBAR();
      const u64 tag = read_tag(ws);
      DRAIN();
      STAMP(1);
      const unsigned tl = (unsigned)tag & 0xffu, th = (unsigned)(tag >> 32);
      const int off = (isq ? WS_QP + (cb >> 3) * 65536 + (l & 7) * 8192 + kh * 4096 + (cb & 7) * 512 : WS_HP + (cb >> 1) * 16384 + (l & 7) * 2048 + kh * 1024 + (cb & 1) * 512)
                      + (4 * g + ((l >> 3) & 1) * 2) * 8;
#pragma unroll
      for (int ct = 0; ct < 4; ++ct) {
        f32x4 d = {0.f, 0.f, 0.f, 0.f};
#pragma unroll
        for (int k = 0; k < 8; ++k) d = mfma32(tile_frag(L + L_WT, l, k, ct), xa[k], d);
        const unsigned v0 = __float_as_uint((l & 8) ? d[2] : d[0]), v1 = __float_as_uint((l & 8) ? d[3] : d[1]);
        ST_SC1((v4u{(v0 & 0xffffff00u) | tl, th, (v1 & 0xffffff00u) | tl, th}), off + ct * 128);
      }
    } else if (ffn2) {
      dma_tile<128>(a.w2 + fc * 128 * DM + fn * 128, DM * 2, lbase + L_WT, l);
      dma_tile<128>(a.w2 + fc * 128 * DM + fn * 128 + 64, DM * 2, lbase + L_WT + 16384, l);
      const unsigned bb = *(const GAS unsigned*)(a.b1 + fc * 128 + l * 2);
      WGBAR();
      const u64 tag = read_tag(ws);
#pragma nounroll
      for (int spin = 0; spin < SPIN_CAP; ++spin) {
        v4u t[16];
#pragma unroll
        for (int i = 0; i < 16; ++i) { WAIT_TICK(t_in, TH + i * GH); t[i] = LD_SC1(WS_HP + fc * 16384 + i * 1024 + l * 16); }
        unsigned bad = 0;
#pragma unroll
        for (int j = 0; j < 8; ++j) {
          const f32x2 z = gran_sum(t[2 * j], t[2 * j + 1], tag, bad);
          const float h0 = rbf(rbf(z.x) + bf_lo(bb)), h1 = rbf(rbf(z.y) + bf_hi(bb));
          const float r0 = h0 > 0.f ? h0 : 0.f, r1 = h1 > 0.f ? h1 : 0.f;
          *(LAS unsigned*)(L + L_H + j * 256 + l * 4) = cvtpk(r0 * r0, r1 * r1);
        }
        if (__builtin_amdgcn_ballot_w64(bad != 0) == 0) break;
        asm volatile("s_sleep 16" ::: "memory");
      }
      STAMP(1);
      DRAIN();
      LDSWAIT();
      v4u hb[4];
#pragma unroll
      for (int k = 0; k < 4; ++k) hb[k] = *(const LAS v4u*)(L + L_H + (l & 7) * 256 + k * 64 + g * 16);
      wait_lds_flag(L + L_FLAG, tag);
#pragma unroll
      for (int cp = 0; cp < 4; ++cp) {
        f32x4 d0 = {0.f, 0.f, 0.f, 0.f}, d1 = {0.f, 0.f, 0.f, 0.f};
#pragma unroll
        for (int k = 0; k < 4; ++k) {
          d0 = mfma32(tile_frag(L + L_WT + (cp >> 1) * 16384, l, k, (cp & 1) * 2), hb[k], d0);
          d1 = mfma32(tile_frag(L + L_WT + (cp >> 1) * 16384, l, k, (cp & 1) * 2 + 1), hb[k], d1);
        }
        ST_SC1(nosent4(sel4(l & 8, d1, d0)), WS_OF + ((fn * 64 + cp * 16 + (l & 15)) * 16 + fc) * 64 + g * 16);
      }
    } else {
      WGBAR();
    }
    STAMP(2);
    FLUSH();
    return;
  }
  if (w == 1) {
    const v4u sv = {SENT, SENT, SENT, SENT};
    ST_SC1(sv, WS_P + (((kvh * 4 + g) * 8 + b) * 4 + sp) * 256 + (l & 15) * 16);
    if (l < 4) ST_SC1(sv, WS_LSE + ((b * 8 + kvh) * 4 + sp) * 128 + l * 16);
#pragma unroll
    for (int j = 0; j < 2; ++j) ST_SC1(sv, WS_O + ((on * 16 + j * 8 + (l >> 3)) * 32 + oh) * 128 + (l & 7) * 16);
    if (ffn2) {
#pragma unroll
      for (int j = 0; j < 4; ++j) ST_SC1(sv, WS_OF + ((fn * 64 + j * 16 + (l >> 2)) * 16 + fc) * 64 + (l & 3) * 16);
    }
    const v4u xv = *(const GAS v4u*)(a.x + (c & 7) * DM + l * 8), gv = *(const GAS v4u*)(a.gw + l * 8);
    const v2u bb = *(const GAS v2u*)(a.b2 + xr * 16 + 4 * (l & 3));
    WGBAR();
    const u64 tag = read_tag(ws);
    const unsigned tl = (unsigned)tag, th = (unsigned)(tag >> 32);
    DRAIN();
    if (l == 0) __builtin_amdgcn_raw_buffer_store_b64(v2u{tl, th}, rws, WS_F + c * 8, 0, AUX_SC1);
    float gs = 0.f;
#pragma unroll
    for (int j = 0; j < 4; ++j) { gs += rbf(bf_lo(xv[j]) * bf_lo(gv[j])); gs += rbf(bf_hi(xv[j]) * bf_hi(gv[j])); }
    gs += dpp<0xB1>(gs); gs += dpp<0x4E>(gs); gs += dpp<0x124>(gs); gs += dpp<0x128>(gs); gs = xrow_sum(gs);
    gs = rbf(gs);
    const float gt = rbf(1.f / (1.f + __expf(-gs))), og = rbf(1.f - gt);
#pragma nounroll
    for (int spin = 0; spin < SPIN_CAP; ++spin) {
      v4u t[8];
#pragma unroll
      for (int i = 0; i < 8; ++i) { WAIT_TICK(t_in, TQ + i * GQ); t[i] = LD_SC1(WS_QP + kvh * 65536 + b * 8192 + i * 1024 + l * 16); }
      unsigned bad = 0;
#pragma unroll
      for (int j = 0; j < 4; ++j) { const f32x2 q = gran_sum(t[j], t[4 + j], tag, bad); *(LAS unsigned*)(L + L_Q + j * 256 + l * 4) = cvtpk(q.x, q.y); }
      if (__builtin_amdgcn_ballot_w64(bad != 0) == 0) break;
      asm volatile("s_sleep 8" ::: "memory");
    }
    LDSWAIT();
    if (l == 0) *(volatile LAS u64*)(L + L_FLAG) = tag;
    STAMP(3);
    WAIT_TICK(t_in, TF);
#pragma nounroll
    for (int spin = 0; spin < SPIN_CAP; ++spin) {
      const v4u f0 = LD_SC1(WS_F + l * 16), f1 = LD_SC1(WS_F + 1024 + l * 16);
      if (__builtin_amdgcn_ballot_w64(((f0.x ^ tl) | (f0.y ^ th) | (f0.z ^ tl) | (f0.w ^ th) | (f1.x ^ tl) | (f1.y ^ th) | (f1.z ^ tl) | (f1.w ^ th)) != 0) == 0) break;
      asm volatile("s_sleep 16" ::: "memory");
    }
    asm volatile("" ::: "memory");
    STAMP(4);
    {
      v4u r2;
#pragma nounroll
      for (int spin = 0; spin < SPIN_CAP; ++spin) {
        r2 = LD_SC1(l < 56 ? WS_OF + c * 1024 + l * 16 : WS_END);
        if (__builtin_amdgcn_ballot_w64(umax4(r2) == SENT) == 0) break;
        asm volatile("s_sleep 16" ::: "memory");
      }
      f32x4 fv = __builtin_bit_cast(f32x4, r2);
      const float bias[4] = {bf_lo(bb.x), bf_hi(bb.x), bf_lo(bb.y), bf_hi(bb.y)};
#pragma unroll
      for (int j = 0; j < 4; ++j) { float u = fv[j]; u += dpp<0x124>(u); u += dpp<0x128>(u); fv[j] = rbf(og * rbf(rbf(xrow_sum(u)) + bias[j])); }
      if (l < 4) *(LAS f32x4*)(L + L_FIN + l * 16) = fv;
      if (l == 0) *(LAS float*)(L + L_FIN + 64) = gt;
    }
    STAMP(5);
    v4u ml;
#pragma nounroll
    for (int spin = 0; spin < SPIN_CAP * 16; ++spin) {
      ml = *(volatile const LAS v4u*)(L + L_ML + (l & 31) * 16);
      if (__builtin_amdgcn_ballot_w64(((ml.z ^ tl) | (ml.w ^ th)) != 0) == 0) break;
      asm volatile("s_sleep 4" ::: "memory");
    }
    const float mw = __uint_as_float(ml.x);
    float M = fmaxf(mw, dpp<0xB1>(mw)); M = fmaxf(M, dpp<0x4E>(M)); M = fmaxf(M, dpp<0x141>(M));
    float Ls = __builtin_amdgcn_exp2f(mw - M) * __uint_as_float(ml.y);
    Ls += dpp<0xB1>(Ls); Ls += dpp<0x4E>(Ls); Ls += dpp<0x141>(Ls);
    const unsigned lse = nosent(__float_as_uint(M + __builtin_amdgcn_logf(Ls)));
    const v4u lv = {(unsigned)__builtin_amdgcn_readlane((int)lse, 0), (unsigned)__builtin_amdgcn_readlane((int)lse, 8), (unsigned)__builtin_amdgcn_readlane((int)lse, 16),
                    (unsigned)__builtin_amdgcn_readlane((int)lse, 24)};
    if (l < 4) ST_SC1(lv, WS_LSE + ((b * 8 + kvh) * 4 + sp) * 128 + l * 16);
    STAMP(6);
    unsigned ls;
#pragma nounroll
    for (int spin = 0; spin < SPIN_CAP; ++spin) {
      ls = __builtin_amdgcn_raw_buffer_load_b32(rws, WS_LSE + ((b * 8 + kvh) * 4 + (l & 3)) * 128 + ((l >> 3) & 3) * 4, 0, AUX_SC1);
      if (__builtin_amdgcn_ballot_w64(ls == SENT) == 0) break;
      asm volatile("s_sleep 8" ::: "memory");
    }
    const float lf = __uint_as_float(ls);
    float M2 = fmaxf(lf, dpp<0xB1>(lf)); M2 = fmaxf(M2, dpp<0x4E>(M2));
    float E = __builtin_amdgcn_exp2f(lf - M2);
    E += dpp<0xB1>(E); E += dpp<0x4E>(E);
    if (l < 32) *(LAS float*)(L + L_CF + l * 4) = __builtin_amdgcn_exp2f(mw - M2) * __builtin_amdgcn_rcpf(E);
    LDSWAIT();
    STAMP(7);
    FLUSH();
    return;
  }

  const int sw = w - 2;
  const unsigned tb = t_in + T0 + ((sw * (DT / 8)) >> 4);
#define PACE_TO(j) do { asm volatile("s_waitcnt vmcnt(%0)" :: "n"(WIN - 1) : "memory"); WAIT_TICK(tb, slot_tick(j)); } while (0)
  const size_t base = ((size_t)(b * HKV + kvh) * SK + sp * 1024 + sw * 128) * 256;
  const v4u rk = make_srd(a.kc + base, 32768), rv = make_srd(a.vc + base, 32768);
  const int ko = ((l >> 2) & 3) * 256 + g * 64 + (l & 3) * 16, vo = l * 16;
  const int rot = (4 * sw + xr) & 31;
#define OFF(i) ((((i) + rot) & 31) * 1024)
#define LATEST(j) do { } while (0)
#define LOAD_V(i) asm volatile("buffer_load_dwordx4 %0, %1, %2, %3 offen nt" : "+v"(kv[i]) : "v"(vo), "s"(rv), "s"(OFF(i)) : "memory")
  WGBAR();
  {
    const v4u ro = make_srd(a.wo + (oh * 128 + sw * 16) * DM + on * 64, 15 * 1024 + 128);
    const unsigned oo = (unsigned)(l >> 3) * 1024u + (unsigned)(l & 7) * 16u;
    PACE_TO(0); dma_rows(ro, oo, 0, lbase + L_WO + sw * 2048);
    PACE_TO(1); dma_rows(ro, oo, 8192, lbase + L_WO + sw * 2048 + 1024);
  }
  v4u kv[32];
  __builtin_amdgcn_sched_barrier(0);
#pragma unroll
  for (int i = 0; i < 32; ++i) {
    PACE_TO(2 + i);
    asm volatile("buffer_load_dwordx4 %0, %1, %2, %3 offen nt" : "=v"(kv[i]) : "v"(ko), "s"(rk), "s"(OFF(i)) : "memory");
    LATEST(2 + i);
    __builtin_amdgcn_sched_barrier(0);
  }
  STAMP(1);
  const u64 tag = read_tag(ws);
  wait_lds_flag(L + L_FLAG, tag);
  STAMP(2);
  const v4u qf = *(const LAS v4u*)(L + L_Q + ((l >> 2) & 3) * 256 + g * 64 + (l & 3) * 16);
  __builtin_amdgcn_sched_barrier(0);
  constexpr float C2 = 0.088388347648318440f * 1.4426950408889634f;
  float sc[8];
  const int s = l & 3;
#pragma unroll
  for (int m = 0; m < 8; ++m) {
    asm volatile("s_waitcnt vmcnt(%4)" : "+v"(kv[4 * m]), "+v"(kv[4 * m + 1]), "+v"(kv[4 * m + 2]), "+v"(kv[4 * m + 3]) : "n"(28 - 4 * m + m) : "memory");
    f32x4 d[4];
#pragma unroll
    for (int r = 0; r < 4; ++r) d[r] = mfma32(kv[4 * m + r], qf, f32x4{0.f, 0.f, 0.f, 0.f});
#pragma unroll
    for (int r = 0; r < 4; ++r) {
      float t = dpp<0x00>(d[r][0]); t = dpp<0x55>(d[r][1]) + t; t = dpp<0xAA>(d[r][2]) + t; t = dpp<0xFF>(d[r][3]) + t;
      asm volatile("" : "+v"(t));
      sc[m] = (r == 0 || s == r) ? t : sc[m];
    }
    asm volatile("" : "+v"(sc[m]));
    __builtin_amdgcn_sched_barrier(0);
    PACE_TO(34 + m);
    LOAD_V(m);
    LATEST(34 + m);
    __builtin_amdgcn_sched_barrier(0);
  }
  STAMP(3);
  float mx = sc[0];
#pragma unroll
  for (int m = 1; m < 8; ++m) mx = fmaxf(mx, sc[m]);
  mx = fmaxf(mx, dpp<0xB1>(mx)); mx = fmaxf(mx, dpp<0x4E>(mx)); mx = xrow_max(mx);
  const float mneg = -mx * C2;
  float lsum = 0.f;
  unsigned pk[8];
#pragma unroll
  for (int m = 0; m < 8; ++m) { const float p = __builtin_amdgcn_exp2f(fmaf(sc[m], C2, mneg)); pk[m] = cvtpk(p, p); lsum += bf_lo(pk[m]); }
  lsum += dpp<0xB1>(lsum); lsum += dpp<0x4E>(lsum); lsum = xrow_sum(lsum);
  if (l < 16 && (l & 3) == 0) *(LAS v4u*)(L + L_ML + ((l >> 2) * 8 + sw) * 16) = v4u{__float_as_uint(mx * C2), __float_as_uint(lsum), (unsigned)tag, (unsigned)(tag >> 32)};
  const unsigned half = (l & 1) ? 0xffff0000u : 0x0000ffffu;
  const unsigned m0 = (l & 2) ? 0u : half, m1 = (l & 2) ? half : 0u;
  f32x4 o0 = {0.f, 0.f, 0.f, 0.f}, o1 = {0.f, 0.f, 0.f, 0.f};
#pragma unroll
  for (int j = 8; j < 40; ++j) {
    if (j < 32) { PACE_TO(34 + j); LOAD_V(j); LATEST(34 + j); __builtin_amdgcn_sched_barrier(0); }
    if (j == 31) STAMP(11);
    const int i = j - 8;
    const unsigned pb = (i & 3) == 0 ? dppu<0x00>(pk[i >> 2]) : (i & 3) == 1 ? dppu<0x55>(pk[i >> 2]) : (i & 3) == 2 ? dppu<0xAA>(pk[i >> 2]) : dppu<0xFF>(pk[i >> 2]);
    const v2u av = {pb & m0, pb & m1};
    asm volatile("s_waitcnt vmcnt(%1)" : "+v"(kv[i]) : "n"((j < 32 ? j : 31) - i) : "memory");
    o0 = mfma16(av, v2u{kv[i].x, kv[i].y}, o0);
    o1 = mfma16(av, v2u{kv[i].z, kv[i].w}, o1);
    __builtin_amdgcn_sched_barrier(0);
  }
  *(LAS f32x4*)(L + L_MRG + (sw * 4 + g) * 512 + (l & 15) * 32) = o0;
  *(LAS f32x4*)(L + L_MRG + (sw * 4 + g) * 512 + (l & 15) * 32 + 16) = o1;
  STAMP(4);
  LDSWAIT();
  WGBAR();
  if (sw < 4) {
    STAMP(5);
    const f32x4 c0 = *(const LAS f32x4*)(L + L_CF + sw * 32), c1 = *(const LAS f32x4*)(L + L_CF + sw * 32 + 16);
    f32x2 acc = {0.f, 0.f};
#pragma unroll
    for (int j = 0; j < 8; ++j) {
      const f32x2 ov = *(const LAS f32x2*)(L + L_MRG + (j * 4 + sw) * 512 + l * 8);
      const float e = j < 4 ? c0[j & 3] : c1[j & 3];
      acc.x = fmaf(e, ov.x, acc.x); acc.y = fmaf(e, ov.y, acc.y);
    }
    const unsigned pv = nosent(cvtpk(acc.x, acc.y));
    const v4u pq = {dppu<0x00>(pv), dppu<0x55>(pv), dppu<0xAA>(pv), dppu<0xFF>(pv)};
    if ((l & 3) == 0) ST_SC1(pq, WS_P + (((kvh * 4 + sw) * 8 + b) * 4 + sp) * 256 + l * 4);
    STAMP(6);
    v4u wf[4];
#pragma unroll
    for (int k = 0; k < 4; ++k) wf[k] = tile_frag(L + L_WO, l, k, sw);
    v4u p0, p1;
    if (PDELAY1) asm volatile("s_sleep %0" :: "n"(PDELAY1) : "memory");
    poll2(rws, WS_P + (oh * 8 + 2 * sw) * 1024 + l * 16, WS_P + (oh * 8 + 2 * sw) * 1024 + 1024 + l * 16, p0, p1);
    *(LAS v4u*)(L + L_PG + (8 * sw + g) * 272 + (l & 15) * 16) = p0;
    *(LAS v4u*)(L + L_PG + (8 * sw + 4 + g) * 272 + (l & 15) * 16) = p1;
    STAMP(7);
    LDSWAIT();
    WGBAR();
    const int pb = l & 7, sl = (l >> 3) & 1;
    f32x4 da = {0.f, 0.f, 0.f, 0.f}, db = {0.f, 0.f, 0.f, 0.f};
#pragma unroll
    for (int k = 0; k < 4; ++k) {
      const v4u pa = *(const LAS v4u*)(L + L_PG + (pb * 4 + sl) * 272 + k * 64 + g * 16), pc = *(const LAS v4u*)(L + L_PG + (pb * 4 + 2 + sl) * 272 + k * 64 + g * 16);
      da = mfma32(wf[k], pa, da); db = mfma32(wf[k], pc, db);
    }
    f32x4 r;
#pragma unroll
    for (int j = 0; j < 4; ++j) { const float t = da[j] + db[j]; r[j] = t + dpp<0x128>(t); }
    if (sl == 0) ST_SC1(nosent4(r), WS_O + (((on * 4 + sw) * 4 + (pb >> 1)) * 32 + oh) * 128 + (pb & 1) * 64 + g * 16);
    STAMP(8);
    if (sw == 0) {
      const f32x4 fv = *(const LAS f32x4*)(L + L_FIN + (l & 3) * 16);
      const float gt = *(const LAS float*)(L + L_FIN + 64);
      v4u r0, r1;
      if (PDELAY2) asm volatile("s_sleep %0" :: "n"(PDELAY2) : "memory");
      poll2(rws, WS_O + (c >> 1) * 4096 + (l >> 2) * 128 + (c & 1) * 64 + (l & 3) * 16, WS_O + (c >> 1) * 4096 + 2048 + (l >> 2) * 128 + (c & 1) * 64 + (l & 3) * 16, r0, r1);
      STAMP(9);
      f32x4 av = __builtin_bit_cast(f32x4, r0) + __builtin_bit_cast(f32x4, r1);
#pragma unroll
      for (int j = 0; j < 4; ++j) { float t = av[j]; t += dpp<0x124>(t); t += dpp<0x128>(t); av[j] = rbf(gt * rbf(xrow_sum(t))) + fv[j]; }
      if (l < 4) *(GAS v2u*)(a.out + (c & 7) * DM + xr * 16 + 4 * l) = v2u{cvtpk(av[0], av[1]), cvtpk(av[2], av[3])};
      if (c == 0 && l == 0) *(GAS u64*)(ws + WS_E) = tag;
      STAMP(10);
      asm volatile("s_waitcnt vmcnt(0)" :: "v"(r0), "v"(r1) : "memory");
    }
    asm volatile("s_waitcnt vmcnt(0)" :: "v"(p0), "v"(p1) : "memory");
  }
  FLUSH();
}

extern "C" void kernel_launch(void* const* d_in, const int* in_sizes, int n_in, void* d_out, int out_size, void* d_ws, size_t ws_size,
                              hipStream_t stream) {
  Args a;
  a.x = (const unsigned short*)d_in[0];
  a.kc = (const unsigned char*)d_in[1];
  a.vc = (const unsigned char*)d_in[2];
  a.wq = (const unsigned short*)d_in[3];
  a.wo = (const unsigned short*)d_in[4];
  a.w1 = (const unsigned short*)d_in[6];
  a.b1 = (const unsigned short*)d_in[7];
  a.w2 = (const unsigned short*)d_in[8];
  a.b2 = (const unsigned short*)d_in[9];
  a.gw = (const unsigned short*)d_in[10];
  a.ws = (unsigned char*)d_ws + WSOFF;
  a.out = (unsigned short*)d_out;
  fused_decode<<<256, NWAVE * 64, 0, stream>>>(a);
}
```

```cpp
#define DT 485
#define T0 130
#define SHIFT 0
#define NB 2
#include <hip/hip_runtime.h>
#include <hip/hip_bf16.h>
#include <stdint.h>

typedef unsigned v4u __attribute__((ext_vector_type(4)));
typedef unsigned v2u __attribute__((ext_vector_type(2)));
typedef float f32x4 __attribute__((ext_vector_type(4)));
typedef float f32x2 __attribute__((ext_vector_type(2)));
typedef __bf16 bf16x8 __attribute__((ext_vector_type(8)));
typedef __bf16 bf16x2 __attribute__((ext_vector_type(2)));
typedef short s16x4 __attribute__((ext_vector_type(4)));
typedef unsigned long long u64;
#define GAS __attribute__((address_space(1)))
#define LAS __attribute__((address_space(3)))

constexpr int DM = 512, HKV = 8, SK = 4096, FF = 1792;
constexpr int AUX_SC1 = 16;
#define WIN 5
#define TQ 560
#define TH 560
#define TF 1500
#define GQ 25
#define GH 15
#define PDELAY1 12
#define PDELAY2 12
constexpr int J0 = ((TQ - T0) * 16) / DT;
constexpr int slot_tick(int j) { return (j * DT + (SHIFT ? (j <= J0 ? 0 : j - J0 < 8 ? j - J0 : 8) * (DT / 8) : 0)) >> 4; }
#define NBD 0
#define RMAP 0
constexpr int NWAVE = 10;

constexpr int WS_E = 0;
constexpr int WS_F = 2048;
constexpr int WS_QP = 4096;
constexpr int WS_HP = WS_QP + 8 * 8 * 2 * 512 * 8;
constexpr int WS_X = WS_HP + 7 * 8 * 2 * 256 * 8;
constexpr int WS_LSE = WS_X + 256 * 128;
constexpr int WS_END = WS_LSE + 256 * 128;
constexpr int WS_O = WS_QP;
constexpr int WS_P = WS_HP;
static_assert(WS_LSE - WS_P == 32 * 8 * 4 * 256, "the partials fill exactly the lines of HP and X");
constexpr int WS_ST = 8 << 20;
constexpr int L_WO = 0, L_WT = 16384, L_MRG = 49152, L_ML = L_MRG + 16384, L_CF = L_ML + 512, L_Q = L_CF + 128, L_H = L_Q + 1024, L_FF = L_H + 4096,
              L_G = L_FF + 4096, L_FIN = L_G + 32, L_FLAG = L_FIN + 96, L_END = L_FLAG + 16;
constexpr int L_PG = L_WT;

__device__ __forceinline__ f32x4 mfma32(v4u a, v4u b, f32x4 c) {
  return __builtin_amdgcn_mfma_f32_16x16x32_bf16(__builtin_bit_cast(bf16x8, a), __builtin_bit_cast(bf16x8, b), c, 0, 0, 0);
}
__device__ __forceinline__ f32x4 mfma16(v2u a, v2u b, f32x4 c) {
  return __builtin_amdgcn_mfma_f32_16x16x16bf16_1k(__builtin_bit_cast(s16x4, a), __builtin_bit_cast(s16x4, b), c, 0, 0, 0);
}
__device__ __forceinline__ unsigned cvtpk(float lo, float hi) { bf16x2 r = {(__bf16)lo, (__bf16)hi}; return __builtin_bit_cast(unsigned, r); }
__device__ __forceinline__ float bf_lo(unsigned u) { return __builtin_bit_cast(float, u << 16); }
__device__ __forceinline__ float bf_hi(unsigned u) { return __builtin_bit_cast(float, u & 0xffff0000u); }
__device__ __forceinline__ float rbf(float x) { return (float)(__bf16)x; }
template <int CTRL> __device__ __forceinline__ float dpp(float x) { return __builtin_bit_cast(float, __builtin_amdgcn_mov_dpp(__builtin_bit_cast(int, x), CTRL, 0xf, 0xf, true)); }
template <int CTRL> __device__ __forceinline__ unsigned dppu(unsigned x) { return (unsigned)__builtin_amdgcn_mov_dpp((int)x, CTRL, 0xf, 0xf, true); }
__device__ __forceinline__ float xrow_max(float x) {
  auto s = __builtin_amdgcn_permlane16_swap(__float_as_uint(x), __float_as_uint(x), false, false);
  x = fmaxf(__uint_as_float(s[0]), __uint_as_float(s[1]));
  auto t = __builtin_amdgcn_permlane32_swap(__float_as_uint(x), __float_as_uint(x), false, false);
  return fmaxf(__uint_as_float(t[0]), __uint_as_float(t[1]));
}
__device__ __forceinline__ float xrow_sum(float x) {
  auto s = __builtin_amdgcn_permlane16_swap(__float_as_uint(x), __float_as_uint(x), false, false);
  x = __uint_as_float(s[0]) + __uint_as_float(s[1]);
  auto t = __builtin_amdgcn_permlane32_swap(__float_as_uint(x), __float_as_uint(x), false, false);
  return __uint_as_float(t[0]) + __uint_as_float(t[1]);
}
__device__ __forceinline__ v2u tr_read(const LAS unsigned char* p) { return __builtin_bit_cast(v2u, __builtin_amdgcn_ds_read_tr16_b64_v4i16((LAS s16x4*)p)); }
__device__ __forceinline__ v4u make_srd(const void* p, unsigned bytes) {
  const u64 a = (u64)p;
  return v4u{(unsigned)a, (unsigned)(a >> 32) & 0xffffu, bytes, 0x00020000u};
}
__device__ __forceinline__ void dma_rows(v4u srd, unsigned voff, unsigned soff, unsigned lds_addr) {
  unsigned keep;
  asm volatile("s_mov_b32 %0, m0\n\ts_mov_b32 m0, %1\n\ts_nop 0\n\tbuffer_load_dwordx4 %2, %3, %4 offen nt lds\n\ts_mov_b32 m0, %0"
               : "=&s"(keep) : "s"(lds_addr), "v"(voff), "s"(srd), "s"(soff) : "memory");
}
template <int ROWS> __device__ __forceinline__ void dma_tile(const void* src, unsigned ldb, unsigned lds_addr, int l) {
  const v4u srd = make_srd(src, (ROWS - 1) * ldb + 128u);
  const unsigned vo = (unsigned)(l >> 3) * ldb + (unsigned)(l & 7) * 16u;
#pragma unroll 4
  for (int i = 0; i < ROWS / 8; ++i) dma_rows(srd, vo, i * 8 * ldb, lds_addr + i * 1024);
}
__device__ __forceinline__ v4u tile_frag(const LAS unsigned char* tile, int l, int k, int ct) {
  const LAS unsigned char* p = tile + (8 * (l >> 4) + ((l & 15) >> 2)) * 128 + (l & 3) * 8 + k * 4096 + ct * 32;
  const v2u t0 = tr_read(p), t1 = tr_read(p + 512);
  return v4u{t0.x, t0.y, t1.x, t1.y};
}
#define DRAIN() asm volatile("s_waitcnt vmcnt(0)" ::: "memory")
#define LDSWAIT() asm volatile("s_waitcnt lgkmcnt(0)" ::: "memory")
#define WGBAR() do { __builtin_amdgcn_sched_barrier(0); __builtin_amdgcn_s_barrier(); __builtin_amdgcn_sched_barrier(0); asm volatile("" ::: "memory"); } while (0)
__device__ __forceinline__ u64 read_tag(const unsigned char* ws) {
  u64 t;
  asm volatile("s_load_dwordx2 %0, %1, 0x0\n\ts_waitcnt lgkmcnt(0)" : "=s"(t) : "s"(ws) : "memory");
  const unsigned old = (unsigned)t;
  t += 0x9E3779B97F4A7C15ull;
  t = (t ^ (t >> 30)) * 0xBF58476D1CE4E5B9ull; t = (t ^ (t >> 27)) * 0x94D049BB133111EBull;
  t ^= t >> 31;
  return (unsigned)t == old ? t ^ 1u : t;
}
__device__ __forceinline__ unsigned now_ticks() {
  u64 t;
  asm volatile("s_memrealtime %0\n\ts_waitcnt lgkmcnt(0)" : "=s"(t) :: "memory");
  return (unsigned)t;
}
#define WAIT_TICK(base, delta) \
  asm volatile("s_branch 2f\n1:\n\ts_sleep 1\n2:\n\ts_memrealtime vcc\n\ts_waitcnt lgkmcnt(0)\n\ts_sub_u32 vcc_lo, vcc_lo, %0\n\ts_cmpk_lt_i32 vcc_lo, %1\n\ts_cbranch_scc1 1b" \
               :: "s"(base), "n"(delta) : "scc", "vcc", "memory")
constexpr int SPIN_CAP = 1 << 18;
#define LD_SC1(off) __builtin_amdgcn_raw_buffer_load_b128(rws, (off), 0, AUX_SC1)
#define ST_SC1(v, off) __builtin_amdgcn_raw_buffer_store_b128((v), rws, (off), 0, AUX_SC1)
__device__ __forceinline__ v4u gran2(float a, float b, unsigned tg) { return v4u{__float_as_uint(a), tg, __float_as_uint(b), tg}; }
__device__ __forceinline__ unsigned gbad(v4u g, unsigned tg) { return (g.y ^ tg) | (g.w ^ tg); }
__device__ __forceinline__ f32x2 gval(v4u g) { return f32x2{__uint_as_float(g.x), __uint_as_float(g.z)}; }
__device__ __forceinline__ v4u gran_d(f32x4 d, int l, unsigned tg) { return gran2((l & 8) ? d[2] : d[0], (l & 8) ? d[3] : d[1], tg); }

#define STAMP(k) do { } while (0)
#define FLUSH() do { } while (0)

__device__ __forceinline__ void poll2(__amdgpu_buffer_rsrc_t rws, int o0, int o1, unsigned tg, v4u& r0, v4u& r1) {
  bool need = true;
#pragma nounroll
  for (int spin = 0; spin < SPIN_CAP && need; ++spin) {
    r0 = LD_SC1(o0); r1 = LD_SC1(o1);
    need = (r0.y == tg) | (r0.w == tg) | (r1.y == tg) | (r1.w == tg);
    asm volatile("" ::: "memory");
  }
}
__device__ __forceinline__ unsigned notag(unsigned x, unsigned tg) { return x == tg ? x ^ 1u : x; }
__device__ __forceinline__ float gate_of(v4u xv, v4u gv) {
  float gs = 0.f;
#pragma unroll
  for (int j = 0; j < 4; ++j) { gs += rbf(bf_lo(xv[j]) * bf_lo(gv[j])); gs += rbf(bf_hi(xv[j]) * bf_hi(gv[j])); }
  gs += dpp<0xB1>(gs); gs += dpp<0x4E>(gs); gs += dpp<0x124>(gs); gs += dpp<0x128>(gs); gs = xrow_sum(gs);
  return rbf(1.f / (1.f + __expf(-rbf(gs))));
}

struct Args {
  const unsigned short *x, *wq, *wo, *w1, *b1, *w2, *b2, *gw;
  const unsigned char *kc, *vc;
  unsigned char* ws;
  unsigned short* out;
};

__global__ void __launch_bounds__(NWAVE * 64) fused_decode(Args a) {
  __shared__ __attribute__((aligned(16))) unsigned char smem[L_END];
  LAS unsigned char* L = (LAS unsigned char*)smem;
  const unsigned lbase = (unsigned)(uintptr_t)L;
  const int tid = threadIdx.x, l = tid & 63, w = __builtin_amdgcn_readfirstlane(tid >> 6);
  const int c = blockIdx.x, xr = c >> 3, sp = xr & 3, kvh = xr >> 2, b = (c - kvh) & 7, i32 = b * 4 + sp;
  unsigned char* ws = a.ws;
  STAMP(0);
  const unsigned t_in = now_ticks();
  const __amdgpu_buffer_rsrc_t rws = __builtin_amdgcn_make_buffer_rsrc((void*)ws, 0, WS_END, 0x00020000);
  const int g = l >> 4;
  const int oh = (c & 7) * 4 + (xr >> 3), on = xr & 7;
  const int rr = RMAP ? ((c & ~15) | ((c & 7) << 1) | ((c >> 3) & 1)) : c, rb = rr & 7, rc = rr >> 3;
  const bool ffn2 = xr >= 24 && (c & 7) < 7;

  if (w == 0) {
    if (xr < 23) {
      const bool isq = xr < 16;
      const int t = isq ? ((xr >> 1) * 8 + ((c + xr) & 7)) * 2 + (xr & 1) : (c & 7) * 7 + xr - 16;
      const int cb = t >> 1, kh = t & 1;
      const unsigned ldb = isq ? 8192u : (unsigned)FF * 2u;
      dma_tile<256>((const unsigned char*)(isq ? a.wq : a.w1) + (size_t)kh * 256 * ldb + cb * 128, ldb, lbase + L_WT, l);
      v4u xa[8];
#pragma unroll
      for (int k = 0; k < 8; ++k) xa[k] = *(const GAS v4u*)(a.x + (l & 7) * DM + kh * 256 + 32 * k + 8 * g);
      const unsigned tg = (unsigned)read_tag(ws);
      DRAIN();
      STAMP(1);
      const int off = (isq ? WS_QP + (cb >> 3) * 65536 + (l & 7) * 8192 + kh * 4096 + (cb & 7) * 512 : WS_HP + (cb >> 2) * 32768 + (l & 7) * 4096 + kh * 2048 + (cb & 3) * 512)
                      + g * 32 + (l & 8) * 2;
#pragma unroll
      for (int ct = 0; ct < 4; ++ct) {
        f32x4 d = {0.f, 0.f, 0.f, 0.f};
#pragma unroll
        for (int k = 0; k < 8; ++k) d = mfma32(tile_frag(L + L_WT, l, k, ct), xa[k], d);
        ST_SC1(gran_d(d, l, tg), off + ct * 128);
      }
    } else if (ffn2) {
      const int fc = c & 7;
      dma_tile<256>(a.w2 + fc * 256 * DM + on * 64, DM * 2, lbase + L_WT, l);
      const unsigned bb0 = *(const GAS unsigned*)(a.b1 + fc * 256 + l * 2), bb1 = *(const GAS unsigned*)(a.b1 + fc * 256 + 128 + l * 2);
      const v4u gv = *(const GAS v4u*)(a.gw + l * 8);
      float gt = 0.f;
#pragma unroll
      for (int j = 0; j < 8; ++j) { const float t = gate_of(*(const GAS v4u*)(a.x + j * DM + l * 8), gv); gt = (l & 7) == j ? t : gt; }
      if (l < 8) *(LAS float*)(L + L_G + l * 4) = gt;
      const float og = rbf(1.f - gt);
      const u64 tag = read_tag(ws);
      const unsigned tg = (unsigned)tag;
#pragma unroll
      for (int hf = 0; hf < 2; ++hf) {
#pragma nounroll
        for (int spin = 0; spin < SPIN_CAP; ++spin) {
          v4u t[16];
#pragma unroll
          for (int i = 0; i < 16; ++i) { WAIT_TICK(t_in, TH + (hf * 16 + i) * GH); t[i] = LD_SC1(WS_HP + fc * 32768 + hf * 16384 + i * 1024 + l * 16); }
          unsigned bad = 0;
#pragma unroll
          for (int j = 0; j < 8; ++j) {
            const int i0 = (j >> 1) * 4 + (j & 1);
            const unsigned bb = (j & 1) ? bb1 : bb0;
            bad |= gbad(t[i0], tg) | gbad(t[i0 + 2], tg);
            const f32x2 z = gval(t[i0]) + gval(t[i0 + 2]);
            const float h0 = rbf(rbf(z.x) + bf_lo(bb)), h1 = rbf(rbf(z.y) + bf_hi(bb));
            const float r0 = h0 > 0.f ? h0 : 0.f, r1 = h1 > 0.f ? h1 : 0.f;
            *(LAS unsigned*)(L + L_H + (hf * 8 + j) * 256 + l * 4) = cvtpk(r0 * r0, r1 * r1);
          }
          if (__builtin_amdgcn_ballot_w64(bad != 0) == 0) break;
          asm volatile("s_sleep 16" ::: "memory");
        }
      }
      STAMP(1);
      DRAIN();
      LDSWAIT();
      if (l == 0) *(volatile LAS u64*)(L + L_FLAG + 8) = tag;
      v4u hb[8];
#pragma unroll
      for (int k = 0; k < 8; ++k) hb[k] = *(const LAS v4u*)(L + L_H + (l & 7) * 512 + k * 64 + g * 16);
#pragma unroll
      for (int ct = 0; ct < 4; ++ct) {
        f32x4 d = {0.f, 0.f, 0.f, 0.f};
#pragma unroll
        for (int k = 0; k < 8; ++k) d = mfma32(tile_frag(L + L_WT, l, k, ct), hb[k], d);
        *(LAS f32x4*)(L + L_FF + ct * 1024 + l * 16) = d * og;
      }
      LDSWAIT();
    }
    STAMP(2);
    FLUSH();
    return;
  }
  if (w == 1) {
    const v4u xv = *(const GAS v4u*)(a.x + rb * DM + l * 8), gv = *(const GAS v4u*)(a.gw + l * 8);
    const v2u bb = *(const GAS v2u*)(a.b2 + rc * 16 + 4 * (l & 3));
    const u64 tag = read_tag(ws);
    const unsigned tg = (unsigned)tag, th = (unsigned)(tag >> 32);
    if (l < 8) ST_SC1((v4u{0u, tg, 0u, tg}), WS_X + c * 128 + l * 16);
    {
      const float gt = gate_of(xv, gv), og = rbf(1.f - gt);
      if (l < 4) *(LAS f32x4*)(L + L_FIN + l * 16) = f32x4{bf_lo(bb.x), bf_hi(bb.x), bf_lo(bb.y), bf_hi(bb.y)} * og;
      if (l == 0) *(LAS float*)(L + L_FIN + 64) = gt;
    }
#pragma nounroll
    for (int spin = 0; spin < SPIN_CAP; ++spin) {
      v4u t[8];
#pragma unroll
      for (int i = 0; i < 8; ++i) { WAIT_TICK(t_in, TQ + i * GQ); t[i] = LD_SC1(WS_QP + kvh * 65536 + b * 8192 + i * 1024 + l * 16); }
      unsigned bad = 0;
#pragma unroll
      for (int j = 0; j < 4; ++j) {
        bad |= gbad(t[j], tg) | gbad(t[4 + j], tg);
        const f32x2 q = gval(t[j]) + gval(t[4 + j]);
        *(LAS unsigned*)(L + L_Q + j * 256 + l * 4) = cvtpk(q.x, q.y);
      }
      if (__builtin_amdgcn_ballot_w64(bad != 0) == 0) break;
      asm volatile("s_sleep 8" ::: "memory");
    }
    LDSWAIT();
    if (l == 0) *(volatile LAS u64*)(L + L_FLAG) = tag;
    STAMP(3);
    if (ffn2) {
#pragma nounroll
      for (int spin = 0; spin < SPIN_CAP * 16; ++spin) { if (*(volatile const LAS u64*)(L + L_FLAG + 8) == tag) break; asm volatile("s_sleep 8" ::: "memory"); }
    }
    DRAIN();
    if (l == 0) __builtin_amdgcn_raw_buffer_store_b64(v2u{tg, th}, rws, WS_F + c * 8, 0, AUX_SC1);
    STAMP(5);
    v4u ml;
#pragma nounroll
    for (int spin = 0; spin < SPIN_CAP * 16; ++spin) {
      ml = *(volatile const LAS v4u*)(L + L_ML + (l & 31) * 16);
      if (__builtin_amdgcn_ballot_w64(((ml.z ^ tg) | (ml.w ^ th)) != 0) == 0) break;
      asm volatile("s_sleep 4" ::: "memory");
    }
    const float mw = __uint_as_float(ml.x);
    float M = fmaxf(mw, dpp<0xB1>(mw)); M = fmaxf(M, dpp<0x4E>(M)); M = fmaxf(M, dpp<0x141>(M));
    float Ls = __builtin_amdgcn_exp2f(mw - M) * __uint_as_float(ml.y);
    Ls += dpp<0xB1>(Ls); Ls += dpp<0x4E>(Ls); Ls += dpp<0x141>(Ls);
    const float lse = M + __builtin_amdgcn_logf(Ls);
    const v4u lg = gran2(lse, dpp<0x128>(lse), tg);
    if (l < 32 && (l & 15) == 0) ST_SC1(lg, WS_LSE + ((b * 8 + kvh) * 4 + sp) * 128 + l);
    STAMP(6);
    v2u ls;
    asm volatile("s_sleep 16" ::: "memory");
#pragma nounroll
    for (int spin = 0; spin < SPIN_CAP; ++spin) {
      ls = __builtin_amdgcn_raw_buffer_load_b64(rws, WS_LSE + ((b * 8 + kvh) * 4 + (l & 3)) * 128 + ((l >> 3) & 3) * 8, 0, AUX_SC1);
      if (__builtin_amdgcn_ballot_w64(ls.y != tg) == 0) break;
      asm volatile("s_sleep 8" ::: "memory");
    }
    const float lf = __uint_as_float(ls.x);
    float M2 = fmaxf(lf, dpp<0xB1>(lf)); M2 = fmaxf(M2, dpp<0x4E>(M2));
    float E = __builtin_amdgcn_exp2f(lf - M2);
    E += dpp<0xB1>(E); E += dpp<0x4E>(E);
    if (l < 32) *(LAS float*)(L + L_CF + l * 4) = __builtin_amdgcn_exp2f(mw - M2) * __builtin_amdgcn_rcpf(E);
    WAIT_TICK(t_in, TF);
#pragma nounroll
    for (int spin = 0; spin < SPIN_CAP; ++spin) {
      const v4u f0 = LD_SC1(WS_F + l * 16), f1 = LD_SC1(WS_F + 1024 + l * 16);
      if (__builtin_amdgcn_ballot_w64(((f0.x ^ tg) | (f0.y ^ th) | (f0.z ^ tg) | (f0.w ^ th) | (f1.x ^ tg) | (f1.y ^ th) | (f1.z ^ tg) | (f1.w ^ th)) != 0) == 0) break;
      asm volatile("s_sleep 16" ::: "memory");
    }
    LDSWAIT();
    STAMP(7);
    FLUSH();
    return;
  }

  const int sw = w - 2;
  const unsigned tb = t_in + T0 + ((sw * (DT / 8)) >> 4);
#define PACE_TO(j) do { if ((j) <= 65 - NB) { asm volatile("s_waitcnt vmcnt(%0)" :: "n"(WIN - 1) : "memory"); WAIT_TICK(tb, slot_tick(j)); } else if (NBD) WAIT_TICK(tb, slot_tick(65 - NB) + ((j) - (65 - NB)) * NBD); } while (0)
  const size_t base = ((size_t)(b * HKV + kvh) * SK + sp * 1024 + sw * 128) * 256;
  const v4u rk = make_srd(a.kc + base, 32768), rv = make_srd(a.vc + base, 32768);
  const int ko = ((l >> 2) & 3) * 256 + g * 64 + (l & 3) * 16, vo = l * 16;
  const int rot = (4 * sw + xr) & 31;
#define OFF(i) ((((i) + rot) & 31) * 1024)
#define LOAD_V(i) asm volatile("buffer_load_dwordx4 %0, %1, %2, %3 offen nt" : "+v"(kv[i]) : "v"(vo), "s"(rv), "s"(OFF(i)) : "memory")
  {
    const v4u ro = make_srd(a.wo + (oh * 128 + sw * 16) * DM + on * 64, 15 * 1024 + 128);
    const unsigned oo = (unsigned)(l >> 3) * 1024u + (unsigned)(l & 7) * 16u;
    PACE_TO(0); dma_rows(ro, oo, 0, lbase + L_WO + sw * 2048);
    PACE_TO(1); dma_rows(ro, oo, 8192, lbase + L_WO + sw * 2048 + 1024);
  }
  v4u kv[32];
  __builtin_amdgcn_sched_barrier(0);
#pragma unroll
  for (int i = 0; i < 32; ++i) {
    PACE_TO(2 + i);
    asm volatile("buffer_load_dwordx4 %0, %1, %2, %3 offen nt" : "=v"(kv[i]) : "v"(ko), "s"(rk), "s"(OFF(i)) : "memory");
    __builtin_amdgcn_sched_barrier(0);
  }
  STAMP(1);
  const u64 tag = read_tag(ws);
  const unsigned tg = (unsigned)tag;
#pragma nounroll
  for (int spin = 0; spin < SPIN_CAP * 16; ++spin) { if (*(volatile const LAS u64*)(L + L_FLAG) == tag) break; asm volatile("s_sleep 2" ::: "memory"); }
  asm volatile("" ::: "memory");
  STAMP(2);
  const v4u qf = *(const LAS v4u*)(L + L_Q + ((l >> 2) & 3) * 256 + g * 64 + (l & 3) * 16);
  __builtin_amdgcn_sched_barrier(0);
  constexpr float C2 = 0.088388347648318440f * 1.4426950408889634f;
  float sc[8];
  const int s = l & 3;
#pragma unroll
  for (int m = 0; m < 8; ++m) {
    asm volatile("s_waitcnt vmcnt(%4)" : "+v"(kv[4 * m]), "+v"(kv[4 * m + 1]), "+v"(kv[4 * m + 2]), "+v"(kv[4 * m + 3]) : "n"(28 - 4 * m + m) : "memory");
    f32x4 d[4];
#pragma unroll
    for (int r = 0; r < 4; ++r) d[r] = mfma32(kv[4 * m + r], qf, f32x4{0.f, 0.f, 0.f, 0.f});
#pragma unroll
    for (int r = 0; r < 4; ++r) {
      float t = dpp<0x00>(d[r][0]); t = dpp<0x55>(d[r][1]) + t; t = dpp<0xAA>(d[r][2]) + t; t = dpp<0xFF>(d[r][3]) + t;
      asm volatile("" : "+v"(t));
      sc[m] = (r == 0 || s == r) ? t : sc[m];
    }
    asm volatile("" : "+v"(sc[m]));
    __builtin_amdgcn_sched_barrier(0);
    PACE_TO(34 + m);
    LOAD_V(m);
    __builtin_amdgcn_sched_barrier(0);
  }
  STAMP(3);
  float mx = sc[0];
#pragma unroll
  for (int m = 1; m < 8; ++m) mx = fmaxf(mx, sc[m]);
  mx = fmaxf(mx, dpp<0xB1>(mx)); mx = fmaxf(mx, dpp<0x4E>(mx)); mx = xrow_max(mx);
  const float mneg = -mx * C2;
  float lsum = 0.f;
  unsigned pk[8];
#pragma unroll
  for (int m = 0; m < 8; ++m) { const float p = __builtin_amdgcn_exp2f(fmaf(sc[m], C2, mneg)); pk[m] = cvtpk(p, p); lsum += bf_lo(pk[m]); }
  lsum += dpp<0xB1>(lsum); lsum += dpp<0x4E>(lsum); lsum = xrow_sum(lsum);
  if (l < 16 && (l & 3) == 0) *(LAS v4u*)(L + L_ML + ((l >> 2) * 8 + sw) * 16) = v4u{__float_as_uint(mx * C2), __float_as_uint(lsum), tg, (unsigned)(tag >> 32)};
  const unsigned half = (l & 1) ? 0xffff0000u : 0x0000ffffu;
  const unsigned m0 = (l & 2) ? 0u : half, m1 = (l & 2) ? half : 0u;
  f32x4 o0 = {0.f, 0.f, 0.f, 0.f}, o1 = {0.f, 0.f, 0.f, 0.f};
#pragma unroll
  for (int j = 8; j < 40; ++j) {
    if (j < 32) { PACE_TO(34 + j); LOAD_V(j); __builtin_amdgcn_sched_barrier(0); }
    if (j == 31) STAMP(11);
    const int i = j - 8;
    const unsigned pb = (i & 3) == 0 ? dppu<0x00>(pk[i >> 2]) : (i & 3) == 1 ? dppu<0x55>(pk[i >> 2]) : (i & 3) == 2 ? dppu<0xAA>(pk[i >> 2]) : dppu<0xFF>(pk[i >> 2]);
    const v2u av = {pb & m0, pb & m1};
    asm volatile("s_waitcnt vmcnt(%1)" : "+v"(kv[i]) : "n"((j < 32 ? j : 31) - i) : "memory");
    o0 = mfma16(av, v2u{kv[i].x, kv[i].y}, o0);
    o1 = mfma16(av, v2u{kv[i].z, kv[i].w}, o1);
    __builtin_amdgcn_sched_barrier(0);
  }
  *(LAS f32x4*)(L + L_MRG + (sw * 4 + g) * 512 + (l & 15) * 32) = o0;
  *(LAS f32x4*)(L + L_MRG + (sw * 4 + g) * 512 + (l & 15) * 32 + 16) = o1;
  STAMP(4);
  LDSWAIT();
  WGBAR();
  if (sw < 4) {
    STAMP(5);
    const f32x4 c0 = *(const LAS f32x4*)(L + L_CF + sw * 32), c1 = *(const LAS f32x4*)(L + L_CF + sw * 32 + 16);
    f32x2 acc = {0.f, 0.f};
#pragma unroll
    for (int j = 0; j < 8; ++j) {
      const f32x2 ov = *(const LAS f32x2*)(L + L_MRG + (j * 4 + sw) * 512 + l * 8);
      const float e = j < 4 ? c0[j & 3] : c1[j & 3];
      acc.x = fmaf(e, ov.x, acc.x); acc.y = fmaf(e, ov.y, acc.y);
    }
    const unsigned pv = notag(cvtpk(acc.x, acc.y), tg);
    const v4u pq = {dppu<0x00>(pv), dppu<0x55>(pv), dppu<0xAA>(pv), dppu<0xFF>(pv)};
    if ((l & 3) == 0) ST_SC1(pq, WS_P + (((kvh * 4 + sw) * 8 + b) * 4 + sp) * 256 + l * 4);
    STAMP(6);
    v4u wf[4];
#pragma unroll
    for (int k = 0; k < 4; ++k) wf[k] = tile_frag(L + L_WO, l, k, sw);
    f32x4 ff = {0.f, 0.f, 0.f, 0.f};
    float gm = 1.f;
    if (ffn2) { ff = *(const LAS f32x4*)(L + L_FF + sw * 1024 + l * 16); gm = *(const LAS float*)(L + L_G + (l & 7) * 4); }
    v4u p0, p1;
    asm volatile("s_sleep %0" :: "n"(PDELAY1) : "memory");
    poll2(rws, WS_P + (oh * 8 + 2 * sw) * 1024 + l * 16, WS_P + (oh * 8 + 2 * sw) * 1024 + 1024 + l * 16, tg, p0, p1);
    *(LAS v4u*)(L + L_PG + (8 * sw + g) * 272 + (l & 15) * 16) = p0;
    *(LAS v4u*)(L + L_PG + (8 * sw + 4 + g) * 272 + (l & 15) * 16) = p1;
    STAMP(7);
    LDSWAIT();
    WGBAR();
    const int pb = l & 7, sl = (l >> 3) & 1;
    v4u pa[8];
#pragma unroll
    for (int k = 0; k < 8; ++k) pa[k] = *(const LAS v4u*)(L + L_PG + (pb * 4 + (k >> 2) * 2 + sl) * 272 + (k & 3) * 64 + g * 16);
    __builtin_amdgcn_sched_barrier(0);
    f32x4 da = {0.f, 0.f, 0.f, 0.f}, db = {0.f, 0.f, 0.f, 0.f};
#pragma unroll
    for (int k = 0; k < 4; ++k) { da = mfma32(wf[k], pa[k], da); db = mfma32(wf[k], pa[4 + k], db); }
    v4u r;
#pragma unroll
    for (int j = 0; j < 4; ++j) { const float t = da[j] + db[j]; r[j] = notag(__float_as_uint(fmaf(t + dpp<0x128>(t), gm, ff[j])), tg); }
    if (sl == 0) ST_SC1(r, WS_O + (((on * 4 + sw) * 4 + (pb >> 1)) * 32 + oh) * 128 + (pb & 1) * 64 + g * 16);
    STAMP(8);
    if (sw == 0) {
      const f32x4 fin = *(const LAS f32x4*)(L + L_FIN + (l & 3) * 16);
      const float gt = *(const LAS float*)(L + L_FIN + 64);
      const float g0 = (l & 12) == 12 ? 1.f : gt, g1 = l < 48 ? g0 : gt;
      const int oo = WS_O + (rr >> 1) * 4096 + (l >> 2) * 128 + (rr & 1) * 64 + (l & 3) * 16;
      v4u r0, r1;
      asm volatile("s_sleep %0" :: "n"(PDELAY2) : "memory");
      poll2(rws, oo, oo + 2048, tg, r0, r1);
      STAMP(9);
      f32x4 av = __builtin_bit_cast(f32x4, r0) * g0 + __builtin_bit_cast(f32x4, r1) * g1;
#pragma unroll
      for (int j = 0; j < 4; ++j) av[j] += dpp<0x124>(av[j]);
#pragma unroll
      for (int j = 0; j < 4; ++j) av[j] += dpp<0x128>(av[j]);
      {
        auto s0 = __builtin_amdgcn_permlane16_swap(__float_as_uint(av[0]), __float_as_uint(av[1]), false, false);
        auto s1 = __builtin_amdgcn_permlane16_swap(__float_as_uint(av[2]), __float_as_uint(av[3]), false, false);
        const float u0 = __uint_as_float(s0[0]) + __uint_as_float(s0[1]), u1 = __uint_as_float(s1[0]) + __uint_as_float(s1[1]);
        auto s2 = __builtin_amdgcn_permlane32_swap(__float_as_uint(u0), __float_as_uint(u1), false, false);
        const float v = __uint_as_float(s2[0]) + __uint_as_float(s2[1]);
        const float o = v + (g == 0 ? fin[0] : g == 1 ? fin[1] : g == 2 ? fin[2] : fin[3]);
        if ((l & 15) < 4) *(GAS unsigned short*)(a.out + rb * DM + rc * 16 + 4 * (l & 3) + g) = (unsigned short)cvtpk(o, o);
      }
      if (c == 0 && l == 0) *(GAS u64*)(ws + WS_E) = tag;
      STAMP(10);
    }
  }
  FLUSH();
}

extern "C" void kernel_launch(void* const* d_in, const int* in_sizes, int n_in, void* d_out, int out_size, void* d_ws, size_t ws_size,
                              hipStream_t stream) {
  Args a;
  a.x = (const unsigned short*)d_in[0];
  a.kc = (const unsigned char*)d_in[1];
  a.vc = (const unsigned char*)d_in[2];
  a.wq = (const unsigned short*)d_in[3];
  a.wo = (const unsigned short*)d_in[4];
  a.w1 = (const unsigned short*)d_in[6];
  a.b1 = (const unsigned short*)d_in[7];
  a.w2 = (const unsigned short*)d_in[8];
  a.b2 = (const unsigned short*)d_in[9];
  a.gw = (const unsigned short*)d_in[10];
  a.ws = (unsigned char*)d_ws;
  a.out = (unsigned short*)d_out;
  fused_decode<<<256, NWAVE * 64, 0, stream>>>(a);
}
```

```cpp
#define DT 480
#define WOD 35
#include <hip/hip_runtime.h>
#include <hip/hip_bf16.h>
#include <stdint.h>

typedef unsigned v4u __attribute__((ext_vector_type(4)));
typedef unsigned v2u __attribute__((ext_vector_type(2)));
typedef float f32x4 __attribute__((ext_vector_type(4)));
typedef float f32x2 __attribute__((ext_vector_type(2)));
typedef __bf16 bf16x8 __attribute__((ext_vector_type(8)));
typedef __bf16 bf16x2 __attribute__((ext_vector_type(2)));
typedef short s16x4 __attribute__((ext_vector_type(4)));
typedef unsigned long long u64;
#define GAS __attribute__((address_space(1)))
#define LAS __attribute__((address_space(3)))

constexpr int DM = 512, HKV = 8, SK = 4096, FF = 1792;
constexpr int AUX_SC1 = 16;
#define T0 100
#define WIN 5
#define TQ 560
#define TH 560
#define TF 1500
#define GQ 25
#define GH 15
#define SHIFT 1
#define PDELAY1 12
#define PDELAY2 12
#define SKEW 20
#define LATEWO 1
constexpr int NS0 = LATEWO ? 0 : 2;
#define RMAP 0
#define EAL 18
#define EAS 18
constexpr int NWAVE = 10;

constexpr int WS_E = 0;
constexpr int WS_F = 2048;
constexpr int WS_QP = 4096;
constexpr int WS_HP = WS_QP + 8 * 8 * 2 * 512 * 8;
constexpr int WS_X = WS_HP + 7 * 8 * 2 * 256 * 8;
constexpr int WS_LSE = WS_X + 256 * 128;
constexpr int WS_END = WS_LSE + 256 * 128;
constexpr int WS_O = WS_QP;
constexpr int WS_P = WS_HP;
static_assert(WS_LSE - WS_P == 32 * 8 * 4 * 256, "the partials fill exactly the lines of HP and X");
constexpr int WS_ST = 8 << 20;
constexpr int L_WO = 0, L_WT = 16384, L_MRG = 49152, L_ML = L_MRG + 16384, L_CF = L_ML + 512, L_Q = L_CF + 128, L_H = L_Q + 1024, L_FF = L_H + 4096,
              L_G = L_FF + 4096, L_FIN = L_G + 32, L_FLAG = L_FIN + 96, L_END = L_FLAG + 32;
constexpr int L_PG = L_WT;

__device__ __forceinline__ f32x4 mfma32(v4u a, v4u b, f32x4 c) {
  return __builtin_amdgcn_mfma_f32_16x16x32_bf16(__builtin_bit_cast(bf16x8, a), __builtin_bit_cast(bf16x8, b), c, 0, 0, 0);
}
__device__ __forceinline__ f32x4 mfma16(v2u a, v2u b, f32x4 c) {
  return __builtin_amdgcn_mfma_f32_16x16x16bf16_1k(__builtin_bit_cast(s16x4, a), __builtin_bit_cast(s16x4, b), c, 0, 0, 0);
}
__device__ __forceinline__ unsigned cvtpk(float lo, float hi) { bf16x2 r = {(__bf16)lo, (__bf16)hi}; return __builtin_bit_cast(unsigned, r); }
__device__ __forceinline__ float bf_lo(unsigned u) { return __builtin_bit_cast(float, u << 16); }
__device__ __forceinline__ float bf_hi(unsigned u) { return __builtin_bit_cast(float, u & 0xffff0000u); }
__device__ __forceinline__ float rbf(float x) { return (float)(__bf16)x; }
template <int CTRL> __device__ __forceinline__ float dpp(float x) { return __builtin_bit_cast(float, __builtin_amdgcn_mov_dpp(__builtin_bit_cast(int, x), CTRL, 0xf, 0xf, true)); }
template <int CTRL> __device__ __forceinline__ unsigned dppu(unsigned x) { return (unsigned)__builtin_amdgcn_mov_dpp((int)x, CTRL, 0xf, 0xf, true); }
__device__ __forceinline__ float xrow_max(float x) {
  auto s = __builtin_amdgcn_permlane16_swap(__float_as_uint(x), __float_as_uint(x), false, false);
  x = fmaxf(__uint_as_float(s[0]), __uint_as_float(s[1]));
  auto t = __builtin_amdgcn_permlane32_swap(__float_as_uint(x), __float_as_uint(x), false, false);
  return fmaxf(__uint_as_float(t[0]), __uint_as_float(t[1]));
}
__device__ __forceinline__ float xrow_sum(float x) {
  auto s = __builtin_amdgcn_permlane16_swap(__float_as_uint(x), __float_as_uint(x), false, false);
  x = __uint_as_float(s[0]) + __uint_as_float(s[1]);
  auto t = __builtin_amdgcn_permlane32_swap(__float_as_uint(x), __float_as_uint(x), false, false);
  return __uint_as_float(t[0]) + __uint_as_float(t[1]);
}
__device__ __forceinline__ v2u tr_read(const LAS unsigned char* p) { return __builtin_bit_cast(v2u, __builtin_amdgcn_ds_read_tr16_b64_v4i16((LAS s16x4*)p)); }
__device__ __forceinline__ v4u make_srd(const void* p, unsigned bytes) {
  const u64 a = (u64)p;
  return v4u{(unsigned)a, (unsigned)(a >> 32) & 0xffffu, bytes, 0x00020000u};
}
__device__ __forceinline__ void dma_rows(v4u srd, unsigned voff, unsigned soff, unsigned lds_addr) {
  unsigned keep;
  asm volatile("s_mov_b32 %0, m0\n\ts_mov_b32 m0, %1\n\ts_nop 0\n\tbuffer_load_dwordx4 %2, %3, %4 offen nt lds\n\ts_mov_b32 m0, %0"
               : "=&s"(keep) : "s"(lds_addr), "v"(voff), "s"(srd), "s"(soff) : "memory");
}
template <int ROWS> __device__ __forceinline__ void dma_tile(const void* src, unsigned ldb, unsigned lds_addr, int l) {
  const v4u srd = make_srd(src, (ROWS - 1) * ldb + 128u);
  const unsigned vo = (unsigned)(l >> 3) * ldb + (unsigned)(l & 7) * 16u;
#pragma unroll 4
  for (int i = 0; i < ROWS / 8; ++i) dma_rows(srd, vo, i * 8 * ldb, lds_addr + i * 1024);
}
__device__ __forceinline__ v4u tile_frag(const LAS unsigned char* tile, int l, int k, int ct) {
  const LAS unsigned char* p = tile + (8 * (l >> 4) + ((l & 15) >> 2)) * 128 + (l & 3) * 8 + k * 4096 + ct * 32;
  const v2u t0 = tr_read(p), t1 = tr_read(p + 512);
  return v4u{t0.x, t0.y, t1.x, t1.y};
}
#define DRAIN() asm volatile("s_waitcnt vmcnt(0)" ::: "memory")
#define LDSWAIT() asm volatile("s_waitcnt lgkmcnt(0)" ::: "memory")
#define WGBAR() do { __builtin_amdgcn_sched_barrier(0); __builtin_amdgcn_s_barrier(); __builtin_amdgcn_sched_barrier(0); asm volatile("" ::: "memory"); } while (0)
__device__ __forceinline__ u64 read_tag(const unsigned char* ws) {
  u64 t;
  asm volatile("s_load_dwordx2 %0, %1, 0x0\n\ts_waitcnt lgkmcnt(0)" : "=s"(t) : "s"(ws) : "memory");
  const unsigned old = (unsigned)t;
  t += 0x9E3779B97F4A7C15ull;
  t = (t ^ (t >> 30)) * 0xBF58476D1CE4E5B9ull; t = (t ^ (t >> 27)) * 0x94D049BB133111EBull;
  t ^= t >> 31;
  return (unsigned)t == old ? t ^ 1u : t;
}
__device__ __forceinline__ unsigned now_ticks() {
  u64 t;
  asm volatile("s_memrealtime %0\n\ts_waitcnt lgkmcnt(0)" : "=s"(t) :: "memory");
  return (unsigned)t;
}
#define WAIT_TICK(base, delta) \
  asm volatile("s_branch 2f\n1:\n\ts_sleep 1\n2:\n\ts_memrealtime vcc\n\ts_waitcnt lgkmcnt(0)\n\ts_sub_u32 vcc_lo, vcc_lo, %0\n\ts_cmpk_lt_i32 vcc_lo, %1\n\ts_cbranch_scc1 1b" \
               :: "s"(base), "n"(delta) : "scc", "vcc", "memory")
#define WAIT_NEXT(acc, step) \
  asm volatile("s_branch 2f\n1:\n\ts_sleep 1\n2:\n\ts_memrealtime vcc\n\ts_waitcnt lgkmcnt(0)\n\ts_lshl_b32 vcc_lo, vcc_lo, 4\n\ts_sub_u32 vcc_lo, vcc_lo, %0\n\ts_cmp_lt_i32 vcc_lo, 0\n\ts_cbranch_scc1 1b\n\ts_add_u32 %0, %0, %1" \
               : "+s"(acc) : "s"(step) : "scc", "vcc", "memory")
constexpr int SPIN_CAP = 1 << 18;
#define LD_SC1(off) __builtin_amdgcn_raw_buffer_load_b128(rws, (off), 0, AUX_SC1)
#define ST_SC1(v, off) __builtin_amdgcn_raw_buffer_store_b128((v), rws, (off), 0, AUX_SC1)
__device__ __forceinline__ v4u gran2(float a, float b, unsigned tg) { return v4u{__float_as_uint(a), tg, __float_as_uint(b), tg}; }
__device__ __forceinline__ unsigned gbad(v4u g, unsigned tg) { return (g.y ^ tg) | (g.w ^ tg); }
__device__ __forceinline__ f32x2 gval(v4u g) { return f32x2{__uint_as_float(g.x), __uint_as_float(g.z)}; }
__device__ __forceinline__ v4u gran_d(f32x4 d, int l, unsigned tg) { return gran2((l & 8) ? d[2] : d[0], (l & 8) ? d[3] : d[1], tg); }

#define STAMP(k) do { } while (0)
#define FLUSH() do { } while (0)

__device__ __forceinline__ void poll2(__amdgpu_buffer_rsrc_t rws, int o0, int o1, unsigned tg, v4u& r0, v4u& r1) {
  bool need = true;
#pragma nounroll
  for (int spin = 0; spin < SPIN_CAP && need; ++spin) {
    r0 = LD_SC1(o0); r1 = LD_SC1(o1);
    need = (r0.y == tg) | (r0.w == tg) | (r1.y == tg) | (r1.w == tg);
    asm volatile("" ::: "memory");
  }
}
__device__ __forceinline__ unsigned notag(unsigned x, unsigned tg) { return x == tg ? x ^ 1u : x; }
__device__ __forceinline__ float gate_of(v4u xv, v4u gv) {
  float gs = 0.f;
#pragma unroll
  for (int j = 0; j < 4; ++j) { gs += rbf(bf_lo(xv[j]) * bf_lo(gv[j])); gs += rbf(bf_hi(xv[j]) * bf_hi(gv[j])); }
  gs += dpp<0xB1>(gs); gs += dpp<0x4E>(gs); gs += dpp<0x124>(gs); gs += dpp<0x128>(gs); gs = xrow_sum(gs);
  return rbf(1.f / (1.f + __expf(-rbf(gs))));
}

struct Args {
  const unsigned short *x, *wq, *wo, *w1, *b1, *w2, *b2, *gw;
  const unsigned char *kc, *vc;
  unsigned char* ws;
  unsigned short* out;
};

__global__ void __launch_bounds__(NWAVE * 64) fused_decode(Args a) {
  __shared__ __attribute__((aligned(16))) unsigned char smem[L_END];
  LAS unsigned char* L = (LAS unsigned char*)smem;
  const unsigned lbase = (unsigned)(uintptr_t)L;
  const int tid = threadIdx.x, l = tid & 63, w = __builtin_amdgcn_readfirstlane(tid >> 6);
  const int c = blockIdx.x, xr = c >> 3, sp = xr & 3, kvh = xr >> 2, b = (c - kvh) & 7, i32 = b * 4 + sp;
  unsigned char* ws = a.ws;
  STAMP(0);
  const unsigned t_in = now_ticks();
  const __amdgpu_buffer_rsrc_t rws = __builtin_amdgcn_make_buffer_rsrc((void*)ws, 0, WS_END, 0x00020000);
  const int g = l >> 4;
  const int oh = (c & 7) * 4 + (xr >> 3), on = xr & 7;
  const int rr = RMAP ? ((c & ~15) | ((c & 7) << 1) | ((c >> 3) & 1)) : c, rb = rr & 7, rc = rr >> 3;
  const bool ffn2 = xr >= 24 && (c & 7) < 7;

  if (w == 0) {
    if (xr < 23) {
      const bool isq = xr < 16;
      const int t = isq ? ((xr >> 1) * 8 + ((c + xr) & 7)) * 2 + (xr & 1) : (c & 7) * 7 + xr - 16;
      const int cb = t >> 1, kh = t & 1;
      const unsigned ldb = isq ? 8192u : (unsigned)FF * 2u;
      dma_tile<256>((const unsigned char*)(isq ? a.wq : a.w1) + (size_t)kh * 256 * ldb + cb * 128, ldb, lbase + L_WT, l);
      v4u xa[8];
#pragma unroll
      for (int k = 0; k < 8; ++k) xa[k] = *(const GAS v4u*)(a.x + (l & 7) * DM + kh * 256 + 32 * k + 8 * g);
      const unsigned tg = (unsigned)read_tag(ws);
      DRAIN();
      STAMP(1);
      const int off = (isq ? WS_QP + (cb >> 3) * 65536 + (l & 7) * 8192 + kh * 4096 + (cb & 7) * 512 : WS_HP + (cb >> 2) * 32768 + (l & 7) * 4096 + kh * 2048 + (cb & 3) * 512)
                      + g * 32 + (l & 8) * 2;
#pragma unroll
      for (int ct = 0; ct < 4; ++ct) {
        f32x4 d = {0.f, 0.f, 0.f, 0.f};
#pragma unroll
        for (int k = 0; k < 8; ++k) d = mfma32(tile_frag(L + L_WT, l, k, ct), xa[k], d);
        __builtin_amdgcn_raw_buffer_store_b128(gran_d(d, l, tg), rws, off + ct * 128, 0, EAS);
      }
    } else if (ffn2) {
      const int fc = c & 7;
      dma_tile<256>(a.w2 + fc * 256 * DM + on * 64, DM * 2, lbase + L_WT, l);
      const unsigned bb0 = *(const GAS unsigned*)(a.b1 + fc * 256 + l * 2), bb1 = *(const GAS unsigned*)(a.b1 + fc * 256 + 128 + l * 2);
      const v4u gv = *(const GAS v4u*)(a.gw + l * 8);
      float gt = 0.f;
#pragma unroll
      for (int j = 0; j < 8; ++j) { const float t = gate_of(*(const GAS v4u*)(a.x + j * DM + l * 8), gv); gt = (l & 7) == j ? t : gt; }
      if (l < 8) *(LAS float*)(L + L_G + l * 4) = gt;
      const float og = rbf(1.f - gt);
      const u64 tag = read_tag(ws);
      const unsigned tg = (unsigned)tag;
#pragma unroll
      for (int hf = 0; hf < 2; ++hf) {
#pragma nounroll
        for (int spin = 0; spin < SPIN_CAP; ++spin) {
          v4u t[16];
#pragma unroll
          for (int i = 0; i < 16; ++i) { WAIT_TICK(t_in, TH + (hf * 16 + i) * GH); t[i] = __builtin_amdgcn_raw_buffer_load_b128(rws, WS_HP + fc * 32768 + hf * 16384 + i * 1024 + l * 16, 0, EAL); }
          unsigned bad = 0;
#pragma unroll
          for (int j = 0; j < 8; ++j) {
            const int i0 = (j >> 1) * 4 + (j & 1);
            const unsigned bb = (j & 1) ? bb1 : bb0;
            bad |= gbad(t[i0], tg) | gbad(t[i0 + 2], tg);
            const f32x2 z = gval(t[i0]) + gval(t[i0 + 2]);
            const float h0 = rbf(rbf(z.x) + bf_lo(bb)), h1 = rbf(rbf(z.y) + bf_hi(bb));
            const float r0 = h0 > 0.f ? h0 : 0.f, r1 = h1 > 0.f ? h1 : 0.f;
            *(LAS unsigned*)(L + L_H + (hf * 8 + j) * 256 + l * 4) = cvtpk(r0 * r0, r1 * r1);
          }
          if (__builtin_amdgcn_ballot_w64(bad != 0) == 0) break;
          asm volatile("s_sleep 16" ::: "memory");
        }
      }
      STAMP(1);
      DRAIN();
      LDSWAIT();
      if (l == 0) *(volatile LAS u64*)(L + L_FLAG + 8) = tag;
      v4u hb[8];
#pragma unroll
      for (int k = 0; k < 8; ++k) hb[k] = *(const LAS v4u*)(L + L_H + (l & 7) * 512 + k * 64 + g * 16);
#pragma unroll
      for (int ct = 0; ct < 4; ++ct) {
        f32x4 d = {0.f, 0.f, 0.f, 0.f};
#pragma unroll
        for (int k = 0; k < 8; ++k) d = mfma32(tile_frag(L + L_WT, l, k, ct), hb[k], d);
        *(LAS f32x4*)(L + L_FF + ct * 1024 + l * 16) = d * og;
      }
      LDSWAIT();
    }
    STAMP(2);
    FLUSH();
    return;
  }
  if (w == 1) {
    const v4u xv = *(const GAS v4u*)(a.x + rb * DM + l * 8), gv = *(const GAS v4u*)(a.gw + l * 8);
    const v2u bb = *(const GAS v2u*)(a.b2 + rc * 16 + 4 * (l & 3));
    const u64 tag = read_tag(ws);
    const unsigned tg = (unsigned)tag, th = (unsigned)(tag >> 32);
    if (l < 8) __builtin_amdgcn_raw_buffer_store_b128(v4u{t_in, tg, t_in, tg}, rws, WS_X + c * 128 + l * 16, 0, EAS);
    {
      const float gt = gate_of(xv, gv), og = rbf(1.f - gt);
      if (l < 4) *(LAS f32x4*)(L + L_FIN + l * 16) = f32x4{bf_lo(bb.x), bf_hi(bb.x), bf_lo(bb.y), bf_hi(bb.y)} * og;
      if (l == 0) *(LAS float*)(L + L_FIN + 64) = gt;
    }
    WAIT_TICK(t_in, TQ - GQ);
    const v2u st = __builtin_amdgcn_raw_buffer_load_b64(rws, WS_X + (l & 7) * 128, 0, AUX_SC1);
#pragma nounroll
    for (int spin = 0; spin < SPIN_CAP; ++spin) {
      v4u t[8];
#pragma unroll
      for (int i = 0; i < 8; ++i) { WAIT_TICK(t_in, TQ + i * GQ); t[i] = __builtin_amdgcn_raw_buffer_load_b128(rws, WS_QP + kvh * 65536 + b * 8192 + i * 1024 + l * 16, 0, EAL); }
      unsigned bad = 0;
#pragma unroll
      for (int j = 0; j < 4; ++j) {
        bad |= gbad(t[j], tg) | gbad(t[4 + j], tg);
        const f32x2 q = gval(t[j]) + gval(t[4 + j]);
        *(LAS unsigned*)(L + L_Q + j * 256 + l * 4) = cvtpk(q.x, q.y);
      }
      if (__builtin_amdgcn_ballot_w64(bad != 0) == 0) break;
      asm volatile("s_sleep 8" ::: "memory");
    }
    unsigned dtv = DT;
    {
      int late = st.y == tg ? (int)(t_in - st.x) : 0;
      late = max(late, (int)dppu<0xB1>((unsigned)late)); late = max(late, (int)dppu<0x4E>((unsigned)late)); late = max(late, (int)dppu<0x141>((unsigned)late));
      late = min(late, 100);
      if (SKEW >= 0) dtv = __builtin_amdgcn_readfirstlane(DT - (late - SKEW) / 2);
      if (l == 0) *(LAS unsigned*)(L + L_FLAG + 16) = dtv;
    }
    LDSWAIT();
    if (l == 0) *(volatile LAS u64*)(L + L_FLAG) = tag;
    STAMP(3);
    if (ffn2) {
#pragma nounroll
      for (int spin = 0; spin < SPIN_CAP * 16; ++spin) { if (*(volatile const LAS u64*)(L + L_FLAG + 8) == tag) break; asm volatile("s_sleep 8" ::: "memory"); }
    }
    DRAIN();
    if (l == 0) __builtin_amdgcn_raw_buffer_store_b64(v2u{tg, th}, rws, WS_F + c * 8, 0, AUX_SC1);
    STAMP(5);
    v4u ml;
#pragma nounroll
    for (int spin = 0; spin < SPIN_CAP * 16; ++spin) {
      ml = *(volatile const LAS v4u*)(L + L_ML + (l & 31) * 16);
      if (__builtin_amdgcn_ballot_w64(((ml.z ^ tg) | (ml.w ^ th)) != 0) == 0) break;
      asm volatile("s_sleep 4" ::: "memory");
    }
    const float mw = __uint_as_float(ml.x);
    float M = fmaxf(mw, dpp<0xB1>(mw)); M = fmaxf(M, dpp<0x4E>(M)); M = fmaxf(M, dpp<0x141>(M));
    float Ls = __builtin_amdgcn_exp2f(mw - M) * __uint_as_float(ml.y);
    Ls += dpp<0xB1>(Ls); Ls += dpp<0x4E>(Ls); Ls += dpp<0x141>(Ls);
    const float lse = M + __builtin_amdgcn_logf(Ls);
    const v4u lg = gran2(lse, dpp<0x128>(lse), tg);
    if (l < 32 && (l & 15) == 0) ST_SC1(lg, WS_LSE + ((b * 8 + kvh) * 4 + sp) * 128 + l);
    STAMP(6);
    v2u ls;
    asm volatile("s_sleep 16" ::: "memory");
#pragma nounroll
    for (int spin = 0; spin < SPIN_CAP; ++spin) {
      ls = __builtin_amdgcn_raw_buffer_load_b64(rws, WS_LSE + ((b * 8 + kvh) * 4 + (l & 3)) * 128 + ((l >> 3) & 3) * 8, 0, AUX_SC1);
      if (__builtin_amdgcn_ballot_w64(ls.y != tg) == 0) break;
      asm volatile("s_sleep 8" ::: "memory");
    }
    const float lf = __uint_as_float(ls.x);
    float M2 = fmaxf(lf, dpp<0xB1>(lf)); M2 = fmaxf(M2, dpp<0x4E>(M2));
    float E = __builtin_amdgcn_exp2f(lf - M2);
    E += dpp<0xB1>(E); E += dpp<0x4E>(E);
    if (l < 32) *(LAS float*)(L + L_CF + l * 4) = __builtin_amdgcn_exp2f(mw - M2) * __builtin_amdgcn_rcpf(E);
    WAIT_TICK(t_in, TF);
#pragma nounroll
    for (int spin = 0; spin < SPIN_CAP; ++spin) {
      const v4u f0 = LD_SC1(WS_F + l * 16), f1 = LD_SC1(WS_F + 1024 + l * 16);
      if (__builtin_amdgcn_ballot_w64(((f0.x ^ tg) | (f0.y ^ th) | (f0.z ^ tg) | (f0.w ^ th) | (f1.x ^ tg) | (f1.y ^ th) | (f1.z ^ tg) | (f1.w ^ th)) != 0) == 0) break;
      asm volatile("s_sleep 16" ::: "memory");
    }
    LDSWAIT();
    STAMP(7);
    if (LATEWO) {
      unsigned tw = ((t_in + T0 + WOD) << 4) + 32 * DT + 32 * dtv;
      WAIT_NEXT(tw, dtv);
      dma_tile<128>(a.wo + oh * 128 * DM + on * 64, DM * 2, lbase + L_WO, l);
      WGBAR();
      DRAIN();
      if (l == 0) *(volatile LAS u64*)(L + L_FLAG + 24) = tag;
      LDSWAIT();
      STAMP(8);
    }
    FLUSH();
    return;
  }

  const int sw = w - 2;
  unsigned tnext = ((t_in + T0) << 4) + sw * (DT / 8), dt = DT;
#define PACE_TO(j) do { asm volatile("s_waitcnt vmcnt(%0)" :: "n"(WIN - 1) : "memory"); WAIT_NEXT(tnext, dt); } while (0)
  const size_t base = ((size_t)(b * HKV + kvh) * SK + sp * 1024 + sw * 128) * 256;
  const v4u rk = make_srd(a.kc + base, 32768), rv = make_srd(a.vc + base, 32768);
  const int ko = ((l >> 2) & 3) * 256 + g * 64 + (l & 3) * 16, vo = l * 16;
  const int rot = (4 * sw + xr) & 31;
#define OFF(i) ((((i) + rot) & 31) * 1024)
#define LOAD_V(i) asm volatile("buffer_load_dwordx4 %0, %1, %2, %3 offen nt" : "+v"(kv[i]) : "v"(vo), "s"(rv), "s"(OFF(i)) : "memory")
  if (!LATEWO) {
    const v4u ro = make_srd(a.wo + (oh * 128 + sw * 16) * DM + on * 64, 15 * 1024 + 128);
    const unsigned oo = (unsigned)(l >> 3) * 1024u + (unsigned)(l & 7) * 16u;
    PACE_TO(0); dma_rows(ro, oo, 0, lbase + L_WO + sw * 2048);
    PACE_TO(1); dma_rows(ro, oo, 8192, lbase + L_WO + sw * 2048 + 1024);
  }
  v4u kv[32];
  __builtin_amdgcn_sched_barrier(0);
#pragma unroll
  for (int i = 0; i < 32; ++i) {
    PACE_TO(2 + i);
    asm volatile("buffer_load_dwordx4 %0, %1, %2, %3 offen nt" : "=v"(kv[i]) : "v"(ko), "s"(rk), "s"(OFF(i)) : "memory");
    __builtin_amdgcn_sched_barrier(0);
  }
  STAMP(1);
  const u64 tag = read_tag(ws);
  const unsigned tg = (unsigned)tag;
#pragma nounroll
  for (int spin = 0; spin < SPIN_CAP * 16; ++spin) { if (*(volatile const LAS u64*)(L + L_FLAG) == tag) break; asm volatile("s_sleep 2" ::: "memory"); }
  asm volatile("" ::: "memory");
  STAMP(2);
  const v4u qf = *(const LAS v4u*)(L + L_Q + ((l >> 2) & 3) * 256 + g * 64 + (l & 3) * 16);
  dt = __builtin_amdgcn_readfirstlane(*(const LAS unsigned*)(L + L_FLAG + 16));
  __builtin_amdgcn_sched_barrier(0);
  constexpr float C2 = 0.088388347648318440f * 1.4426950408889634f;
  float sc[8];
  const int s = l & 3;
#pragma unroll
  for (int m = 0; m < 8; ++m) {
    asm volatile("s_waitcnt vmcnt(%4)" : "+v"(kv[4 * m]), "+v"(kv[4 * m + 1]), "+v"(kv[4 * m + 2]), "+v"(kv[4 * m + 3]) : "n"(28 - 4 * m + m) : "memory");
    f32x4 d[4];
#pragma unroll
    for (int r = 0; r < 4; ++r) d[r] = mfma32(kv[4 * m + r], qf, f32x4{0.f, 0.f, 0.f, 0.f});
#pragma unroll
    for (int r = 0; r < 4; ++r) {
      float t = dpp<0x00>(d[r][0]); t = dpp<0x55>(d[r][1]) + t; t = dpp<0xAA>(d[r][2]) + t; t = dpp<0xFF>(d[r][3]) + t;
      asm volatile("" : "+v"(t));
      sc[m] = (r == 0 || s == r) ? t : sc[m];
    }
    asm volatile("" : "+v"(sc[m]));
    __builtin_amdgcn_sched_barrier(0);
    PACE_TO(34 + m);
    LOAD_V(m);
    __builtin_amdgcn_sched_barrier(0);
  }
  STAMP(3);
  float mx = sc[0];
#pragma unroll
  for (int m = 1; m < 8; ++m) mx = fmaxf(mx, sc[m]);
  mx = fmaxf(mx, dpp<0xB1>(mx)); mx = fmaxf(mx, dpp<0x4E>(mx)); mx = xrow_max(mx);
  const float mneg = -mx * C2;
  float lsum = 0.f;
  unsigned pk[8];
#pragma unroll
  for (int m = 0; m < 8; ++m) { const float p = __builtin_amdgcn_exp2f(fmaf(sc[m], C2, mneg)); pk[m] = cvtpk(p, p); lsum += bf_lo(pk[m]); }
  lsum += dpp<0xB1>(lsum); lsum += dpp<0x4E>(lsum); lsum = xrow_sum(lsum);
  if (l < 16 && (l & 3) == 0) *(LAS v4u*)(L + L_ML + ((l >> 2) * 8 + sw) * 16) = v4u{__float_as_uint(mx * C2), __float_as_uint(lsum), tg, (unsigned)(tag >> 32)};
  const unsigned half = (l & 1) ? 0xffff0000u : 0x0000ffffu;
  const unsigned m0 = (l & 2) ? 0u : half, m1 = (l & 2) ? half : 0u;
  f32x4 o0 = {0.f, 0.f, 0.f, 0.f}, o1 = {0.f, 0.f, 0.f, 0.f};
#pragma unroll
  for (int j = 8; j < 40; ++j) {
    if (j < 32) { PACE_TO(34 + j); LOAD_V(j); __builtin_amdgcn_sched_barrier(0); }
    if (j == 31) STAMP(11);
    const int i = j - 8;
    const unsigned pb = (i & 3) == 0 ? dppu<0x00>(pk[i >> 2]) : (i & 3) == 1 ? dppu<0x55>(pk[i >> 2]) : (i & 3) == 2 ? dppu<0xAA>(pk[i >> 2]) : dppu<0xFF>(pk[i >> 2]);
    const v2u av = {pb & m0, pb & m1};
    asm volatile("s_waitcnt vmcnt(%1)" : "+v"(kv[i]) : "n"((j < 32 ? j : 31) - i) : "memory");
    o0 = mfma16(av, v2u{kv[i].x, kv[i].y}, o0);
    o1 = mfma16(av, v2u{kv[i].z, kv[i].w}, o1);
    __builtin_amdgcn_sched_barrier(0);
  }
  *(LAS f32x4*)(L + L_MRG + (sw * 4 + g) * 512 + (l & 15) * 32) = o0;
  *(LAS f32x4*)(L + L_MRG + (sw * 4 + g) * 512 + (l & 15) * 32 + 16) = o1;
  STAMP(4);
  LDSWAIT();
  WGBAR();
  if (sw < 4) {
    STAMP(5);
    const f32x4 c0 = *(const LAS f32x4*)(L + L_CF + sw * 32), c1 = *(const LAS f32x4*)(L + L_CF + sw * 32 + 16);
    f32x2 acc = {0.f, 0.f};
#pragma unroll
    for (int j = 0; j < 8; ++j) {
      const f32x2 ov = *(const LAS f32x2*)(L + L_MRG + (j * 4 + sw) * 512 + l * 8);
      const float e = j < 4 ? c0[j & 3] : c1[j & 3];
      acc.x = fmaf(e, ov.x, acc.x); acc.y = fmaf(e, ov.y, acc.y);
    }
    const unsigned pv = notag(cvtpk(acc.x, acc.y), tg);
    const v4u pq = {dppu<0x00>(pv), dppu<0x55>(pv), dppu<0xAA>(pv), dppu<0xFF>(pv)};
    if ((l & 3) == 0) ST_SC1(pq, WS_P + (((kvh * 4 + sw) * 8 + b) * 4 + sp) * 256 + l * 4);
    STAMP(6);
    f32x4 ff = {0.f, 0.f, 0.f, 0.f};
    float gm = 1.f;
    if (ffn2) { ff = *(const LAS f32x4*)(L + L_FF + sw * 1024 + l * 16); gm = *(const LAS float*)(L + L_G + (l & 7) * 4); }
    const int po = WS_P + (oh * 8 + 2 * sw) * 1024 + l * 16;
    asm volatile("s_sleep %0" :: "n"(PDELAY1) : "memory");
    v4u p0 = LD_SC1(po), p1 = LD_SC1(po + 1024);
    if (LATEWO) {
#pragma nounroll
      for (int spin = 0; spin < SPIN_CAP * 16; ++spin) { if (*(volatile const LAS u64*)(L + L_FLAG + 24) == tag) break; }
      asm volatile("" ::: "memory");
    }
    v4u wf[4];
#pragma unroll
    for (int k = 0; k < 4; ++k) wf[k] = tile_frag(L + L_WO, l, k, sw);
    __builtin_amdgcn_sched_barrier(0);
    if ((p0.y == tg) | (p0.w == tg) | (p1.y == tg) | (p1.w == tg)) poll2(rws, po, po + 1024, tg, p0, p1);
    *(LAS v4u*)(L + L_PG + (8 * sw + g) * 272 + (l & 15) * 16) = p0;
    *(LAS v4u*)(L + L_PG + (8 * sw + 4 + g) * 272 + (l & 15) * 16) = p1;
    STAMP(7);
    LDSWAIT();
    WGBAR();
    const int pb = l & 7, sl = (l >> 3) & 1;
    v4u pa[8];
#pragma unroll
    for (int k = 0; k < 8; ++k) pa[k] = *(const LAS v4u*)(L + L_PG + (pb * 4 + (k >> 2) * 2 + sl) * 272 + (k & 3) * 64 + g * 16);
    __builtin_amdgcn_sched_barrier(0);
    f32x4 da = {0.f, 0.f, 0.f, 0.f}, db = {0.f, 0.f, 0.f, 0.f};
#pragma unroll
    for (int k = 0; k < 4; ++k) { da = mfma32(wf[k], pa[k], da); db = mfma32(wf[k], pa[4 + k], db); }
    v4u r;
#pragma unroll
    for (int j = 0; j < 4; ++j) { const float t = da[j] + db[j]; r[j] = notag(__float_as_uint(fmaf(t + dpp<0x128>(t), gm, ff[j])), tg); }
    if (sl == 0) ST_SC1(r, WS_O + (((on * 4 + sw) * 4 + (pb >> 1)) * 32 + oh) * 128 + (pb & 1) * 64 + g * 16);
    STAMP(8);
    if (sw == 0) {
      const f32x4 fin = *(const LAS f32x4*)(L + L_FIN + (l & 3) * 16);
      const float gt = *(const LAS float*)(L + L_FIN + 64);
      const float g0 = (l & 12) == 12 ? 1.f : gt, g1 = l < 48 ? g0 : gt;
      const int oo = WS_O + (rr >> 1) * 4096 + (l >> 2) * 128 + (rr & 1) * 64 + (l & 3) * 16;
      v4u r0, r1;
      asm volatile("s_sleep %0" :: "n"(PDELAY2) : "memory");
      poll2(rws, oo, oo + 2048, tg, r0, r1);
      STAMP(9);
      f32x4 av = __builtin_bit_cast(f32x4, r0) * g0 + __builtin_bit_cast(f32x4, r1) * g1;
#pragma unroll
      for (int j = 0; j < 4; ++j) av[j] += dpp<0x124>(av[j]);
#pragma unroll
      for (int j = 0; j < 4; ++j) av[j] += dpp<0x128>(av[j]);
      {
        auto s0 = __builtin_amdgcn_permlane16_swap(__float_as_uint(av[0]), __float_as_uint(av[1]), false, false);
        auto s1 = __builtin_amdgcn_permlane16_swap(__float_as_uint(av[2]), __float_as_uint(av[3]), false, false);
        const float u0 = __uint_as_float(s0[0]) + __uint_as_float(s0[1]), u1 = __uint_as_float(s1[0]) + __uint_as_float(s1[1]);
        auto s2 = __builtin_amdgcn_permlane32_swap(__float_as_uint(u0), __float_as_uint(u1), false, false);
        const float v = __uint_as_float(s2[0]) + __uint_as_float(s2[1]);
        const float o = v + (g == 0 ? fin[0] : g == 1 ? fin[1] : g == 2 ? fin[2] : fin[3]);
        if ((l & 15) < 4) *(GAS unsigned short*)(a.out + rb * DM + rc * 16 + 4 * (l & 3) + g) = (unsigned short)cvtpk(o, o);
      }
      if (c == 0 && l == 0) *(GAS u64*)(ws + WS_E) = tag;
      STAMP(10);
    }
  }
  FLUSH();
}

extern "C" void kernel_launch(void* const* d_in, const int* in_sizes, int n_in, void* d_out, int out_size, void* d_ws, size_t ws_size,
                              hipStream_t stream) {
  Args a;
  a.x = (const unsigned short*)d_in[0];
  a.kc = (const unsigned char*)d_in[1];
  a.vc = (const unsigned char*)d_in[2];
  a.wq = (const unsigned short*)d_in[3];
  a.wo = (const unsigned short*)d_in[4];
  a.w1 = (const unsigned short*)d_in[6];
  a.b1 = (const unsigned short*)d_in[7];
  a.w2 = (const unsigned short*)d_in[8];
  a.b2 = (const unsigned short*)d_in[9];
  a.gw = (const unsigned short*)d_in[10];
  a.ws = (unsigned char*)d_ws;
  a.out = (unsigned short*)d_out;
  fused_decode<<<256, NWAVE * 64, 0, stream>>>(a);
}
```

```cpp
#define DT 480
#define T0 130
#include <hip/hip_runtime.h>
#include <hip/hip_bf16.h>
#include <stdint.h>

typedef unsigned v4u __attribute__((ext_vector_type(4)));
typedef unsigned v2u __attribute__((ext_vector_type(2)));
typedef float f32x4 __attribute__((ext_vector_type(4)));
typedef float f32x2 __attribute__((ext_vector_type(2)));
typedef __bf16 bf16x8 __attribute__((ext_vector_type(8)));
typedef __bf16 bf16x2 __attribute__((ext_vector_type(2)));
typedef short s16x4 __attribute__((ext_vector_type(4)));
typedef unsigned long long u64;
#define GAS __attribute__((address_space(1)))
#define LAS __attribute__((address_space(3)))

constexpr int DM = 512, HKV = 8, SK = 4096, FF = 1792;
constexpr int AUX_SC1 = 16;
#define WIN 5
#define TQ 560
#define TH 480
#define TF 1500
#define GQ 6
#define GH 3
#define SHIFT 1
#define PDELAY1 12
#define PDELAY2 12
#define SKEW 20
#define LATEWO 1
#define WOD 35
constexpr int NS0 = LATEWO ? 0 : 2;
#define RMAP 0
#define EAL 18
#define EAS 18
#define CODEKB 16
#define CODEB 15360
constexpr int NWAVE = 10;

constexpr int WS_E = 0;
constexpr int WS_F = 2048;
constexpr int WS_QP = 4096;
constexpr int WS_HP = WS_QP + 8 * 8 * 2 * 512 * 8;
constexpr int WS_X = WS_HP + 7 * 8 * 2 * 256 * 8;
constexpr int WS_LSE = WS_X + 256 * 128;
constexpr int WS_END = WS_LSE + 256 * 128;
constexpr int WS_O = WS_QP;
constexpr int WS_P = WS_HP;
static_assert(WS_LSE - WS_P == 32 * 8 * 4 * 256, "the partials fill exactly the lines of HP and X");
constexpr int WS_ST = 8 << 20;
constexpr int L_WO = 0, L_WT = 16384, L_MRG = 49152, L_ML = L_MRG + 16384, L_CF = L_ML + 512, L_Q = L_CF + 128, L_X = L_Q + 1024, L_H = L_X + 8192, L_NIL = L_H + 8192, L_FF = L_NIL + 1024,
              L_G = L_FF + 4096, L_FIN = L_G + 32, L_FLAG = L_FIN + 96, L_END = L_FLAG + 32;
constexpr int L_PG = L_WT;

__device__ __forceinline__ f32x4 mfma32(v4u a, v4u b, f32x4 c) {
  return __builtin_amdgcn_mfma_f32_16x16x32_bf16(__builtin_bit_cast(bf16x8, a), __builtin_bit_cast(bf16x8, b), c, 0, 0, 0);
}
__device__ __forceinline__ f32x4 mfma16(v2u a, v2u b, f32x4 c) {
  return __builtin_amdgcn_mfma_f32_16x16x16bf16_1k(__builtin_bit_cast(s16x4, a), __builtin_bit_cast(s16x4, b), c, 0, 0, 0);
}
__device__ __forceinline__ unsigned cvtpk(float lo, float hi) { bf16x2 r = {(__bf16)lo, (__bf16)hi}; return __builtin_bit_cast(unsigned, r); }
__device__ __forceinline__ float bf_lo(unsigned u) { return __builtin_bit_cast(float, u << 16); }
__device__ __forceinline__ float bf_hi(unsigned u) { return __builtin_bit_cast(float, u & 0xffff0000u); }
__device__ __forceinline__ float rbf(float x) { return (float)(__bf16)x; }
template <int CTRL> __device__ __forceinline__ float dpp(float x) { return __builtin_bit_cast(float, __builtin_amdgcn_mov_dpp(__builtin_bit_cast(int, x), CTRL, 0xf, 0xf, true)); }
template <int CTRL> __device__ __forceinline__ unsigned dppu(unsigned x) { return (unsigned)__builtin_amdgcn_mov_dpp((int)x, CTRL, 0xf, 0xf, true); }
__device__ __forceinline__ float xrow_max(float x) {
  auto s = __builtin_amdgcn_permlane16_swap(__float_as_uint(x), __float_as_uint(x), false, false);
  x = fmaxf(__uint_as_float(s[0]), __uint_as_float(s[1]));
  auto t = __builtin_amdgcn_permlane32_swap(__float_as_uint(x), __float_as_uint(x), false, false);
  return fmaxf(__uint_as_float(t[0]), __uint_as_float(t[1]));
}
__device__ __forceinline__ float xrow_sum(float x) {
  auto s = __builtin_amdgcn_permlane16_swap(__float_as_uint(x), __float_as_uint(x), false, false);
  x = __uint_as_float(s[0]) + __uint_as_float(s[1]);
  auto t = __builtin_amdgcn_permlane32_swap(__float_as_uint(x), __float_as_uint(x), false, false);
  return __uint_as_float(t[0]) + __uint_as_float(t[1]);
}
__device__ __forceinline__ v2u tr_read(const LAS unsigned char* p) { return __builtin_bit_cast(v2u, __builtin_amdgcn_ds_read_tr16_b64_v4i16((LAS s16x4*)p)); }
__device__ __forceinline__ v4u make_srd(const void* p, unsigned bytes) {
  const u64 a = (u64)p;
  return v4u{(unsigned)a, (unsigned)(a >> 32) & 0xffffu, bytes, 0x00020000u};
}
__device__ __forceinline__ void dma_rows(v4u srd, unsigned voff, unsigned soff, unsigned lds_addr) {
  unsigned keep;
  asm volatile("s_mov_b32 %0, m0\n\ts_mov_b32 m0, %1\n\ts_nop 0\n\tbuffer_load_dwordx4 %2, %3, %4 offen nt lds\n\ts_mov_b32 m0, %0"
               : "=&s"(keep) : "s"(lds_addr), "v"(voff), "s"(srd), "s"(soff) : "memory");
}
#define DMA_LOOP(POL, srd, voff, step, lds_addr, n) do { unsigned keep_, so_ = 0, n_ = (n); \
  asm volatile("s_cmp_eq_u32 %2, 0\n\ts_cbranch_scc1 2f\n\ts_mov_b32 %0, m0\n\ts_mov_b32 m0, %3\n\ts_nop 0\n1:\n\tbuffer_load_dwordx4 %4, %5, %1 offen " POL " lds\n\t" \
               "s_add_u32 m0, m0, 0x400\n\ts_add_u32 %1, %1, %6\n\ts_sub_u32 %2, %2, 1\n\ts_cmp_lg_u32 %2, 0\n\ts_cbranch_scc1 1b\n\ts_mov_b32 m0, %0\n2:" \
               : "=&s"(keep_), "+&s"(so_), "+&s"(n_) : "s"(lds_addr), "v"(voff), "s"(srd), "s"(step) : "scc", "memory"); } while (0)
__device__ __forceinline__ v4u tile_frag(const LAS unsigned char* tile, int l, int k, int ct) {
  const LAS unsigned char* p = tile + (8 * (l >> 4) + ((l & 15) >> 2)) * 128 + (l & 3) * 8 + k * 4096 + ct * 32;
  const v2u t0 = tr_read(p), t1 = tr_read(p + 512);
  return v4u{t0.x, t0.y, t1.x, t1.y};
}
#define DRAIN() asm volatile("s_waitcnt vmcnt(0)" ::: "memory")
#define LDSWAIT() asm volatile("s_waitcnt lgkmcnt(0)" ::: "memory")
#define WGBAR() do { __builtin_amdgcn_sched_barrier(0); __builtin_amdgcn_s_barrier(); __builtin_amdgcn_sched_barrier(0); asm volatile("" ::: "memory"); } while (0)
__device__ __forceinline__ u64 read_tag(const unsigned char* ws) {
  u64 t;
  asm volatile("s_load_dwordx2 %0, %1, 0x0\n\ts_waitcnt lgkmcnt(0)" : "=s"(t) : "s"(ws) : "memory");
  const unsigned old = (unsigned)t;
  t += 0x9E3779B97F4A7C15ull;
  t = (t ^ (t >> 30)) * 0xBF58476D1CE4E5B9ull; t = (t ^ (t >> 27)) * 0x94D049BB133111EBull;
  t ^= t >> 31;
  return (unsigned)t == old ? t ^ 1u : t;
}
__device__ __forceinline__ unsigned now_ticks() {
  u64 t;
  asm volatile("s_memrealtime %0\n\ts_waitcnt lgkmcnt(0)" : "=s"(t) :: "memory");
  return (unsigned)t;
}
#define WAIT_TICK(base, delta) \
  asm volatile("s_branch 2f\n1:\n\ts_sleep 1\n2:\n\ts_memrealtime vcc\n\ts_waitcnt lgkmcnt(0)\n\ts_sub_u32 vcc_lo, vcc_lo, %0\n\ts_cmpk_lt_i32 vcc_lo, %1\n\ts_cbranch_scc1 1b" \
               :: "s"(base), "n"(delta) : "scc", "vcc", "memory")
#define WAIT_NEXT(acc, step) \
  asm volatile("s_branch 2f\n1:\n\ts_sleep 1\n2:\n\ts_memrealtime vcc\n\ts_waitcnt lgkmcnt(0)\n\ts_lshl_b32 vcc_lo, vcc_lo, 4\n\ts_sub_u32 vcc_lo, vcc_lo, %0\n\ts_cmp_lt_i32 vcc_lo, 0\n\ts_cbranch_scc1 1b\n\ts_add_u32 %0, %0, %1" \
               : "+s"(acc) : "s"(step) : "scc", "vcc", "memory")
constexpr int SPIN_CAP = 1 << 18;
#define LD_SC1(off) __builtin_amdgcn_raw_buffer_load_b128(rws, (off), 0, AUX_SC1)
#define ST_SC1(v, off) __builtin_amdgcn_raw_buffer_store_b128((v), rws, (off), 0, AUX_SC1)
__device__ __forceinline__ v4u gran2(float a, float b, unsigned tg) { return v4u{__float_as_uint(a), tg, __float_as_uint(b), tg}; }
__device__ __forceinline__ unsigned gbad(v4u g, unsigned tg) { return (g.y ^ tg) | (g.w ^ tg); }
__device__ __forceinline__ f32x2 gval(v4u g) { return f32x2{__uint_as_float(g.x), __uint_as_float(g.z)}; }
__device__ __forceinline__ v4u gran_d(f32x4 d, int l, unsigned tg) { return gran2((l & 8) ? d[2] : d[0], (l & 8) ? d[3] : d[1], tg); }

#define STAMP(k) do { } while (0)
#define FLUSH() do { } while (0)

#define PSTAMP(k) do { } while (0)
__device__ __forceinline__ void poll2(__amdgpu_buffer_rsrc_t rws, int o0, int o1, unsigned tg, v4u& r0, v4u& r1) {
  bool need = true;
#pragma nounroll
  for (int spin = 0; spin < SPIN_CAP && need; ++spin) {
    r0 = LD_SC1(o0); r1 = LD_SC1(o1);
    need = (r0.y == tg) | (r0.w == tg) | (r1.y == tg) | (r1.w == tg);
    asm volatile("" ::: "memory");
  }
}
__device__ __forceinline__ unsigned notag(unsigned x, unsigned tg) { return x == tg ? x ^ 1u : x; }
__device__ __forceinline__ float gate_of(v4u xv, v4u gv) {
  float gs = 0.f;
#pragma unroll
  for (int j = 0; j < 4; ++j) { gs += rbf(bf_lo(xv[j]) * bf_lo(gv[j])); gs += rbf(bf_hi(xv[j]) * bf_hi(gv[j])); }
  gs += dpp<0xB1>(gs); gs += dpp<0x4E>(gs); gs += dpp<0x124>(gs); gs += dpp<0x128>(gs); gs = xrow_sum(gs);
  return rbf(1.f / (1.f + __expf(-rbf(gs))));
}

struct Args {
  const unsigned short *x, *wq, *wo, *w1, *b1, *w2, *b2, *gw;
  const unsigned char *kc, *vc;
  unsigned char* ws;
  unsigned short* out;
};

typedef unsigned v16u __attribute__((ext_vector_type(16)));
typedef unsigned v8u __attribute__((ext_vector_type(8)));

__global__ void __launch_bounds__(NWAVE * 64) fused_decode(Args a_) {
  v16u ka0; v8u ka1; u64 t_in64;
  asm volatile("s_load_dwordx16 %0, %3, 0x0\n\ts_load_dwordx8 %1, %3, 0x40\n\ts_memrealtime %2" : "=&s"(ka0), "=&s"(ka1), "=&s"(t_in64) : "s"(__builtin_amdgcn_kernarg_segment_ptr()));
  __shared__ __attribute__((aligned(16))) unsigned char smem[L_END + 64];
  LAS unsigned char* L = (LAS unsigned char*)smem;
  const unsigned lbase = (unsigned)(uintptr_t)L;
  const int tid = threadIdx.x, l = tid & 63, w = __builtin_amdgcn_readfirstlane(tid >> 6);
  const int c = blockIdx.x, xr = c >> 3, sp = xr & 3, kvh = xr >> 2, b = (c - kvh) & 7, i32 = b * 4 + sp;
  {
    u64 pc;
    asm volatile("s_getpc_b64 %0" : "=s"(pc));
    const v4u rc = make_srd((const void*)(pc & ~255ull), CODEB);
    DMA_LOOP("", rc, ((xr * 2 + w) & (CODEKB - 1)) * 1024 + l * 16, 0, lbase + L_NIL, (12u >> w) & 1u);
  }
  const int g = l >> 4;
  const int oh = (c & 7) * 4 + (xr >> 3), on = xr & 7;
  const bool ffn2 = xr >= 24 && (c & 7) < 7;
  const bool isq = xr < 16, side = xr < 23 || ffn2;
  const int tl = isq ? ((xr >> 1) * 8 + ((c + xr) & 7)) * 2 + (xr & 1) : (c & 7) * 7 + xr - 16;
  const int cb = ffn2 ? on : tl >> 1, kh = ffn2 ? c & 7 : tl & 1;
#define KP(v, i) ((u64)v[i] | ((u64)v[(i) + 1] << 32))
  if (__builtin_expect(w == 0, 1)) {
    const unsigned ldb = isq ? 8192u : ffn2 ? 1024u : (unsigned)FF * 2u;
    unsigned woff = (unsigned)kh * 256u * ldb + cb * 128, wlen = 255u * ldb + 128u, wstep = 8u * ldb, wvo = (unsigned)(l >> 3) * ldb + (unsigned)(l & 7) * 16u;
    unsigned nw = side ? 32 : 0;
    asm volatile("s_waitcnt lgkmcnt(0)" : "+s"(ka0), "+s"(woff), "+s"(wlen), "+s"(wstep), "+s"(nw), "+v"(wvo) :: "memory");
    u64 pq = KP(ka0, 2), p1 = KP(ka0, 6), p2 = KP(ka0, 10);
    asm volatile("" : "+s"(pq), "+s"(p1), "+s"(p2));
    DMA_LOOP("nt", make_srd((const unsigned char*)(isq ? pq : ffn2 ? p2 : p1) + woff, wlen), wvo, wstep, lbase + L_WT, nw);
    DMA_LOOP("", make_srd((const void*)KP(ka0, 0), 8192), l * 16, 1024, lbase + L_X, nw >> 2);
  }
  asm volatile("s_waitcnt lgkmcnt(0)" : "+s"(t_in64), "+s"(ka0), "+s"(ka1) :: "memory");
  const unsigned t_in = (unsigned)t_in64;
  Args a;
  a.x = (const unsigned short*)KP(ka0, 0); a.wq = (const unsigned short*)KP(ka0, 2); a.wo = (const unsigned short*)KP(ka0, 4); a.w1 = (const unsigned short*)KP(ka0, 6);
  a.b1 = (const unsigned short*)KP(ka0, 8); a.w2 = (const unsigned short*)KP(ka0, 10); a.b2 = (const unsigned short*)KP(ka0, 12); a.gw = (const unsigned short*)KP(ka0, 14);
  a.kc = (const unsigned char*)KP(ka1, 0); a.vc = (const unsigned char*)KP(ka1, 2); a.ws = (unsigned char*)KP(ka1, 4); a.out = (unsigned short*)KP(ka1, 6);
  unsigned char* ws = a.ws;
  PSTAMP(21);
  const __amdgpu_buffer_rsrc_t rws = __builtin_amdgcn_make_buffer_rsrc((void*)ws, 0, WS_END, 0x00020000);
  const int rr = c, rb = rr & 7, rc = rr >> 3;

  if (w == 0) {
    if (side) {
      const u64 tag = read_tag(ws);
      const unsigned tg = (unsigned)tag;
      float og = 0.f;
      if (ffn2) {
        const unsigned bb0 = *(const GAS unsigned*)(a.b1 + kh * 256 + l * 2), bb1 = *(const GAS unsigned*)(a.b1 + kh * 256 + 128 + l * 2);
        const v4u gv = *(const GAS v4u*)(a.gw + l * 8);
        DRAIN();
        float gt = 0.f;
#pragma nounroll
        for (int j = 0; j < 8; ++j) { const float t = gate_of(*(const LAS v4u*)(L + L_X + j * 1024 + l * 16), gv); gt = (l & 7) == j ? t : gt; }
        if (l < 8) *(LAS float*)(L + L_G + l * 4) = gt;
        og = rbf(1.f - gt);
        WAIT_TICK(t_in, TH);
        int spin = 0;
#pragma nounroll
        for (int hf = 0; hf < 2;) {
          v4u t[16];
#pragma unroll
          for (int i = 0; i < 16; ++i) {
            t[i] = __builtin_amdgcn_raw_buffer_load_b128(rws, WS_HP + kh * 32768 + hf * 16384 + l * 16, i * 1024, EAL);
            asm volatile("s_sleep %0" :: "n"(GH) : "memory");
          }
          unsigned bad = 0;
#pragma unroll
          for (int j = 0; j < 8; ++j) {
            const int i0 = (j >> 1) * 4 + (j & 1);
            const unsigned bb = (j & 1) ? bb1 : bb0;
            bad |= gbad(t[i0], tg) | gbad(t[i0 + 2], tg);
            const f32x2 z = gval(t[i0]) + gval(t[i0 + 2]);
            const float h0 = rbf(rbf(z.x) + bf_lo(bb)), h1 = rbf(rbf(z.y) + bf_hi(bb));
            const float r0 = h0 > 0.f ? h0 : 0.f, r1 = h1 > 0.f ? h1 : 0.f;
            *(LAS unsigned*)(L + L_H + (hf * 4 + (j >> 1)) * 1024 + (j & 1) * 256 + l * 4) = cvtpk(r0 * r0, r1 * r1);
          }
          if (__builtin_amdgcn_ballot_w64(bad != 0) == 0 || ++spin > SPIN_CAP) ++hf;
        }
        LDSWAIT();
        if (l == 0) *(volatile LAS u64*)(L + L_FLAG + 8) = tag;
      }
      DRAIN();
      STAMP(1);
      PSTAMP(23);
      v4u xb[8];
      const LAS unsigned char* xp = L + (ffn2 ? L_H : L_X + kh * 512) + (l & 7) * 1024 + g * 16;
#pragma unroll
      for (int k = 0; k < 8; ++k) xb[k] = *(const LAS v4u*)(xp + k * 64);
      const int off = (isq ? WS_QP + (cb >> 3) * 65536 + (l & 7) * 8192 + kh * 4096 + (cb & 7) * 512 : WS_HP + (cb >> 2) * 32768 + (l & 7) * 4096 + kh * 2048 + (cb & 3) * 512)
                      + g * 32 + (l & 8) * 2;
#pragma nounroll
      for (int ct = 0; ct < 4; ++ct) {
        f32x4 d = {0.f, 0.f, 0.f, 0.f};
#pragma unroll
        for (int k = 0; k < 8; ++k) d = mfma32(tile_frag(L + L_WT, l, k, ct), xb[k], d);
        if (ffn2) *(LAS f32x4*)(L + L_FF + ct * 1024 + l * 16) = d * og;
        else __builtin_amdgcn_raw_buffer_store_b128(gran_d(d, l, tg), rws, off + ct * 128, 0, EAS);
      }
      LDSWAIT();
      PSTAMP(24);
    }
    STAMP(2);
    FLUSH();
    return;
  }
  if (w == 1) {
    const v4u xv = *(const GAS v4u*)(a.x + rb * DM + l * 8), gv = *(const GAS v4u*)(a.gw + l * 8);
    const v2u bb = *(const GAS v2u*)(a.b2 + rc * 16 + 4 * (l & 3));
    const u64 tag = read_tag(ws);
    const unsigned tg = (unsigned)tag, th = (unsigned)(tag >> 32);
    if (l < 8) __builtin_amdgcn_raw_buffer_store_b128(v4u{t_in, tg, t_in, tg}, rws, WS_X + c * 128 + l * 16, 0, EAS);
    {
      const float gt = gate_of(xv, gv), og = rbf(1.f - gt);
      if (l < 4) *(LAS f32x4*)(L + L_FIN + l * 16) = f32x4{bf_lo(bb.x), bf_hi(bb.x), bf_lo(bb.y), bf_hi(bb.y)} * og;
      if (l == 0) *(LAS float*)(L + L_FIN + 64) = gt;
    }
    WAIT_TICK(t_in, TQ);
    const v2u st = __builtin_amdgcn_raw_buffer_load_b64(rws, WS_X + (l & 7) * 128, 0, AUX_SC1);
#pragma nounroll
    for (int spin = 0; spin < SPIN_CAP; ++spin) {
      v4u t[8];
#pragma unroll
      for (int i = 0; i < 8; ++i) { asm volatile("s_sleep %0" :: "n"(GQ) : "memory"); t[i] = __builtin_amdgcn_raw_buffer_load_b128(rws, WS_QP + kvh * 65536 + b * 8192 + i * 1024 + l * 16, 0, EAL); }
      unsigned bad = 0;
#pragma unroll
      for (int j = 0; j < 4; ++j) {
        bad |= gbad(t[j], tg) | gbad(t[4 + j], tg);
        const f32x2 q = gval(t[j]) + gval(t[4 + j]);
        *(LAS unsigned*)(L + L_Q + j * 256 + l * 4) = cvtpk(q.x, q.y);
      }
      if (__builtin_amdgcn_ballot_w64(bad != 0) == 0) break;
      asm volatile("s_sleep 8" ::: "memory");
    }
    unsigned dtv = DT;
    {
      int late = st.y == tg ? (int)(t_in - st.x) : 0;
      late = max(late, (int)dppu<0xB1>((unsigned)late)); late = max(late, (int)dppu<0x4E>((unsigned)late)); late = max(late, (int)dppu<0x141>((unsigned)late));
      late = min(late, 100);
      if (SKEW >= 0) dtv = __builtin_amdgcn_readfirstlane(DT - (late - SKEW) / 2);
      if (l == 0) *(LAS unsigned*)(L + L_FLAG + 16) = dtv;
    }
    LDSWAIT();
    if (l == 0) *(volatile LAS u64*)(L + L_FLAG) = tag;
    STAMP(3);
    if (ffn2) {
#pragma nounroll
      for (int spin = 0; spin < SPIN_CAP * 16; ++spin) { if (*(volatile const LAS u64*)(L + L_FLAG + 8) == tag) break; asm volatile("s_sleep 8" ::: "memory"); }
    }
    DRAIN();
    if (l == 0) __builtin_amdgcn_raw_buffer_store_b64(v2u{tg, th}, rws, WS_F + c * 8, 0, AUX_SC1);
    STAMP(5);
    v4u ml;
#pragma nounroll
    for (int spin = 0; spin < SPIN_CAP * 16; ++spin) {
      ml = *(volatile const LAS v4u*)(L + L_ML + (l & 31) * 16);
      if (__builtin_amdgcn_ballot_w64(((ml.z ^ tg) | (ml.w ^ th)) != 0) == 0) break;
      asm volatile("s_sleep 4" ::: "memory");
    }
    const float mw = __uint_as_float(ml.x);
    float M = fmaxf(mw, dpp<0xB1>(mw)); M = fmaxf(M, dpp<0x4E>(M)); M = fmaxf(M, dpp<0x141>(M));
    float Ls = __builtin_amdgcn_exp2f(mw - M) * __uint_as_float(ml.y);
    Ls += dpp<0xB1>(Ls); Ls += dpp<0x4E>(Ls); Ls += dpp<0x141>(Ls);
    const float lse = M + __builtin_amdgcn_logf(Ls);
    const v4u lg = gran2(lse, dpp<0x128>(lse), tg);
    if (l < 32 && (l & 15) == 0) ST_SC1(lg, WS_LSE + ((b * 8 + kvh) * 4 + sp) * 128 + l);
    STAMP(6);
    v2u ls;
    asm volatile("s_sleep 16" ::: "memory");
#pragma nounroll
    for (int spin = 0; spin < SPIN_CAP; ++spin) {
      ls = __builtin_amdgcn_raw_buffer_load_b64(rws, WS_LSE + ((b * 8 + kvh) * 4 + (l & 3)) * 128 + ((l >> 3) & 3) * 8, 0, AUX_SC1);
      if (__builtin_amdgcn_ballot_w64(ls.y != tg) == 0) break;
      asm volatile("s_sleep 8" ::: "memory");
    }
    const float lf = __uint_as_float(ls.x);
    float M2 = fmaxf(lf, dpp<0xB1>(lf)); M2 = fmaxf(M2, dpp<0x4E>(M2));
    float E = __builtin_amdgcn_exp2f(lf - M2);
    E += dpp<0xB1>(E); E += dpp<0x4E>(E);
    if (l < 32) *(LAS float*)(L + L_CF + l * 4) = __builtin_amdgcn_exp2f(mw - M2) * __builtin_amdgcn_rcpf(E);
    WAIT_TICK(t_in, TF);
#pragma nounroll
    for (int spin = 0; spin < SPIN_CAP; ++spin) {
      const v4u f0 = LD_SC1(WS_F + l * 16), f1 = LD_SC1(WS_F + 1024 + l * 16);
      if (__builtin_amdgcn_ballot_w64(((f0.x ^ tg) | (f0.y ^ th) | (f0.z ^ tg) | (f0.w ^ th) | (f1.x ^ tg) | (f1.y ^ th) | (f1.z ^ tg) | (f1.w ^ th)) != 0) == 0) break;
      asm volatile("s_sleep 16" ::: "memory");
    }
    LDSWAIT();
    STAMP(7);
    if (LATEWO) {
      unsigned tw = ((t_in + T0 + WOD) << 4) + 32 * DT + 32 * dtv;
      WAIT_NEXT(tw, dtv);
      DMA_LOOP("nt", make_srd(a.wo + oh * 128 * DM + on * 64, 127 * 1024 + 128), (unsigned)(l >> 3) * 1024u + (unsigned)(l & 7) * 16u, 8192, lbase + L_WO, 16);
      WGBAR();
      DRAIN();
      if (l == 0) *(volatile LAS u64*)(L + L_FLAG + 24) = tag;
      LDSWAIT();
      STAMP(8);
    }
    FLUSH();
    return;
  }

  const int sw = w - 2;
  unsigned tnext = ((t_in + T0) << 4) + sw * (DT / 8), dt = DT;
#define PACE_TO(j) do { asm volatile("s_waitcnt vmcnt(%0)" :: "n"(WIN - 1) : "memory"); WAIT_NEXT(tnext, dt); } while (0)
  const size_t base = ((size_t)(b * HKV + kvh) * SK + sp * 1024 + sw * 128) * 256;
  const v4u rk = make_srd(a.kc + base, 32768), rv = make_srd(a.vc + base, 32768);
  const int ko = ((l >> 2) & 3) * 256 + g * 64 + (l & 3) * 16, vo = l * 16;
  const int rot = (4 * sw + xr) & 31;
#define OFF(i) ((((i) + rot) & 31) * 1024)
#define LOAD_V(i) asm volatile("buffer_load_dwordx4 %0, %1, %2, %3 offen nt" : "+v"(kv[i]) : "v"(vo), "s"(rv), "s"(OFF(i)) : "memory")
  if (!LATEWO) {
    const v4u ro = make_srd(a.wo + (oh * 128 + sw * 16) * DM + on * 64, 15 * 1024 + 128);
    const unsigned oo = (unsigned)(l >> 3) * 1024u + (unsigned)(l & 7) * 16u;
    PACE_TO(0); dma_rows(ro, oo, 0, lbase + L_WO + sw * 2048);
    PACE_TO(1); dma_rows(ro, oo, 8192, lbase + L_WO + sw * 2048 + 1024);
  }
  v4u kv[32];
  __builtin_amdgcn_sched_barrier(0);
#pragma unroll
  for (int i = 0; i < 32; ++i) {
    PACE_TO(2 + i);
    asm volatile("buffer_load_dwordx4 %0, %1, %2, %3 offen nt" : "=v"(kv[i]) : "v"(ko), "s"(rk), "s"(OFF(i)) : "memory");
    PSTAMP(100 + i);
    __builtin_amdgcn_sched_barrier(0);
  }
  STAMP(1);
  const u64 tag = read_tag(ws);
  const unsigned tg = (unsigned)tag;
#pragma nounroll
  for (int spin = 0; spin < SPIN_CAP * 16; ++spin) { if (*(volatile const LAS u64*)(L + L_FLAG) == tag) break; asm volatile("s_sleep 2" ::: "memory"); }
  asm volatile("" ::: "memory");
  STAMP(2);
  const v4u qf = *(const LAS v4u*)(L + L_Q + ((l >> 2) & 3) * 256 + g * 64 + (l & 3) * 16);
  dt = __builtin_amdgcn_readfirstlane(*(const LAS unsigned*)(L + L_FLAG + 16));
  __builtin_amdgcn_sched_barrier(0);
  constexpr float C2 = 0.088388347648318440f * 1.4426950408889634f;
  float sc[8];
  const int s = l & 3;
#pragma unroll
  for (int m = 0; m < 8; ++m) {
    asm volatile("s_waitcnt vmcnt(%4)" : "+v"(kv[4 * m]), "+v"(kv[4 * m + 1]), "+v"(kv[4 * m + 2]), "+v"(kv[4 * m + 3]) : "n"(28 - 4 * m + m) : "memory");
    f32x4 d[4];
#pragma unroll
    for (int r = 0; r < 4; ++r) d[r] = mfma32(kv[4 * m + r], qf, f32x4{0.f, 0.f, 0.f, 0.f});
#pragma unroll
    for (int r = 0; r < 4; ++r) {
      float t = dpp<0x00>(d[r][0]); t = dpp<0x55>(d[r][1]) + t; t = dpp<0xAA>(d[r][2]) + t; t = dpp<0xFF>(d[r][3]) + t;
      asm volatile("" : "+v"(t));
      sc[m] = (r == 0 || s == r) ? t : sc[m];
    }
    asm volatile("" : "+v"(sc[m]));
    __builtin_amdgcn_sched_barrier(0);
    PACE_TO(34 + m);
    LOAD_V(m);
    __builtin_amdgcn_sched_barrier(0);
  }
  STAMP(3);
  float mx = sc[0];
#pragma unroll
  for (int m = 1; m < 8; ++m) mx = fmaxf(mx, sc[m]);
  mx = fmaxf(mx, dpp<0xB1>(mx)); mx = fmaxf(mx, dpp<0x4E>(mx)); mx = xrow_max(mx);
  const float mneg = -mx * C2;
  float lsum = 0.f;
  unsigned pk[8];
#pragma unroll
  for (int m = 0; m < 8; ++m) { const float p = __builtin_amdgcn_exp2f(fmaf(sc[m], C2, mneg)); pk[m] = cvtpk(p, p); lsum += bf_lo(pk[m]); }
  lsum += dpp<0xB1>(lsum); lsum += dpp<0x4E>(lsum); lsum = xrow_sum(lsum);
  if (l < 16 && (l & 3) == 0) *(LAS v4u*)(L + L_ML + ((l >> 2) * 8 + sw) * 16) = v4u{__float_as_uint(mx * C2), __float_as_uint(lsum), tg, (unsigned)(tag >> 32)};
  const unsigned half = (l & 1) ? 0xffff0000u : 0x0000ffffu;
  const unsigned m0 = (l & 2) ? 0u : half, m1 = (l & 2) ? half : 0u;
  f32x4 o0 = {0.f, 0.f, 0.f, 0.f}, o1 = {0.f, 0.f, 0.f, 0.f};
#pragma unroll
  for (int j = 8; j < 40; ++j) {
    if (j < 32) { PACE_TO(34 + j); LOAD_V(j); __builtin_amdgcn_sched_barrier(0); }
    if (j == 31) STAMP(11);
    const int i = j - 8;
    const unsigned pb = (i & 3) == 0 ? dppu<0x00>(pk[i >> 2]) : (i & 3) == 1 ? dppu<0x55>(pk[i >> 2]) : (i & 3) == 2 ? dppu<0xAA>(pk[i >> 2]) : dppu<0xFF>(pk[i >> 2]);
    const v2u av = {pb & m0, pb & m1};
    asm volatile("s_waitcnt vmcnt(%1)" : "+v"(kv[i]) : "n"((j < 32 ? j : 31) - i) : "memory");
    o0 = mfma16(av, v2u{kv[i].x, kv[i].y}, o0);
    o1 = mfma16(av, v2u{kv[i].z, kv[i].w}, o1);
    __builtin_amdgcn_sched_barrier(0);
  }
  *(LAS f32x4*)(L + L_MRG + (sw * 4 + g) * 512 + (l & 15) * 32) = o0;
  *(LAS f32x4*)(L + L_MRG + (sw * 4 + g) * 512 + (l & 15) * 32 + 16) = o1;
  STAMP(4);
  LDSWAIT();
  WGBAR();
  if (sw < 4) {
    STAMP(5);
    const f32x4 c0 = *(const LAS f32x4*)(L + L_CF + sw * 32), c1 = *(const LAS f32x4*)(L + L_CF + sw * 32 + 16);
    f32x2 acc = {0.f, 0.f};
#pragma unroll
    for (int j = 0; j < 8; ++j) {
      const f32x2 ov = *(const LAS f32x2*)(L + L_MRG + (j * 4 + sw) * 512 + l * 8);
      const float e = j < 4 ? c0[j & 3] : c1[j & 3];
      acc.x = fmaf(e, ov.x, acc.x); acc.y = fmaf(e, ov.y, acc.y);
    }
    const unsigned pv = notag(cvtpk(acc.x, acc.y), tg);
    const v4u pq = {dppu<0x00>(pv), dppu<0x55>(pv), dppu<0xAA>(pv), dppu<0xFF>(pv)};
    if ((l & 3) == 0) ST_SC1(pq, WS_P + (((kvh * 4 + sw) * 8 + b) * 4 + sp) * 256 + l * 4);
    STAMP(6);
    f32x4 ff = {0.f, 0.f, 0.f, 0.f};
    float gm = 1.f;
    if (ffn2) { ff = *(const LAS f32x4*)(L + L_FF + sw * 1024 + l * 16); gm = *(const LAS float*)(L + L_G + (l & 7) * 4); }
    const int po = WS_P + (oh * 8 + 2 * sw) * 1024 + l * 16;
    asm volatile("s_sleep %0" :: "n"(PDELAY1) : "memory");
    v4u p0 = LD_SC1(po), p1 = LD_SC1(po + 1024);
    if (LATEWO) {
#pragma nounroll
      for (int spin = 0; spin < SPIN_CAP * 16; ++spin) { if (*(volatile const LAS u64*)(L + L_FLAG + 24) == tag) break; }
      asm volatile("" ::: "memory");
    }
    v4u wf[4];
#pragma unroll
    for (int k = 0; k < 4; ++k) wf[k] = tile_frag(L + L_WO, l, k, sw);
    __builtin_amdgcn_sched_barrier(0);
    if ((p0.y == tg) | (p0.w == tg) | (p1.y == tg) | (p1.w == tg)) poll2(rws, po, po + 1024, tg, p0, p1);
    *(LAS v4u*)(L + L_PG + (8 * sw + g) * 272 + (l & 15) * 16) = p0;
    *(LAS v4u*)(L + L_PG + (8 * sw + 4 + g) * 272 + (l & 15) * 16) = p1;
    STAMP(7);
    LDSWAIT();
    WGBAR();
    const int pb = l & 7, sl = (l >> 3) & 1;
    v4u pa[8];
#pragma unroll
    for (int k = 0; k < 8; ++k) pa[k] = *(const LAS v4u*)(L + L_PG + (pb * 4 + (k >> 2) * 2 + sl) * 272 + (k & 3) * 64 + g * 16);
    __builtin_amdgcn_sched_barrier(0);
    f32x4 da = {0.f, 0.f, 0.f, 0.f}, db = {0.f, 0.f, 0.f, 0.f};
#pragma unroll
    for (int k = 0; k < 4; ++k) { da = mfma32(wf[k], pa[k], da); db = mfma32(wf[k], pa[4 + k], db); }
    v4u r;
#pragma unroll
    for (int j = 0; j < 4; ++j) { const float t = da[j] + db[j]; r[j] = notag(__float_as_uint(fmaf(t + dpp<0x128>(t), gm, ff[j])), tg); }
    if (sl == 0) ST_SC1(r, WS_O + (((on * 4 + sw) * 4 + (pb >> 1)) * 32 + oh) * 128 + (pb & 1) * 64 + g * 16);
    STAMP(8);
    if (sw == 0) {
      const f32x4 fin = *(const LAS f32x4*)(L + L_FIN + (l & 3) * 16);
      const float gt = *(const LAS float*)(L + L_FIN + 64);
      const float g0 = (l & 12) == 12 ? 1.f : gt, g1 = l < 48 ? g0 : gt;
      const int oo = WS_O + (rr >> 1) * 4096 + (l >> 2) * 128 + (rr & 1) * 64 + (l & 3) * 16;
      v4u r0, r1;
      asm volatile("s_sleep %0" :: "n"(PDELAY2) : "memory");
      poll2(rws, oo, oo + 2048, tg, r0, r1);
      STAMP(9);
      f32x4 av = __builtin_bit_cast(f32x4, r0) * g0 + __builtin_bit_cast(f32x4, r1) * g1;
#pragma unroll
      for (int j = 0; j < 4; ++j) av[j] += dpp<0x124>(av[j]);
#pragma unroll
      for (int j = 0; j < 4; ++j) av[j] += dpp<0x128>(av[j]);
      {
        auto s0 = __builtin_amdgcn_permlane16_swap(__float_as_uint(av[0]), __float_as_uint(av[1]), false, false);
        auto s1 = __builtin_amdgcn_permlane16_swap(__float_as_uint(av[2]), __float_as_uint(av[3]), false, false);
        const float u0 = __uint_as_float(s0[0]) + __uint_as_float(s0[1]), u1 = __uint_as_float(s1[0]) + __uint_as_float(s1[1]);
        auto s2 = __builtin_amdgcn_permlane32_swap(__float_as_uint(u0), __float_as_uint(u1), false, false);
        const float v = __uint_as_float(s2[0]) + __uint_as_float(s2[1]);
        const float o = v + (g == 0 ? fin[0] : g == 1 ? fin[1] : g == 2 ? fin[2] : fin[3]);
        if ((l & 15) < 4) *(GAS unsigned short*)(a.out + rb * DM + rc * 16 + 4 * (l & 3) + g) = (unsigned short)cvtpk(o, o);
      }
      if (c == 0 && l == 0) *(GAS u64*)(ws + WS_E) = tag;
      STAMP(10);
    }
  }
  FLUSH();
}

extern "C" void kernel_launch(void* const* d_in, const int* in_sizes, int n_in, void* d_out, int out_size, void* d_ws, size_t ws_size,
                              hipStream_t stream) {
  Args a;
  a.x = (const unsigned short*)d_in[0];
  a.kc = (const unsigned char*)d_in[1];
  a.vc = (const unsigned char*)d_in[2];
  a.wq = (const unsigned short*)d_in[3];
  a.wo = (const unsigned short*)d_in[4];
  a.w1 = (const unsigned short*)d_in[6];
  a.b1 = (const unsigned short*)d_in[7];
  a.w2 = (const unsigned short*)d_in[8];
  a.b2 = (const unsigned short*)d_in[9];
  a.gw = (const unsigned short*)d_in[10];
  a.ws = (unsigned char*)d_ws;
  a.out = (unsigned short*)d_out;
  fused_decode<<<256, NWAVE * 64, 0, stream>>>(a);
}
```

```cpp
#define PROBE 105
#define PW0 2
#define PW1 10
#define SLIP -1
#include <hip/hip_runtime.h>
#include <hip/hip_bf16.h>
#include <stdint.h>

typedef unsigned v4u __attribute__((ext_vector_type(4)));
typedef unsigned v2u __attribute__((ext_vector_type(2)));
typedef float f32x4 __attribute__((ext_vector_type(4)));
typedef float f32x2 __attribute__((ext_vector_type(2)));
typedef __bf16 bf16x8 __attribute__((ext_vector_type(8)));
typedef __bf16 bf16x2 __attribute__((ext_vector_type(2)));
typedef short s16x4 __attribute__((ext_vector_type(4)));
typedef unsigned long long u64;
#define GAS __attribute__((address_space(1)))
#define LAS __attribute__((address_space(3)))

constexpr int DM = 512, HKV = 8, SK = 4096, FF = 1792;
constexpr int AUX_SC1 = 16;
#define DT 480
#define T0 130
#define WIN 5
#define TQ 560
#define TH 480
#define TF 1500
#define GQ 6
#define GH 3
#define SHIFT 1
#define PDELAY1 12
#define PDELAY2 12
#define SKEW 20
#define LATEWO 1
#define WOD 35
constexpr int NS0 = LATEWO ? 0 : 2;
#define RMAP 0
#define EAL 18
#define EAS 18
#define CODEKB 16
#define CODEB 15360
#define TOUCH 1
#define WOS 10
constexpr int NWAVE = 10;

constexpr int WS_E = 0;
constexpr int WS_F = 2048;
constexpr int WS_QP = 4096;
constexpr int WS_HP = WS_QP + 8 * 8 * 2 * 512 * 8;
constexpr int WS_X = WS_HP + 7 * 8 * 2 * 256 * 8;
constexpr int WS_LSE = WS_X + 256 * 128;
constexpr int WS_END = WS_LSE + 256 * 128;
constexpr int WS_O = WS_QP;
constexpr int WS_P = WS_HP;
static_assert(WS_LSE - WS_P == 32 * 8 * 4 * 256, "the partials fill exactly the lines of HP and X");
constexpr int WS_ST = 8 << 20;
constexpr int L_WO = 0, L_WT = 16384, L_MRG = 49152, L_ML = L_MRG + 16384, L_CF = L_ML + 512, L_Q = L_CF + 128, L_X = L_Q + 1024, L_H = L_X + 8192, L_NIL = L_H + 8192, L_FF = L_NIL + 1024,
              L_G = L_FF + 4096, L_FIN = L_G + 32, L_FLAG = L_FIN + 96, L_DONE = L_FLAG + 32, L_END = L_DONE + 64;
constexpr int L_PG = L_WT;

__device__ __forceinline__ f32x4 mfma32(v4u a, v4u b, f32x4 c) {
  return __builtin_amdgcn_mfma_f32_16x16x32_bf16(__builtin_bit_cast(bf16x8, a), __builtin_bit_cast(bf16x8, b), c, 0, 0, 0);
}
__device__ __forceinline__ f32x4 mfma16(v2u a, v2u b, f32x4 c) {
  return __builtin_amdgcn_mfma_f32_16x16x16bf16_1k(__builtin_bit_cast(s16x4, a), __builtin_bit_cast(s16x4, b), c, 0, 0, 0);
}
__device__ __forceinline__ unsigned cvtpk(float lo, float hi) { bf16x2 r = {(__bf16)lo, (__bf16)hi}; return __builtin_bit_cast(unsigned, r); }
__device__ __forceinline__ float bf_lo(unsigned u) { return __builtin_bit_cast(float, u << 16); }
__device__ __forceinline__ float bf_hi(unsigned u) { return __builtin_bit_cast(float, u & 0xffff0000u); }
__device__ __forceinline__ float rbf(float x) { return (float)(__bf16)x; }
template <int CTRL> __device__ __forceinline__ float dpp(float x) { return __builtin_bit_cast(float, __builtin_amdgcn_mov_dpp(__builtin_bit_cast(int, x), CTRL, 0xf, 0xf, true)); }
template <int CTRL> __device__ __forceinline__ unsigned dppu(unsigned x) { return (unsigned)__builtin_amdgcn_mov_dpp((int)x, CTRL, 0xf, 0xf, true); }
__device__ __forceinline__ float xrow_max(float x) {
  auto s = __builtin_amdgcn_permlane16_swap(__float_as_uint(x), __float_as_uint(x), false, false);
  x = fmaxf(__uint_as_float(s[0]), __uint_as_float(s[1]));
  auto t = __builtin_amdgcn_permlane32_swap(__float_as_uint(x), __float_as_uint(x), false, false);
  return fmaxf(__uint_as_float(t[0]), __uint_as_float(t[1]));
}
__device__ __forceinline__ float xrow_sum(float x) {
  auto s = __builtin_amdgcn_permlane16_swap(__float_as_uint(x), __float_as_uint(x), false, false);
  x = __uint_as_float(s[0]) + __uint_as_float(s[1]);
  auto t = __builtin_amdgcn_permlane32_swap(__float_as_uint(x), __float_as_uint(x), false, false);
  return __uint_as_float(t[0]) + __uint_as_float(t[1]);
}
__device__ __forceinline__ v2u tr_read(const LAS unsigned char* p) { return __builtin_bit_cast(v2u, __builtin_amdgcn_ds_read_tr16_b64_v4i16((LAS s16x4*)p)); }
__device__ __forceinline__ v4u make_srd(const void* p, unsigned bytes) {
  const u64 a = (u64)p;
  return v4u{(unsigned)a, (unsigned)(a >> 32) & 0xffffu, bytes, 0x00020000u};
}
__device__ __forceinline__ void dma_rows(v4u srd, unsigned voff, unsigned soff, unsigned lds_addr) {
  unsigned keep;
  asm volatile("s_mov_b32 %0, m0\n\ts_mov_b32 m0, %1\n\ts_nop 0\n\tbuffer_load_dwordx4 %2, %3, %4 offen nt lds\n\ts_mov_b32 m0, %0"
               : "=&s"(keep) : "s"(lds_addr), "v"(voff), "s"(srd), "s"(soff) : "memory");
}
#define DMA_LOOP(POL, srd, voff, step, lds_addr, n) do { unsigned keep_, so_ = 0, n_ = (n); \
  asm volatile("s_cmp_eq_u32 %2, 0\n\ts_cbranch_scc1 2f\n\ts_mov_b32 %0, m0\n\ts_mov_b32 m0, %3\n\ts_nop 0\n1:\n\tbuffer_load_dwordx4 %4, %5, %1 offen " POL " lds\n\t" \
               "s_add_u32 m0, m0, 0x400\n\ts_add_u32 %1, %1, %6\n\ts_sub_u32 %2, %2, 1\n\ts_cmp_lg_u32 %2, 0\n\ts_cbranch_scc1 1b\n\ts_mov_b32 m0, %0\n2:" \
               : "=&s"(keep_), "+&s"(so_), "+&s"(n_) : "s"(lds_addr), "v"(voff), "s"(srd), "s"(step) : "scc", "memory"); } while (0)
__device__ __forceinline__ v4u tile_frag(const LAS unsigned char* tile, int l, int k, int ct) {
  const LAS unsigned char* p = tile + (8 * (l >> 4) + ((l & 15) >> 2)) * 128 + (l & 3) * 8 + k * 4096 + ct * 32;
  const v2u t0 = tr_read(p), t1 = tr_read(p + 512);
  return v4u{t0.x, t0.y, t1.x, t1.y};
}
#define DRAIN() asm volatile("s_waitcnt vmcnt(0)" ::: "memory")
#define LDSWAIT() asm volatile("s_waitcnt lgkmcnt(0)" ::: "memory")
#define WGBAR() do { __builtin_amdgcn_sched_barrier(0); __builtin_amdgcn_s_barrier(); __builtin_amdgcn_sched_barrier(0); asm volatile("" ::: "memory"); } while (0)
__device__ __forceinline__ u64 read_tag(const unsigned char* ws) {
  u64 t;
  asm volatile("s_load_dwordx2 %0, %1, 0x0\n\ts_waitcnt lgkmcnt(0)" : "=s"(t) : "s"(ws) : "memory");
  const unsigned old = (unsigned)t;
  t += 0x9E3779B97F4A7C15ull;
  t = (t ^ (t >> 30)) * 0xBF58476D1CE4E5B9ull; t = (t ^ (t >> 27)) * 0x94D049BB133111EBull;
  t ^= t >> 31;
  return (unsigned)t == old ? t ^ 1u : t;
}
__device__ __forceinline__ unsigned now_ticks() {
  u64 t;
  asm volatile("s_memrealtime %0\n\ts_waitcnt lgkmcnt(0)" : "=s"(t) :: "memory");
  return (unsigned)t;
}
#define WAIT_TICK(base, delta) \
  asm volatile("s_branch 2f\n1:\n\ts_sleep 1\n2:\n\ts_memrealtime vcc\n\ts_waitcnt lgkmcnt(0)\n\ts_sub_u32 vcc_lo, vcc_lo, %0\n\ts_cmpk_lt_i32 vcc_lo, %1\n\ts_cbranch_scc1 1b" \
               :: "s"(base), "n"(delta) : "scc", "vcc", "memory")
#define WAIT_NEXT(acc, step) \
  asm volatile("s_branch 2f\n1:\n\ts_sleep 1\n2:\n\ts_memrealtime vcc\n\ts_waitcnt lgkmcnt(0)\n\ts_lshl_b32 vcc_lo, vcc_lo, 4\n\ts_sub_u32 vcc_lo, vcc_lo, %0\n\ts_cmp_lt_i32 vcc_lo, 0\n\ts_cbranch_scc1 1b\n\ts_add_u32 %0, %0, %1" \
               : "+s"(acc) : "s"(step) : "scc", "vcc", "memory")
constexpr int SPIN_CAP = 1 << 18;
#define LD_SC1(off) __builtin_amdgcn_raw_buffer_load_b128(rws, (off), 0, AUX_SC1)
#define ST_SC1(v, off) __builtin_amdgcn_raw_buffer_store_b128((v), rws, (off), 0, AUX_SC1)
__device__ __forceinline__ v4u gran2(float a, float b, unsigned tg) { return v4u{__float_as_uint(a), tg, __float_as_uint(b), tg}; }
__device__ __forceinline__ unsigned gbad(v4u g, unsigned tg) { return (g.y ^ tg) | (g.w ^ tg); }
__device__ __forceinline__ f32x2 gval(v4u g) { return f32x2{__uint_as_float(g.x), __uint_as_float(g.z)}; }
__device__ __forceinline__ v4u gran_d(f32x4 d, int l, unsigned tg) { return gran2((l & 8) ? d[2] : d[0], (l & 8) ? d[3] : d[1], tg); }

#define PSTAMP(k) STAMP(k)
#define STAMP(k) do { if ((k) == PROBE && w >= PW0 && w < PW1) { const unsigned t_ = now_ticks() - t_in; if (l == 0) *(volatile LAS unsigned*)(L + L_END + w * 4) = t_; LDSWAIT(); } } while (0)
#define FLUSH() do { } while (0)

__device__ __forceinline__ void poll2(__amdgpu_buffer_rsrc_t rws, int o0, int o1, unsigned tg, v4u& r0, v4u& r1) {
  bool need = true;
#pragma nounroll
  for (int spin = 0; spin < SPIN_CAP && need; ++spin) {
    r0 = LD_SC1(o0); r1 = LD_SC1(o1);
    need = (r0.y == tg) | (r0.w == tg) | (r1.y == tg) | (r1.w == tg);
    asm volatile("" ::: "memory");
  }
}
__device__ __forceinline__ unsigned notag(unsigned x, unsigned tg) { return x == tg ? x ^ 1u : x; }
__device__ __forceinline__ float gate_of(v4u xv, v4u gv) {
  float gs = 0.f;
#pragma unroll
  for (int j = 0; j < 4; ++j) { gs += rbf(bf_lo(xv[j]) * bf_lo(gv[j])); gs += rbf(bf_hi(xv[j]) * bf_hi(gv[j])); }
  gs += dpp<0xB1>(gs); gs += dpp<0x4E>(gs); gs += dpp<0x124>(gs); gs += dpp<0x128>(gs); gs = xrow_sum(gs);
  return rbf(1.f / (1.f + __expf(-rbf(gs))));
}

struct Args {
  const unsigned short *x, *wq, *wo, *w1, *b1, *w2, *b2, *gw;
  const unsigned char *kc, *vc;
  unsigned char* ws;
  unsigned short* out;
};

typedef unsigned v16u __attribute__((ext_vector_type(16)));
typedef unsigned v8u __attribute__((ext_vector_type(8)));

__global__ void __launch_bounds__(NWAVE * 64) fused_decode(Args a_) {
  v16u ka0; v8u ka1; u64 t_in64;
  asm volatile("s_load_dwordx16 %0, %3, 0x0\n\ts_load_dwordx8 %1, %3, 0x40\n\ts_memrealtime %2" : "=&s"(ka0), "=&s"(ka1), "=&s"(t_in64) : "s"(__builtin_amdgcn_kernarg_segment_ptr()));
  __shared__ __attribute__((aligned(16))) unsigned char smem[L_END + 64];
  LAS unsigned char* L = (LAS unsigned char*)smem;
  const unsigned lbase = (unsigned)(uintptr_t)L;
  const int tid = threadIdx.x, l = tid & 63, w = __builtin_amdgcn_readfirstlane(tid >> 6);
  const int c = blockIdx.x, xr = c >> 3, sp = xr & 3, kvh = xr >> 2, b = (c - kvh) & 7, i32 = b * 4 + sp;
  {
    u64 pc;
    asm volatile("s_getpc_b64 %0" : "=s"(pc));
    const v4u rc = make_srd((const void*)(pc & ~255ull), CODEB);
    DMA_LOOP("", rc, ((xr * 2 + w) & (CODEKB - 1)) * 1024 + l * 16, 0, lbase + L_NIL, (12u >> w) & 1u);
  }
  const int g = l >> 4;
  const int oh = (c & 7) * 4 + (xr >> 3), on = xr & 7;
  const bool ffn2 = xr >= 24 && (c & 7) < 7;
  const bool isq = xr < 16, side = xr < 23 || ffn2;
  const int tl = isq ? ((xr >> 1) * 8 + ((c + xr) & 7)) * 2 + (xr & 1) : (c & 7) * 7 + xr - 16;
  const int cb = ffn2 ? on : tl >> 1, kh = ffn2 ? c & 7 : tl & 1;
#define KP(v, i) ((u64)v[i] | ((u64)v[(i) + 1] << 32))
  if (__builtin_expect(w == 0, 1)) {
    const unsigned ldb = isq ? 8192u : ffn2 ? 1024u : (unsigned)FF * 2u;
    unsigned woff = (unsigned)kh * 256u * ldb + cb * 128, wlen = 255u * ldb + 128u, wstep = 8u * ldb, wvo = (unsigned)(l >> 3) * ldb + (unsigned)(l & 7) * 16u;
    unsigned nw = side ? 32 : 0;
    asm volatile("s_waitcnt lgkmcnt(0)" : "+s"(ka0), "+s"(woff), "+s"(wlen), "+s"(wstep), "+s"(nw), "+v"(wvo) :: "memory");
    u64 pq = KP(ka0, 2), p1 = KP(ka0, 6), p2 = KP(ka0, 10);
    asm volatile("" : "+s"(pq), "+s"(p1), "+s"(p2));
    DMA_LOOP("nt", make_srd((const unsigned char*)(isq ? pq : ffn2 ? p2 : p1) + woff, wlen), wvo, wstep, lbase + L_WT, nw);
    DMA_LOOP("", make_srd((const void*)KP(ka0, 0), 8192), l * 16, 1024, lbase + L_X, nw >> 2);
  }
  asm volatile("s_waitcnt lgkmcnt(0)" : "+s"(t_in64), "+s"(ka0), "+s"(ka1) :: "memory");
  const unsigned t_in = (unsigned)t_in64;
  Args a;
  a.x = (const unsigned short*)KP(ka0, 0); a.wq = (const unsigned short*)KP(ka0, 2); a.wo = (const unsigned short*)KP(ka0, 4); a.w1 = (const unsigned short*)KP(ka0, 6);
  a.b1 = (const unsigned short*)KP(ka0, 8); a.w2 = (const unsigned short*)KP(ka0, 10); a.b2 = (const unsigned short*)KP(ka0, 12); a.gw = (const unsigned short*)KP(ka0, 14);
  a.kc = (const unsigned char*)KP(ka1, 0); a.vc = (const unsigned char*)KP(ka1, 2); a.ws = (unsigned char*)KP(ka1, 4); a.out = (unsigned short*)KP(ka1, 6);
  unsigned char* ws = a.ws;
  if (l == 0) *(volatile LAS unsigned*)(L + L_END + w * 4) = 0;
  PSTAMP(21);
  const __amdgpu_buffer_rsrc_t rws = __builtin_amdgcn_make_buffer_rsrc((void*)ws, 0, WS_END, 0x00020000);
  const int rr = c, rb = rr & 7, rc = rr >> 3;

  if (w == 0) {
    if (side) {
      const u64 tag = read_tag(ws);
      const unsigned tg = (unsigned)tag;
      float og = 0.f;
      if (ffn2) {
        const unsigned bb0 = *(const GAS unsigned*)(a.b1 + kh * 256 + l * 2), bb1 = *(const GAS unsigned*)(a.b1 + kh * 256 + 128 + l * 2);
        const v4u gv = *(const GAS v4u*)(a.gw + l * 8);
        DRAIN();
        float gt = 0.f;
#pragma nounroll
        for (int j = 0; j < 8; ++j) { const float t = gate_of(*(const LAS v4u*)(L + L_X + j * 1024 + l * 16), gv); gt = (l & 7) == j ? t : gt; }
        if (l < 8) *(LAS float*)(L + L_G + l * 4) = gt;
        og = rbf(1.f - gt);
        WAIT_TICK(t_in, TH);
        int spin = 0;
#pragma nounroll
        for (int hf = 0; hf < 2;) {
          v4u t[16];
#pragma unroll
          for (int i = 0; i < 16; ++i) {
            t[i] = __builtin_amdgcn_raw_buffer_load_b128(rws, WS_HP + kh * 32768 + hf * 16384 + l * 16, i * 1024, EAL);
            asm volatile("s_sleep %0" :: "n"(GH) : "memory");
          }
          unsigned bad = 0;
#pragma unroll
          for (int j = 0; j < 8; ++j) {
            const int i0 = (j >> 1) * 4 + (j & 1);
            const unsigned bb = (j & 1) ? bb1 : bb0;
            bad |= gbad(t[i0], tg) | gbad(t[i0 + 2], tg);
            const f32x2 z = gval(t[i0]) + gval(t[i0 + 2]);
            const float h0 = rbf(rbf(z.x) + bf_lo(bb)), h1 = rbf(rbf(z.y) + bf_hi(bb));
            const float r0 = h0 > 0.f ? h0 : 0.f, r1 = h1 > 0.f ? h1 : 0.f;
            *(LAS unsigned*)(L + L_H + (hf * 4 + (j >> 1)) * 1024 + (j & 1) * 256 + l * 4) = cvtpk(r0 * r0, r1 * r1);
          }
          if (__builtin_amdgcn_ballot_w64(bad != 0) == 0 || ++spin > SPIN_CAP) ++hf;
        }
        LDSWAIT();
        if (l == 0) *(volatile LAS u64*)(L + L_FLAG + 8) = tag;
      }
      DRAIN();
      STAMP(1);
      PSTAMP(23);
      v4u xb[8];
      const LAS unsigned char* xp = L + (ffn2 ? L_H : L_X + kh * 512) + (l & 7) * 1024 + g * 16;
#pragma unroll
      for (int k = 0; k < 8; ++k) xb[k] = *(const LAS v4u*)(xp + k * 64);
      const int off = (isq ? WS_QP + (cb >> 3) * 65536 + (l & 7) * 8192 + kh * 4096 + (cb & 7) * 512 : WS_HP + (cb >> 2) * 32768 + (l & 7) * 4096 + kh * 2048 + (cb & 3) * 512)
                      + g * 32 + (l & 8) * 2;
#pragma nounroll
      for (int ct = 0; ct < 4; ++ct) {
        f32x4 d = {0.f, 0.f, 0.f, 0.f};
#pragma unroll
        for (int k = 0; k < 8; ++k) d = mfma32(tile_frag(L + L_WT, l, k, ct), xb[k], d);
        if (ffn2) *(LAS f32x4*)(L + L_FF + ct * 1024 + l * 16) = d * og;
        else __builtin_amdgcn_raw_buffer_store_b128(gran_d(d, l, tg), rws, off + ct * 128, 0, EAS);
      }
      LDSWAIT();
      PSTAMP(24);
    }
    STAMP(2);
    FLUSH();
    return;
  }
  if (w == 1) {
    const v4u xv = *(const GAS v4u*)(a.x + rb * DM + l * 8), gv = *(const GAS v4u*)(a.gw + l * 8);
    const v2u bb = *(const GAS v2u*)(a.b2 + rc * 16 + 4 * (l & 3));
    if (TOUCH) {
      const v4u rt = make_srd(a.wo + oh * 128 * DM + on * 64, 16);
      DMA_LOOP("nt", rt, l * 16, 0, lbase + L_NIL, 1);
      const v4u ru = make_srd(a.out, 16);
      DMA_LOOP("nt", ru, l * 16, 0, lbase + L_NIL, 1);
    }
    const u64 tag = read_tag(ws);
    const unsigned tg = (unsigned)tag, th = (unsigned)(tag >> 32);
    if (l < 8) __builtin_amdgcn_raw_buffer_store_b128(v4u{t_in, tg, t_in, tg}, rws, WS_X + c * 128 + l * 16, 0, EAS);
    {
      const float gt = gate_of(xv, gv), og = rbf(1.f - gt);
      if (l < 4) *(LAS f32x4*)(L + L_FIN + l * 16) = f32x4{bf_lo(bb.x), bf_hi(bb.x), bf_lo(bb.y), bf_hi(bb.y)} * og;
      if (l == 0) *(LAS float*)(L + L_FIN + 64) = gt;
    }
    WAIT_TICK(t_in, TQ);
    const v2u st = __builtin_amdgcn_raw_buffer_load_b64(rws, WS_X + (l & 7) * 128, 0, AUX_SC1);
#pragma nounroll
    for (int spin = 0; spin < SPIN_CAP; ++spin) {
      v4u t[8];
#pragma unroll
      for (int i = 0; i < 8; ++i) { asm volatile("s_sleep %0" :: "n"(GQ) : "memory"); t[i] = __builtin_amdgcn_raw_buffer_load_b128(rws, WS_QP + kvh * 65536 + b * 8192 + i * 1024 + l * 16, 0, EAL); }
      unsigned bad = 0;
#pragma unroll
      for (int j = 0; j < 4; ++j) {
        bad |= gbad(t[j], tg) | gbad(t[4 + j], tg);
        const f32x2 q = gval(t[j]) + gval(t[4 + j]);
        *(LAS unsigned*)(L + L_Q + j * 256 + l * 4) = cvtpk(q.x, q.y);
      }
      if (__builtin_amdgcn_ballot_w64(bad != 0) == 0) break;
      asm volatile("s_sleep 8" ::: "memory");
    }
    unsigned dtv = DT;
    {
      int late = st.y == tg ? (int)(t_in - st.x) : 0;
      late = max(late, (int)dppu<0xB1>((unsigned)late)); late = max(late, (int)dppu<0x4E>((unsigned)late)); late = max(late, (int)dppu<0x141>((unsigned)late));
      late = min(late, 100);
      if (SKEW >= 0) dtv = __builtin_amdgcn_readfirstlane(DT - (late - SKEW) / 2);
      if (l == 0) *(LAS unsigned*)(L + L_FLAG + 16) = dtv;
    }
    LDSWAIT();
    if (l == 0) *(volatile LAS u64*)(L + L_FLAG) = tag;
    STAMP(3);
    if (ffn2) {
#pragma nounroll
      for (int spin = 0; spin < SPIN_CAP * 16; ++spin) { if (*(volatile const LAS u64*)(L + L_FLAG + 8) == tag) break; asm volatile("s_sleep 8" ::: "memory"); }
    }
    DRAIN();
    if (l == 0) __builtin_amdgcn_raw_buffer_store_b64(v2u{tg, th}, rws, WS_F + c * 8, 0, AUX_SC1);
    STAMP(5);
    v4u ml;
#pragma nounroll
    for (int spin = 0; spin < SPIN_CAP * 16; ++spin) {
      ml = *(volatile const LAS v4u*)(L + L_ML + (l & 31) * 16);
      if (__builtin_amdgcn_ballot_w64(((ml.z ^ tg) | (ml.w ^ th)) != 0) == 0) break;
      asm volatile("s_sleep 4" ::: "memory");
    }
    const float mw = __uint_as_float(ml.x);
    float M = fmaxf(mw, dpp<0xB1>(mw)); M = fmaxf(M, dpp<0x4E>(M)); M = fmaxf(M, dpp<0x141>(M));
    float Ls = __builtin_amdgcn_exp2f(mw - M) * __uint_as_float(ml.y);
    Ls += dpp<0xB1>(Ls); Ls += dpp<0x4E>(Ls); Ls += dpp<0x141>(Ls);
    const float lse = M + __builtin_amdgcn_logf(Ls);
    const v4u lg = gran2(lse, dpp<0x128>(lse), tg);
    if (l < 32 && (l & 15) == 0) ST_SC1(lg, WS_LSE + ((b * 8 + kvh) * 4 + sp) * 128 + l);
    STAMP(6);
    v2u ls;
    asm volatile("s_sleep 16" ::: "memory");
#pragma nounroll
    for (int spin = 0; spin < SPIN_CAP; ++spin) {
      ls = __builtin_amdgcn_raw_buffer_load_b64(rws, WS_LSE + ((b * 8 + kvh) * 4 + (l & 3)) * 128 + ((l >> 3) & 3) * 8, 0, AUX_SC1);
      if (__builtin_amdgcn_ballot_w64(ls.y != tg) == 0) break;
      asm volatile("s_sleep 8" ::: "memory");
    }
    const float lf = __uint_as_float(ls.x);
    float M2 = fmaxf(lf, dpp<0xB1>(lf)); M2 = fmaxf(M2, dpp<0x4E>(M2));
    float E = __builtin_amdgcn_exp2f(lf - M2);
    E += dpp<0xB1>(E); E += dpp<0x4E>(E);
    if (l < 32) *(LAS float*)(L + L_CF + l * 4) = __builtin_amdgcn_exp2f(mw - M2) * __builtin_amdgcn_rcpf(E);
    WAIT_TICK(t_in, TF);
#pragma nounroll
    for (int spin = 0; spin < SPIN_CAP; ++spin) {
      const v4u f0 = LD_SC1(WS_F + l * 16), f1 = LD_SC1(WS_F + 1024 + l * 16);
      if (__builtin_amdgcn_ballot_w64(((f0.x ^ tg) | (f0.y ^ th) | (f0.z ^ tg) | (f0.w ^ th) | (f1.x ^ tg) | (f1.y ^ th) | (f1.z ^ tg) | (f1.w ^ th)) != 0) == 0) break;
      asm volatile("s_sleep 16" ::: "memory");
    }
    LDSWAIT();
    STAMP(7);
    if (LATEWO) {
#pragma nounroll
      for (int spin = 0; spin < SPIN_CAP * 16; ++spin) {
        if (__builtin_amdgcn_ballot_w64(*(volatile const LAS u64*)(L + L_DONE + (l & 7) * 8) != tag) == 0) break;
        asm volatile("s_sleep 1" ::: "memory");
      }
      asm volatile("s_sleep %0" :: "n"(WOS) : "memory");
      DMA_LOOP("nt", make_srd(a.wo + oh * 128 * DM + on * 64, 127 * 1024 + 128), (unsigned)(l >> 3) * 1024u + (unsigned)(l & 7) * 16u, 8192, lbase + L_WO, 16);
      WGBAR();
      DRAIN();
      if (l == 0) *(volatile LAS u64*)(L + L_FLAG + 24) = tag;
      LDSWAIT();
      STAMP(8);
    }
    FLUSH();
    return;
  }

  const int sw = w - 2;
  unsigned tnext = ((t_in + T0) << 4) + sw * (DT / 8), dt = DT;
#define PACE_TO(j) do { asm volatile("s_waitcnt vmcnt(%0)" :: "n"(WIN - 1) : "memory"); WAIT_NEXT(tnext, dt); } while (0)
  const size_t base = ((size_t)(b * HKV + kvh) * SK + sp * 1024 + sw * 128) * 256;
  const v4u rk = make_srd(a.kc + base, 32768), rv = make_srd(a.vc + base, 32768);
  const int ko = ((l >> 2) & 3) * 256 + g * 64 + (l & 3) * 16, vo = l * 16;
  const int rot = (4 * sw + xr) & 31;
#define OFF(i) ((((i) + rot) & 31) * 1024)
#define LOAD_V(i) asm volatile("buffer_load_dwordx4 %0, %1, %2, %3 offen nt" : "+v"(kv[i]) : "v"(vo), "s"(rv), "s"(OFF(i)) : "memory")
  if (!LATEWO) {
    const v4u ro = make_srd(a.wo + (oh * 128 + sw * 16) * DM + on * 64, 15 * 1024 + 128);
    const unsigned oo = (unsigned)(l >> 3) * 1024u + (unsigned)(l & 7) * 16u;
    PACE_TO(0); dma_rows(ro, oo, 0, lbase + L_WO + sw * 2048);
    PACE_TO(1); dma_rows(ro, oo, 8192, lbase + L_WO + sw * 2048 + 1024);
  }
  if (TOUCH) {
    v4u r1 = rk, r2 = rv;
    r1.z = 16; r2.z = 16;
    DMA_LOOP("nt", r1, l * 16, 0, lbase + L_NIL, 1);
    DMA_LOOP("nt", r2, l * 16, 0, lbase + L_NIL, 1);
  }
  v4u kv[32];
  __builtin_amdgcn_sched_barrier(0);
#pragma unroll
  for (int i = 0; i < 32; ++i) {
    PACE_TO(2 + i);
    asm volatile("buffer_load_dwordx4 %0, %1, %2, %3 offen nt" : "=v"(kv[i]) : "v"(ko), "s"(rk), "s"(OFF(i)) : "memory");
    PSTAMP(100 + i);
    if (SLIP >= 0 && (i == WIN || i == 2 * WIN)) { const unsigned t1 = (now_ticks() - SLIP) << 4; if ((int)(t1 - tnext) > 0) tnext = t1 + sw * (DT / 16); }
    __builtin_amdgcn_sched_barrier(0);
  }
  STAMP(1);
  const u64 tag = read_tag(ws);
  const unsigned tg = (unsigned)tag;
#pragma nounroll
  for (int spin = 0; spin < SPIN_CAP * 16; ++spin) { if (*(volatile const LAS u64*)(L + L_FLAG) == tag) break; asm volatile("s_sleep 2" ::: "memory"); }
  asm volatile("" ::: "memory");
  STAMP(2);
  const v4u qf = *(const LAS v4u*)(L + L_Q + ((l >> 2) & 3) * 256 + g * 64 + (l & 3) * 16);
  dt = __builtin_amdgcn_readfirstlane(*(const LAS unsigned*)(L + L_FLAG + 16));
  __builtin_amdgcn_sched_barrier(0);
  constexpr float C2 = 0.088388347648318440f * 1.4426950408889634f;
  float sc[8];
  const int s = l & 3;
#pragma unroll
  for (int m = 0; m < 8; ++m) {
    asm volatile("s_waitcnt vmcnt(%4)" : "+v"(kv[4 * m]), "+v"(kv[4 * m + 1]), "+v"(kv[4 * m + 2]), "+v"(kv[4 * m + 3]) : "n"(28 - 4 * m + m) : "memory");
    f32x4 d[4];
#pragma unroll
    for (int r = 0; r < 4; ++r) d[r] = mfma32(kv[4 * m + r], qf, f32x4{0.f, 0.f, 0.f, 0.f});
#pragma unroll
    for (int r = 0; r < 4; ++r) {
      float t = dpp<0x00>(d[r][0]); t = dpp<0x55>(d[r][1]) + t; t = dpp<0xAA>(d[r][2]) + t; t = dpp<0xFF>(d[r][3]) + t;
      asm volatile("" : "+v"(t));
      sc[m] = (r == 0 || s == r) ? t : sc[m];
    }
    asm volatile("" : "+v"(sc[m]));
    __builtin_amdgcn_sched_barrier(0);
    PACE_TO(34 + m);
    LOAD_V(m);
    __builtin_amdgcn_sched_barrier(0);
  }
  STAMP(3);
  float mx = sc[0];
#pragma unroll
  for (int m = 1; m < 8; ++m) mx = fmaxf(mx, sc[m]);
  mx = fmaxf(mx, dpp<0xB1>(mx)); mx = fmaxf(mx, dpp<0x4E>(mx)); mx = xrow_max(mx);
  const float mneg = -mx * C2;
  float lsum = 0.f;
  unsigned pk[8];
#pragma unroll
  for (int m = 0; m < 8; ++m) { const float p = __builtin_amdgcn_exp2f(fmaf(sc[m], C2, mneg)); pk[m] = cvtpk(p, p); lsum += bf_lo(pk[m]); }
  lsum += dpp<0xB1>(lsum); lsum += dpp<0x4E>(lsum); lsum = xrow_sum(lsum);
  if (l < 16 && (l & 3) == 0) *(LAS v4u*)(L + L_ML + ((l >> 2) * 8 + sw) * 16) = v4u{__float_as_uint(mx * C2), __float_as_uint(lsum), tg, (unsigned)(tag >> 32)};
  const unsigned half = (l & 1) ? 0xffff0000u : 0x0000ffffu;
  const unsigned m0 = (l & 2) ? 0u : half, m1 = (l & 2) ? half : 0u;
  f32x4 o0 = {0.f, 0.f, 0.f, 0.f}, o1 = {0.f, 0.f, 0.f, 0.f};
#pragma unroll
  for (int j = 8; j < 40; ++j) {
    if (j < 32) { PACE_TO(34 + j); LOAD_V(j); __builtin_amdgcn_sched_barrier(0); }
    if (j == 31) { if (l == 0) *(volatile LAS u64*)(L + L_DONE + sw * 8) = tag; STAMP(11); }
    const int i = j - 8;
    const unsigned pb = (i & 3) == 0 ? dppu<0x00>(pk[i >> 2]) : (i & 3) == 1 ? dppu<0x55>(pk[i >> 2]) : (i & 3) == 2 ? dppu<0xAA>(pk[i >> 2]) : dppu<0xFF>(pk[i >> 2]);
    const v2u av = {pb & m0, pb & m1};
    asm volatile("s_waitcnt vmcnt(%1)" : "+v"(kv[i]) : "n"((j < 32 ? j : 31) - i) : "memory");
    o0 = mfma16(av, v2u{kv[i].x, kv[i].y}, o0);
    o1 = mfma16(av, v2u{kv[i].z, kv[i].w}, o1);
    __builtin_amdgcn_sched_barrier(0);
  }
  *(LAS f32x4*)(L + L_MRG + (sw * 4 + g) * 512 + (l & 15) * 32) = o0;
  *(LAS f32x4*)(L + L_MRG + (sw * 4 + g) * 512 + (l & 15) * 32 + 16) = o1;
  STAMP(4);
  LDSWAIT();
  WGBAR();
  if (sw < 4) {
    STAMP(5);
    const f32x4 c0 = *(const LAS f32x4*)(L + L_CF + sw * 32), c1 = *(const LAS f32x4*)(L + L_CF + sw * 32 + 16);
    f32x2 acc = {0.f, 0.f};
#pragma unroll
    for (int j = 0; j < 8; ++j) {
      const f32x2 ov = *(const LAS f32x2*)(L + L_MRG + (j * 4 + sw) * 512 + l * 8);
      const float e = j < 4 ? c0[j & 3] : c1[j & 3];
      acc.x = fmaf(e, ov.x, acc.x); acc.y = fmaf(e, ov.y, acc.y);
    }
    const unsigned pv = notag(cvtpk(acc.x, acc.y), tg);
    const v4u pq = {dppu<0x00>(pv), dppu<0x55>(pv), dppu<0xAA>(pv), dppu<0xFF>(pv)};
    if ((l & 3) == 0) ST_SC1(pq, WS_P + (((kvh * 4 + sw) * 8 + b) * 4 + sp) * 256 + l * 4);
    STAMP(6);
    f32x4 ff = {0.f, 0.f, 0.f, 0.f};
    float gm = 1.f;
    if (ffn2) { ff = *(const LAS f32x4*)(L + L_FF + sw * 1024 + l * 16); gm = *(const LAS float*)(L + L_G + (l & 7) * 4); }
    const int po = WS_P + (oh * 8 + 2 * sw) * 1024 + l * 16;
    asm volatile("s_sleep %0" :: "n"(PDELAY1) : "memory");
    v4u p0 = LD_SC1(po), p1 = LD_SC1(po + 1024);
    if (LATEWO) {
#pragma nounroll
      for (int spin = 0; spin < SPIN_CAP * 16; ++spin) { if (*(volatile const LAS u64*)(L + L_FLAG + 24) == tag) break; }
      asm volatile("" ::: "memory");
    }
    v4u wf[4];
#pragma unroll
    for (int k = 0; k < 4; ++k) wf[k] = tile_frag(L + L_WO, l, k, sw);
    __builtin_amdgcn_sched_barrier(0);
    if ((p0.y == tg) | (p0.w == tg) | (p1.y == tg) | (p1.w == tg)) poll2(rws, po, po + 1024, tg, p0, p1);
    *(LAS v4u*)(L + L_PG + (8 * sw + g) * 272 + (l & 15) * 16) = p0;
    *(LAS v4u*)(L + L_PG + (8 * sw + 4 + g) * 272 + (l & 15) * 16) = p1;
    STAMP(7);
    LDSWAIT();
    WGBAR();
    const int pb = l & 7, sl = (l >> 3) & 1;
    v4u pa[8];
#pragma unroll
    for (int k = 0; k < 8; ++k) pa[k] = *(const LAS v4u*)(L + L_PG + (pb * 4 + (k >> 2) * 2 + sl) * 272 + (k & 3) * 64 + g * 16);
    __builtin_amdgcn_sched_barrier(0);
    f32x4 da = {0.f, 0.f, 0.f, 0.f}, db = {0.f, 0.f, 0.f, 0.f};
#pragma unroll
    for (int k = 0; k < 4; ++k) { da = mfma32(wf[k], pa[k], da); db = mfma32(wf[k], pa[4 + k], db); }
    v4u r;
#pragma unroll
    for (int j = 0; j < 4; ++j) { const float t = da[j] + db[j]; r[j] = notag(__float_as_uint(fmaf(t + dpp<0x128>(t), gm, ff[j])), tg); }
    if (sl == 0) ST_SC1(r, WS_O + (((on * 4 + sw) * 4 + (pb >> 1)) * 32 + oh) * 128 + (pb & 1) * 64 + g * 16);
    STAMP(8);
    if (sw == 0) {
      const f32x4 fin = *(const LAS f32x4*)(L + L_FIN + (l & 3) * 16);
      const float gt = *(const LAS float*)(L + L_FIN + 64);
      const float g0 = (l & 12) == 12 ? 1.f : gt, g1 = l < 48 ? g0 : gt;
      const int oo = WS_O + (rr >> 1) * 4096 + (l >> 2) * 128 + (rr & 1) * 64 + (l & 3) * 16;
      v4u r0, r1;
      asm volatile("s_sleep %0" :: "n"(PDELAY2) : "memory");
      poll2(rws, oo, oo + 2048, tg, r0, r1);
      STAMP(9);
      f32x4 av = __builtin_bit_cast(f32x4, r0) * g0 + __builtin_bit_cast(f32x4, r1) * g1;
#pragma unroll
      for (int j = 0; j < 4; ++j) av[j] += dpp<0x124>(av[j]);
#pragma unroll
      for (int j = 0; j < 4; ++j) av[j] += dpp<0x128>(av[j]);
      {
        auto s0 = __builtin_amdgcn_permlane16_swap(__float_as_uint(av[0]), __float_as_uint(av[1]), false, false);
        auto s1 = __builtin_amdgcn_permlane16_swap(__float_as_uint(av[2]), __float_as_uint(av[3]), false, false);
        const float u0 = __uint_as_float(s0[0]) + __uint_as_float(s0[1]), u1 = __uint_as_float(s1[0]) + __uint_as_float(s1[1]);
        auto s2 = __builtin_amdgcn_permlane32_swap(__float_as_uint(u0), __float_as_uint(u1), false, false);
        const float v = __uint_as_float(s2[0]) + __uint_as_float(s2[1]);
        const float o = v + (g == 0 ? fin[0] : g == 1 ? fin[1] : g == 2 ? fin[2] : fin[3]);
        if ((l & 15) < 4) *(GAS unsigned short*)(a.out + rb * DM + rc * 16 + 4 * (l & 3) + g) = (unsigned short)cvtpk(o, o);
      }
      if (c == 0 && l == 0) *(GAS u64*)(ws + WS_E) = tag;
      STAMP(10);
      { unsigned m = 0;
        for (int i = PW0; i < PW1; ++i) m = max(m, *(volatile LAS unsigned*)(L + L_END + i * 4));
        unsigned tq = (t_in + 4000 + 4 * __builtin_amdgcn_readfirstlane(m)) << 4; WAIT_NEXT(tq, dt); }
    }
  }
  FLUSH();
}

extern "C" void kernel_launch(void* const* d_in, const int* in_sizes, int n_in, void* d_out, int out_size, void* d_ws, size_t ws_size,
                              hipStream_t stream) {
  Args a;
  a.x = (const unsigned short*)d_in[0];
  a.kc = (const unsigned char*)d_in[1];
  a.vc = (const unsigned char*)d_in[2];
  a.wq = (const unsigned short*)d_in[3];
  a.wo = (const unsigned short*)d_in[4];
  a.w1 = (const unsigned short*)d_in[6];
  a.b1 = (const unsigned short*)d_in[7];
  a.w2 = (const unsigned short*)d_in[8];
  a.b2 = (const unsigned short*)d_in[9];
  a.gw = (const unsigned short*)d_in[10];
  a.ws = (unsigned char*)d_ws;
  a.out = (unsigned short*)d_out;
  fused_decode<<<256, NWAVE * 64, 0, stream>>>(a);
}
```

```cpp
#define PROBE 21
#define PW0 2
#define PW1 3
#include <hip/hip_runtime.h>
#include <hip/hip_bf16.h>
#include <stdint.h>

typedef unsigned v4u __attribute__((ext_vector_type(4)));
typedef unsigned v2u __attribute__((ext_vector_type(2)));
typedef float f32x4 __attribute__((ext_vector_type(4)));
typedef float f32x2 __attribute__((ext_vector_type(2)));
typedef __bf16 bf16x8 __attribute__((ext_vector_type(8)));
typedef __bf16 bf16x2 __attribute__((ext_vector_type(2)));
typedef short s16x4 __attribute__((ext_vector_type(4)));
typedef unsigned long long u64;
#define GAS __attribute__((address_space(1)))
#define LAS __attribute__((address_space(3)))

constexpr int DM = 512, HKV = 8, SK = 4096, FF = 1792;
constexpr int AUX_SC1 = 16;
#define DT 480
#define T0 130
#define WIN 5
#define TQ 560
#define TH 480
#define TF 1500
#define GQ 6
#define GH 3
#define SHIFT 1
#define PDELAY1 12
#define PDELAY2 12
#define SKEW 20
#define LATEWO 1
#define WOD 35
constexpr int NS0 = LATEWO ? 0 : 2;
#define RMAP 0
#define EAL 18
#define EAS 18
#define CODEKB 16
#define CODEB 15360
#define TOUCH 0
#define WOS 10
#define SLIP 40
#define GSLIP 1120
#define GEND 0
constexpr int NWAVE = 10;

constexpr int WS_E = 0;
constexpr int WS_F = 2048;
constexpr int WS_QP = 4096;
constexpr int WS_HP = WS_QP + 8 * 8 * 2 * 512 * 8;
constexpr int WS_X = WS_HP + 7 * 8 * 2 * 256 * 8;
constexpr int WS_LSE = WS_X + 256 * 128;
constexpr int WS_END = WS_LSE + 256 * 128;
constexpr int WS_O = WS_QP;
constexpr int WS_P = WS_HP;
static_assert(WS_LSE - WS_P == 32 * 8 * 4 * 256, "the partials fill exactly the lines of HP and X");
constexpr int WS_ST = 8 << 20;
constexpr int L_WO = 0, L_WT = 16384, L_MRG = 49152, L_ML = L_MRG + 16384, L_CF = L_ML + 512, L_Q = L_CF + 128, L_X = L_Q + 1024, L_H = L_X + 8192, L_NIL = L_H + 8192, L_FF = L_NIL + 1024,
              L_G = L_FF + 4096, L_FIN = L_G + 32, L_FLAG = L_FIN + 96, L_DONE = L_FLAG + 32, L_END = L_DONE + 64;
constexpr int L_PG = L_WT;

__device__ __forceinline__ f32x4 mfma32(v4u a, v4u b, f32x4 c) {
  return __builtin_amdgcn_mfma_f32_16x16x32_bf16(__builtin_bit_cast(bf16x8, a), __builtin_bit_cast(bf16x8, b), c, 0, 0, 0);
}
__device__ __forceinline__ f32x4 mfma16(v2u a, v2u b, f32x4 c) {
  return __builtin_amdgcn_mfma_f32_16x16x16bf16_1k(__builtin_bit_cast(s16x4, a), __builtin_bit_cast(s16x4, b), c, 0, 0, 0);
}
__device__ __forceinline__ unsigned cvtpk(float lo, float hi) { bf16x2 r = {(__bf16)lo, (__bf16)hi}; return __builtin_bit_cast(unsigned, r); }
__device__ __forceinline__ float bf_lo(unsigned u) { return __builtin_bit_cast(float, u << 16); }
__device__ __forceinline__ float bf_hi(unsigned u) { return __builtin_bit_cast(float, u & 0xffff0000u); }
__device__ __forceinline__ float rbf(float x) { return (float)(__bf16)x; }
template <int CTRL> __device__ __forceinline__ float dpp(float x) { return __builtin_bit_cast(float, __builtin_amdgcn_mov_dpp(__builtin_bit_cast(int, x), CTRL, 0xf, 0xf, true)); }
template <int CTRL> __device__ __forceinline__ unsigned dppu(unsigned x) { return (unsigned)__builtin_amdgcn_mov_dpp((int)x, CTRL, 0xf, 0xf, true); }
__device__ __forceinline__ float xrow_max(float x) {
  auto s = __builtin_amdgcn_permlane16_swap(__float_as_uint(x), __float_as_uint(x), false, false);
  x = fmaxf(__uint_as_float(s[0]), __uint_as_float(s[1]));
  auto t = __builtin_amdgcn_permlane32_swap(__float_as_uint(x), __float_as_uint(x), false, false);
  return fmaxf(__uint_as_float(t[0]), __uint_as_float(t[1]));
}
__device__ __forceinline__ float xrow_sum(float x) {
  auto s = __builtin_amdgcn_permlane16_swap(__float_as_uint(x), __float_as_uint(x), false, false);
  x = __uint_as_float(s[0]) + __uint_as_float(s[1]);
  auto t = __builtin_amdgcn_permlane32_swap(__float_as_uint(x), __float_as_uint(x), false, false);
  return __uint_as_float(t[0]) + __uint_as_float(t[1]);
}
__device__ __forceinline__ v2u tr_read(const LAS unsigned char* p) { return __builtin_bit_cast(v2u, __builtin_amdgcn_ds_read_tr16_b64_v4i16((LAS s16x4*)p)); }
__device__ __forceinline__ v4u make_srd(const void* p, unsigned bytes) {
  const u64 a = (u64)p;
  return v4u{(unsigned)a, (unsigned)(a >> 32) & 0xffffu, bytes, 0x00020000u};
}
__device__ __forceinline__ void dma_rows(v4u srd, unsigned voff, unsigned soff, unsigned lds_addr) {
  unsigned keep;
  asm volatile("s_mov_b32 %0, m0\n\ts_mov_b32 m0, %1\n\ts_nop 0\n\tbuffer_load_dwordx4 %2, %3, %4 offen nt lds\n\ts_mov_b32 m0, %0"
               : "=&s"(keep) : "s"(lds_addr), "v"(voff), "s"(srd), "s"(soff) : "memory");
}
#define DMA_LOOP(POL, srd, voff, step, lds_addr, n) do { unsigned keep_, so_ = 0, n_ = (n); \
  asm volatile("s_cmp_eq_u32 %2, 0\n\ts_cbranch_scc1 2f\n\ts_mov_b32 %0, m0\n\ts_mov_b32 m0, %3\n\ts_nop 0\n1:\n\tbuffer_load_dwordx4 %4, %5, %1 offen " POL " lds\n\t" \
               "s_add_u32 m0, m0, 0x400\n\ts_add_u32 %1, %1, %6\n\ts_sub_u32 %2, %2, 1\n\ts_cmp_lg_u32 %2, 0\n\ts_cbranch_scc1 1b\n\ts_mov_b32 m0, %0\n2:" \
               : "=&s"(keep_), "+&s"(so_), "+&s"(n_) : "s"(lds_addr), "v"(voff), "s"(srd), "s"(step) : "scc", "memory"); } while (0)
__device__ __forceinline__ v4u tile_frag(const LAS unsigned char* tile, int l, int k, int ct) {
  const LAS unsigned char* p = tile + (8 * (l >> 4) + ((l & 15) >> 2)) * 128 + (l & 3) * 8 + k * 4096 + ct * 32;
  const v2u t0 = tr_read(p), t1 = tr_read(p + 512);
  return v4u{t0.x, t0.y, t1.x, t1.y};
}
#define DRAIN() asm volatile("s_waitcnt vmcnt(0)" ::: "memory")
#define LDSWAIT() asm volatile("s_waitcnt lgkmcnt(0)" ::: "memory")
#define WGBAR() do { __builtin_amdgcn_sched_barrier(0); __builtin_amdgcn_s_barrier(); __builtin_amdgcn_sched_barrier(0); asm volatile("" ::: "memory"); } while (0)
__device__ __forceinline__ u64 read_tag(const unsigned char* ws) {
  u64 t;
  asm volatile("s_load_dwordx2 %0, %1, 0x0\n\ts_waitcnt lgkmcnt(0)" : "=s"(t) : "s"(ws) : "memory");
  const unsigned old = (unsigned)t;
  t += 0x9E3779B97F4A7C15ull;
  t = (t ^ (t >> 30)) * 0xBF58476D1CE4E5B9ull; t = (t ^ (t >> 27)) * 0x94D049BB133111EBull;
  t ^= t >> 31;
  return (unsigned)t == old ? t ^ 1u : t;
}
__device__ __forceinline__ unsigned now_ticks() {
  u64 t;
  asm volatile("s_memrealtime %0\n\ts_waitcnt lgkmcnt(0)" : "=s"(t) :: "memory");
  return (unsigned)t;
}
#define WAIT_TICK(base, delta) \
  asm volatile("s_branch 2f\n1:\n\ts_sleep 1\n2:\n\ts_memrealtime vcc\n\ts_waitcnt lgkmcnt(0)\n\ts_sub_u32 vcc_lo, vcc_lo, %0\n\ts_cmpk_lt_i32 vcc_lo, %1\n\ts_cbranch_scc1 1b" \
               :: "s"(base), "n"(delta) : "scc", "vcc", "memory")
#define WAIT_NEXT(acc, step) \
  asm volatile("s_branch 2f\n1:\n\ts_sleep 1\n2:\n\ts_memrealtime vcc\n\ts_waitcnt lgkmcnt(0)\n\ts_lshl_b32 vcc_lo, vcc_lo, 4\n\ts_sub_u32 vcc_lo, vcc_lo, %0\n\ts_cmp_lt_i32 vcc_lo, 0\n\ts_cbranch_scc1 1b\n\ts_add_u32 %0, %0, %1" \
               : "+s"(acc) : "s"(step) : "scc", "vcc", "memory")
#define WAIT_SLIP(acc, step) \
  asm volatile("s_branch 2f\n1:\n\ts_sleep 1\n2:\n\ts_memrealtime vcc\n\ts_waitcnt lgkmcnt(0)\n\ts_lshl_b32 vcc_lo, vcc_lo, 4\n\ts_sub_u32 vcc_lo, vcc_lo, %0\n\ts_cmp_lt_i32 vcc_lo, 0\n\ts_cbranch_scc1 1b\n\t" \
               "s_addk_i32 vcc_lo, %2\n\ts_max_i32 vcc_lo, vcc_lo, 0\n\ts_add_u32 %0, %0, %1\n\ts_add_u32 %0, %0, vcc_lo" \
               : "+s"(acc) : "s"(step), "n"(-(GSLIP)) : "scc", "vcc", "memory")
constexpr int SPIN_CAP = 1 << 18;
#define LD_SC1(off) __builtin_amdgcn_raw_buffer_load_b128(rws, (off), 0, AUX_SC1)
#define ST_SC1(v, off) __builtin_amdgcn_raw_buffer_store_b128((v), rws, (off), 0, AUX_SC1)
__device__ __forceinline__ v4u gran2(float a, float b, unsigned tg) { return v4u{__float_as_uint(a), tg, __float_as_uint(b), tg}; }
__device__ __forceinline__ unsigned gbad(v4u g, unsigned tg) { return (g.y ^ tg) | (g.w ^ tg); }
__device__ __forceinline__ f32x2 gval(v4u g) { return f32x2{__uint_as_float(g.x), __uint_as_float(g.z)}; }
__device__ __forceinline__ v4u gran_d(f32x4 d, int l, unsigned tg) { return gran2((l & 8) ? d[2] : d[0], (l & 8) ? d[3] : d[1], tg); }

#define PSTAMP(k) STAMP(k)
#define STAMP(k) do { if ((k) == PROBE && w >= PW0 && w < PW1) { const unsigned t_ = now_ticks() - t_in; if (l == 0) *(volatile LAS unsigned*)(L + L_END + w * 4) = t_; LDSWAIT(); } } while (0)
#define FLUSH() do { } while (0)

__device__ __forceinline__ void poll2(__amdgpu_buffer_rsrc_t rws, int o0, int o1, unsigned tg, v4u& r0, v4u& r1) {
  bool need = true;
#pragma nounroll
  for (int spin = 0; spin < SPIN_CAP && need; ++spin) {
    r0 = LD_SC1(o0); r1 = LD_SC1(o1);
    need = (r0.y == tg) | (r0.w == tg) | (r1.y == tg) | (r1.w == tg);
    asm volatile("" ::: "memory");
  }
}
__device__ __forceinline__ unsigned notag(unsigned x, unsigned tg) { return x == tg ? x ^ 1u : x; }
__device__ __forceinline__ float gate_of(v4u xv, v4u gv) {
  float gs = 0.f;
#pragma unroll
  for (int j = 0; j < 4; ++j) { gs += rbf(bf_lo(xv[j]) * bf_lo(gv[j])); gs += rbf(bf_hi(xv[j]) * bf_hi(gv[j])); }
  gs += dpp<0xB1>(gs); gs += dpp<0x4E>(gs); gs += dpp<0x124>(gs); gs += dpp<0x128>(gs); gs = xrow_sum(gs);
  return rbf(1.f / (1.f + __expf(-rbf(gs))));
}

struct Args {
  const unsigned short *x, *wq, *wo, *w1, *b1, *w2, *b2, *gw;
  const unsigned char *kc, *vc;
  unsigned char* ws;
  unsigned short* out;
};

typedef unsigned v16u __attribute__((ext_vector_type(16)));
typedef unsigned v8u __attribute__((ext_vector_type(8)));

__global__ void __launch_bounds__(NWAVE * 64) fused_decode(Args a_) {
  v16u ka0; v8u ka1; u64 t_in64;
  asm volatile("s_load_dwordx16 %0, %3, 0x0\n\ts_load_dwordx8 %1, %3, 0x40\n\ts_memrealtime %2" : "=&s"(ka0), "=&s"(ka1), "=&s"(t_in64) : "s"(__builtin_amdgcn_kernarg_segment_ptr()));
  __shared__ __attribute__((aligned(16))) unsigned char smem[L_END + 64];
  LAS unsigned char* L = (LAS unsigned char*)smem;
  const unsigned lbase = (unsigned)(uintptr_t)L;
  const int tid = threadIdx.x, l = tid & 63, w = __builtin_amdgcn_readfirstlane(tid >> 6);
  const int c = blockIdx.x, xr = c >> 3, sp = xr & 3, kvh = xr >> 2, b = (c - kvh) & 7, i32 = b * 4 + sp;
  {
    u64 pc;
    asm volatile("s_getpc_b64 %0" : "=s"(pc));
    const v4u rc = make_srd((const void*)(pc & ~255ull), CODEB);
    DMA_LOOP("", rc, ((xr * 2 + w) & (CODEKB - 1)) * 1024 + l * 16, 0, lbase + L_NIL, (12u >> w) & 1u);
  }
  const int g = l >> 4;
  const int oh = (c & 7) * 4 + (xr >> 3), on = xr & 7;
  const bool ffn2 = xr >= 24 && (c & 7) < 7;
  const bool isq = xr < 16, side = xr < 23 || ffn2;
  const int tl = isq ? ((xr >> 1) * 8 + ((c + xr) & 7)) * 2 + (xr & 1) : (c & 7) * 7 + xr - 16;
  const int cb = ffn2 ? on : tl >> 1, kh = ffn2 ? c & 7 : tl & 1;
#define KP(v, i) ((u64)v[i] | ((u64)v[(i) + 1] << 32))
  if (__builtin_expect(w == 0, 1)) {
    const unsigned ldb = isq ? 8192u : ffn2 ? 1024u : (unsigned)FF * 2u;
    unsigned woff = (unsigned)kh * 256u * ldb + cb * 128, wlen = 255u * ldb + 128u, wstep = 8u * ldb, wvo = (unsigned)(l >> 3) * ldb + (unsigned)(l & 7) * 16u;
    unsigned nw = side ? 32 : 0;
    asm volatile("s_waitcnt lgkmcnt(0)" : "+s"(ka0), "+s"(woff), "+s"(wlen), "+s"(wstep), "+s"(nw), "+v"(wvo) :: "memory");
    u64 pq = KP(ka0, 2), p1 = KP(ka0, 6), p2 = KP(ka0, 10);
    asm volatile("" : "+s"(pq), "+s"(p1), "+s"(p2));
    DMA_LOOP("nt", make_srd((const unsigned char*)(isq ? pq : ffn2 ? p2 : p1) + woff, wlen), wvo, wstep, lbase + L_WT, nw);
    DMA_LOOP("", make_srd((const void*)KP(ka0, 0), 8192), l * 16, 1024, lbase + L_X, nw >> 2);
  }
  asm volatile("s_waitcnt lgkmcnt(0)" : "+s"(t_in64), "+s"(ka0), "+s"(ka1) :: "memory");
  const unsigned t_in = (unsigned)t_in64;
  Args a;
  a.x = (const unsigned short*)KP(ka0, 0); a.wq = (const unsigned short*)KP(ka0, 2); a.wo = (const unsigned short*)KP(ka0, 4); a.w1 = (const unsigned short*)KP(ka0, 6);
  a.b1 = (const unsigned short*)KP(ka0, 8); a.w2 = (const unsigned short*)KP(ka0, 10); a.b2 = (const unsigned short*)KP(ka0, 12); a.gw = (const unsigned short*)KP(ka0, 14);
  a.kc = (const unsigned char*)KP(ka1, 0); a.vc = (const unsigned char*)KP(ka1, 2); a.ws = (unsigned char*)KP(ka1, 4); a.out = (unsigned short*)KP(ka1, 6);
  unsigned char* ws = a.ws;
  if (l == 0) *(volatile LAS unsigned*)(L + L_END + w * 4) = 0;
  PSTAMP(21);
  const __amdgpu_buffer_rsrc_t rws = __builtin_amdgcn_make_buffer_rsrc((void*)ws, 0, WS_END, 0x00020000);
  const int rr = c, rb = rr & 7, rc = rr >> 3;

  if (w == 0) {
    if (side) {
      const u64 tag = read_tag(ws);
      const unsigned tg = (unsigned)tag;
      float og = 0.f;
      if (ffn2) {
        const unsigned bb0 = *(const GAS unsigned*)(a.b1 + kh * 256 + l * 2), bb1 = *(const GAS unsigned*)(a.b1 + kh * 256 + 128 + l * 2);
        const v4u gv = *(const GAS v4u*)(a.gw + l * 8);
        DRAIN();
        float gt = 0.f;
#pragma nounroll
        for (int j = 0; j < 8; ++j) { const float t = gate_of(*(const LAS v4u*)(L + L_X + j * 1024 + l * 16), gv); gt = (l & 7) == j ? t : gt; }
        if (l < 8) *(LAS float*)(L + L_G + l * 4) = gt;
        og = rbf(1.f - gt);
        WAIT_TICK(t_in, TH);
        int spin = 0;
#pragma nounroll
        for (int hf = 0; hf < 2;) {
          v4u t[16];
#pragma unroll
          for (int i = 0; i < 16; ++i) {
            t[i] = __builtin_amdgcn_raw_buffer_load_b128(rws, WS_HP + kh * 32768 + hf * 16384 + l * 16, i * 1024, EAL);
            asm volatile("s_sleep %0" :: "n"(GH) : "memory");
          }
          unsigned bad = 0;
#pragma unroll
          for (int j = 0; j < 8; ++j) {
            const int i0 = (j >> 1) * 4 + (j & 1);
            const unsigned bb = (j & 1) ? bb1 : bb0;
            bad |= gbad(t[i0], tg) | gbad(t[i0 + 2], tg);
            const f32x2 z = gval(t[i0]) + gval(t[i0 + 2]);
            const float h0 = rbf(rbf(z.x) + bf_lo(bb)), h1 = rbf(rbf(z.y) + bf_hi(bb));
            const float r0 = h0 > 0.f ? h0 : 0.f, r1 = h1 > 0.f ? h1 : 0.f;
            *(LAS unsigned*)(L + L_H + (hf * 4 + (j >> 1)) * 1024 + (j & 1) * 256 + l * 4) = cvtpk(r0 * r0, r1 * r1);
          }
          if (__builtin_amdgcn_ballot_w64(bad != 0) == 0 || ++spin > SPIN_CAP) ++hf;
        }
        LDSWAIT();
        if (l == 0) *(volatile LAS u64*)(L + L_FLAG + 8) = tag;
      }
      DRAIN();
      STAMP(1);
      PSTAMP(23);
      v4u xb[8];
      const LAS unsigned char* xp = L + (ffn2 ? L_H : L_X + kh * 512) + (l & 7) * 1024 + g * 16;
#pragma unroll
      for (int k = 0; k < 8; ++k) xb[k] = *(const LAS v4u*)(xp + k * 64);
      const int off = (isq ? WS_QP + (cb >> 3) * 65536 + (l & 7) * 8192 + kh * 4096 + (cb & 7) * 512 : WS_HP + (cb >> 2) * 32768 + (l & 7) * 4096 + kh * 2048 + (cb & 3) * 512)
                      + g * 32 + (l & 8) * 2;
#pragma nounroll
      for (int ct = 0; ct < 4; ++ct) {
        f32x4 d = {0.f, 0.f, 0.f, 0.f};
#pragma unroll
        for (int k = 0; k < 8; ++k) d = mfma32(tile_frag(L + L_WT, l, k, ct), xb[k], d);
        if (ffn2) *(LAS f32x4*)(L + L_FF + ct * 1024 + l * 16) = d * og;
        else __builtin_amdgcn_raw_buffer_store_b128(gran_d(d, l, tg), rws, off + ct * 128, 0, EAS);
      }
      LDSWAIT();
      PSTAMP(24);
    }
    STAMP(2);
    FLUSH();
    return;
  }
  if (w == 1) {
    const v4u xv = *(const GAS v4u*)(a.x + rb * DM + l * 8), gv = *(const GAS v4u*)(a.gw + l * 8);
    const v2u bb = *(const GAS v2u*)(a.b2 + rc * 16 + 4 * (l & 3));
    if (TOUCH) {
      const v4u rt = make_srd(a.wo + oh * 128 * DM + on * 64, 16);
      DMA_LOOP("nt", rt, l * 16, 0, lbase + L_NIL, 1);
      const v4u ru = make_srd(a.out, 16);
      DMA_LOOP("nt", ru, l * 16, 0, lbase + L_NIL, 1);
    }
    const u64 tag = read_tag(ws);
    const unsigned tg = (unsigned)tag, th = (unsigned)(tag >> 32);
    if (l < 8) __builtin_amdgcn_raw_buffer_store_b128(v4u{t_in, tg, t_in, tg}, rws, WS_X + c * 128 + l * 16, 0, EAS);
    {
      const float gt = gate_of(xv, gv), og = rbf(1.f - gt);
      if (l < 4) *(LAS f32x4*)(L + L_FIN + l * 16) = f32x4{bf_lo(bb.x), bf_hi(bb.x), bf_lo(bb.y), bf_hi(bb.y)} * og;
      if (l == 0) *(LAS float*)(L + L_FIN + 64) = gt;
    }
    WAIT_TICK(t_in, TQ);
    const v2u st = __builtin_amdgcn_raw_buffer_load_b64(rws, WS_X + (l & 7) * 128, 0, AUX_SC1);
#pragma nounroll
    for (int spin = 0; spin < SPIN_CAP; ++spin) {
      v4u t[8];
#pragma unroll
      for (int i = 0; i < 8; ++i) { asm volatile("s_sleep %0" :: "n"(GQ) : "memory"); t[i] = __builtin_amdgcn_raw_buffer_load_b128(rws, WS_QP + kvh * 65536 + b * 8192 + i * 1024 + l * 16, 0, EAL); }
      unsigned bad = 0;
#pragma unroll
      for (int j = 0; j < 4; ++j) {
        bad |= gbad(t[j], tg) | gbad(t[4 + j], tg);
        const f32x2 q = gval(t[j]) + gval(t[4 + j]);
        *(LAS unsigned*)(L + L_Q + j * 256 + l * 4) = cvtpk(q.x, q.y);
      }
      if (__builtin_amdgcn_ballot_w64(bad != 0) == 0) break;
      asm volatile("s_sleep 8" ::: "memory");
    }
    unsigned dtv = DT;
    {
      int late = st.y == tg ? (int)(t_in - st.x) : 0;
      late = max(late, (int)dppu<0xB1>((unsigned)late)); late = max(late, (int)dppu<0x4E>((unsigned)late)); late = max(late, (int)dppu<0x141>((unsigned)late));
      late = min(late, 100);
      if (SKEW >= 0) dtv = __builtin_amdgcn_readfirstlane(DT - (late - SKEW) / 2);
      if (l == 0) *(LAS unsigned*)(L + L_FLAG + 16) = dtv;
    }
    LDSWAIT();
    if (l == 0) *(volatile LAS u64*)(L + L_FLAG) = tag;
    STAMP(3);
    if (ffn2) {
#pragma nounroll
      for (int spin = 0; spin < SPIN_CAP * 16; ++spin) { if (*(volatile const LAS u64*)(L + L_FLAG + 8) == tag) break; asm volatile("s_sleep 8" ::: "memory"); }
    }
    DRAIN();
    if (l == 0) __builtin_amdgcn_raw_buffer_store_b64(v2u{tg, th}, rws, WS_F + c * 8, 0, AUX_SC1);
    STAMP(5);
    v4u ml;
#pragma nounroll
    for (int spin = 0; spin < SPIN_CAP * 16; ++spin) {
      ml = *(volatile const LAS v4u*)(L + L_ML + (l & 31) * 16);
      if (__builtin_amdgcn_ballot_w64(((ml.z ^ tg) | (ml.w ^ th)) != 0) == 0) break;
      asm volatile("s_sleep 4" ::: "memory");
    }
    const float mw = __uint_as_float(ml.x);
    float M = fmaxf(mw, dpp<0xB1>(mw)); M = fmaxf(M, dpp<0x4E>(M)); M = fmaxf(M, dpp<0x141>(M));
    float Ls = __builtin_amdgcn_exp2f(mw - M) * __uint_as_float(ml.y);
    Ls += dpp<0xB1>(Ls); Ls += dpp<0x4E>(Ls); Ls += dpp<0x141>(Ls);
    const float lse = M + __builtin_amdgcn_logf(Ls);
    const v4u lg = gran2(lse, dpp<0x128>(lse), tg);
    if (l < 32 && (l & 15) == 0) ST_SC1(lg, WS_LSE + ((b * 8 + kvh) * 4 + sp) * 128 + l);
    STAMP(6);
    v2u ls;
    asm volatile("s_sleep 16" ::: "memory");
#pragma nounroll
    for (int spin = 0; spin < SPIN_CAP; ++spin) {
      ls = __builtin_amdgcn_raw_buffer_load_b64(rws, WS_LSE + ((b * 8 + kvh) * 4 + (l & 3)) * 128 + ((l >> 3) & 3) * 8, 0, AUX_SC1);
      if (__builtin_amdgcn_ballot_w64(ls.y != tg) == 0) break;
      asm volatile("s_sleep 8" ::: "memory");
    }
    const float lf = __uint_as_float(ls.x);
    float M2 = fmaxf(lf, dpp<0xB1>(lf)); M2 = fmaxf(M2, dpp<0x4E>(M2));
    float E = __builtin_amdgcn_exp2f(lf - M2);
    E += dpp<0xB1>(E); E += dpp<0x4E>(E);
    if (l < 32) *(LAS float*)(L + L_CF + l * 4) = __builtin_amdgcn_exp2f(mw - M2) * __builtin_amdgcn_rcpf(E);
    WAIT_TICK(t_in, TF);
#pragma nounroll
    for (int spin = 0; spin < SPIN_CAP; ++spin) {
      const v4u f0 = LD_SC1(WS_F + l * 16), f1 = LD_SC1(WS_F + 1024 + l * 16);
      if (__builtin_amdgcn_ballot_w64(((f0.x ^ tg) | (f0.y ^ th) | (f0.z ^ tg) | (f0.w ^ th) | (f1.x ^ tg) | (f1.y ^ th) | (f1.z ^ tg) | (f1.w ^ th)) != 0) == 0) break;
      asm volatile("s_sleep 16" ::: "memory");
    }
    LDSWAIT();
    STAMP(7);
    if (LATEWO) {
#pragma nounroll
      for (int spin = 0; spin < SPIN_CAP * 16; ++spin) {
        if (__builtin_amdgcn_ballot_w64(*(volatile const LAS u64*)(L + L_DONE + (l & 7) * 8) != tag) == 0) break;
        asm volatile("s_sleep 1" ::: "memory");
      }
      asm volatile("s_sleep %0" :: "n"(WOS) : "memory");
      DMA_LOOP("nt", make_srd(a.wo + oh * 128 * DM + on * 64, 127 * 1024 + 128), (unsigned)(l >> 3) * 1024u + (unsigned)(l & 7) * 16u, 8192, lbase + L_WO, 16);
      WGBAR();
      DRAIN();
      if (l == 0) *(volatile LAS u64*)(L + L_FLAG + 24) = tag;
      LDSWAIT();
      STAMP(8);
    }
    FLUSH();
    return;
  }

  const int sw = w - 2;
  unsigned tnext = ((t_in + T0) << 4) + sw * (DT / 8), dt = DT;
#define PACE_TO(j) do { asm volatile("s_waitcnt vmcnt(%0)" :: "n"(WIN - 1) : "memory"); if ((j) < GEND) WAIT_SLIP(tnext, dt); else WAIT_NEXT(tnext, dt); } while (0)
  const size_t base = ((size_t)(b * HKV + kvh) * SK + sp * 1024 + sw * 128) * 256;
  const v4u rk = make_srd(a.kc + base, 32768), rv = make_srd(a.vc + base, 32768);
  const int ko = ((l >> 2) & 3) * 256 + g * 64 + (l & 3) * 16, vo = l * 16;
  const int rot = (4 * sw + xr) & 31;
#define OFF(i) ((((i) + rot) & 31) * 1024)
#define LOAD_V(i) asm volatile("buffer_load_dwordx4 %0, %1, %2, %3 offen nt" : "+v"(kv[i]) : "v"(vo), "s"(rv), "s"(OFF(i)) : "memory")
  if (!LATEWO) {
    const v4u ro = make_srd(a.wo + (oh * 128 + sw * 16) * DM + on * 64, 15 * 1024 + 128);
    const unsigned oo = (unsigned)(l >> 3) * 1024u + (unsigned)(l & 7) * 16u;
    PACE_TO(0); dma_rows(ro, oo, 0, lbase + L_WO + sw * 2048);
    PACE_TO(1); dma_rows(ro, oo, 8192, lbase + L_WO + sw * 2048 + 1024);
  }
  if (TOUCH) {
    v4u r1 = rk, r2 = rv;
    r1.z = 16; r2.z = 16;
    DMA_LOOP("nt", r1, l * 16, 0, lbase + L_NIL, 1);
    DMA_LOOP("nt", r2, l * 16, 0, lbase + L_NIL, 1);
  }
  v4u kv[32];
  __builtin_amdgcn_sched_barrier(0);
#pragma unroll
  for (int i = 0; i < 32; ++i) {
    PACE_TO(2 + i);
    asm volatile("buffer_load_dwordx4 %0, %1, %2, %3 offen nt" : "=v"(kv[i]) : "v"(ko), "s"(rk), "s"(OFF(i)) : "memory");
    PSTAMP(100 + i);
    if (SLIP >= 0 && (i == WIN || i == 2 * WIN)) { const unsigned t1 = (now_ticks() - SLIP) << 4; if ((int)(t1 - tnext) > 0) tnext = t1 + sw * (DT / 16); }
    __builtin_amdgcn_sched_barrier(0);
  }
  STAMP(1);
  const u64 tag = read_tag(ws);
  const unsigned tg = (unsigned)tag;
#pragma nounroll
  for (int spin = 0; spin < SPIN_CAP * 16; ++spin) { if (*(volatile const LAS u64*)(L + L_FLAG) == tag) break; asm volatile("s_sleep 2" ::: "memory"); }
  asm volatile("" ::: "memory");
  STAMP(2);
  const v4u qf = *(const LAS v4u*)(L + L_Q + ((l >> 2) & 3) * 256 + g * 64 + (l & 3) * 16);
  dt = __builtin_amdgcn_readfirstlane(*(const LAS unsigned*)(L + L_FLAG + 16));
  __builtin_amdgcn_sched_barrier(0);
  constexpr float C2 = 0.088388347648318440f * 1.4426950408889634f;
  float sc[8];
  const int s = l & 3;
#pragma unroll
  for (int m = 0; m < 8; ++m) {
    asm volatile("s_waitcnt vmcnt(%4)" : "+v"(kv[4 * m]), "+v"(kv[4 * m + 1]), "+v"(kv[4 * m + 2]), "+v"(kv[4 * m + 3]) : "n"(28 - 4 * m + m) : "memory");
    f32x4 d[4];
#pragma unroll
    for (int r = 0; r < 4; ++r) d[r] = mfma32(kv[4 * m + r], qf, f32x4{0.f, 0.f, 0.f, 0.f});
#pragma unroll
    for (int r = 0; r < 4; ++r) {
      float t = dpp<0x00>(d[r][0]); t = dpp<0x55>(d[r][1]) + t; t = dpp<0xAA>(d[r][2]) + t; t = dpp<0xFF>(d[r][3]) + t;
      asm volatile("" : "+v"(t));
      sc[m] = (r == 0 || s == r) ? t : sc[m];
    }
    asm volatile("" : "+v"(sc[m]));
    __builtin_amdgcn_sched_barrier(0);
    PACE_TO(34 + m);
    LOAD_V(m);
    __builtin_amdgcn_sched_barrier(0);
  }
  STAMP(3);
  float mx = sc[0];
#pragma unroll
  for (int m = 1; m < 8; ++m) mx = fmaxf(mx, sc[m]);
  mx = fmaxf(mx, dpp<0xB1>(mx)); mx = fmaxf(mx, dpp<0x4E>(mx)); mx = xrow_max(mx);
  const float mneg = -mx * C2;
  float lsum = 0.f;
  unsigned pk[8];
#pragma unroll
  for (int m = 0; m < 8; ++m) { const float p = __builtin_amdgcn_exp2f(fmaf(sc[m], C2, mneg)); pk[m] = cvtpk(p, p); lsum += bf_lo(pk[m]); }
  lsum += dpp<0xB1>(lsum); lsum += dpp<0x4E>(lsum); lsum = xrow_sum(lsum);
  if (l < 16 && (l & 3) == 0) *(LAS v4u*)(L + L_ML + ((l >> 2) * 8 + sw) * 16) = v4u{__float_as_uint(mx * C2), __float_as_uint(lsum), tg, (unsigned)(tag >> 32)};
  const unsigned half = (l & 1) ? 0xffff0000u : 0x0000ffffu;
  const unsigned m0 = (l & 2) ? 0u : half, m1 = (l & 2) ? half : 0u;
  f32x4 o0 = {0.f, 0.f, 0.f, 0.f}, o1 = {0.f, 0.f, 0.f, 0.f};
#pragma unroll
  for (int j = 8; j < 40; ++j) {
    if (j < 32) { PACE_TO(34 + j); LOAD_V(j); __builtin_amdgcn_sched_barrier(0); }
    if (j == 31) { if (l == 0) *(volatile LAS u64*)(L + L_DONE + sw * 8) = tag; STAMP(11); }
    const int i = j - 8;
    const unsigned pb = (i & 3) == 0 ? dppu<0x00>(pk[i >> 2]) : (i & 3) == 1 ? dppu<0x55>(pk[i >> 2]) : (i & 3) == 2 ? dppu<0xAA>(pk[i >> 2]) : dppu<0xFF>(pk[i >> 2]);
    const v2u av = {pb & m0, pb & m1};
    asm volatile("s_waitcnt vmcnt(%1)" : "+v"(kv[i]) : "n"((j < 32 ? j : 31) - i) : "memory");
    o0 = mfma16(av, v2u{kv[i].x, kv[i].y}, o0);
    o1 = mfma16(av, v2u{kv[i].z, kv[i].w}, o1);
    __builtin_amdgcn_sched_barrier(0);
  }
  *(LAS f32x4*)(L + L_MRG + (sw * 4 + g) * 512 + (l & 15) * 32) = o0;
  *(LAS f32x4*)(L + L_MRG + (sw * 4 + g) * 512 + (l & 15) * 32 + 16) = o1;
  STAMP(4);
  LDSWAIT();
  WGBAR();
  if (sw < 4) {
    STAMP(5);
    const f32x4 c0 = *(const LAS f32x4*)(L + L_CF + sw * 32), c1 = *(const LAS f32x4*)(L + L_CF + sw * 32 + 16);
    f32x2 acc = {0.f, 0.f};
#pragma unroll
    for (int j = 0; j < 8; ++j) {
      const f32x2 ov = *(const LAS f32x2*)(L + L_MRG + (j * 4 + sw) * 512 + l * 8);
      const float e = j < 4 ? c0[j & 3] : c1[j & 3];
      acc.x = fmaf(e, ov.x, acc.x); acc.y = fmaf(e, ov.y, acc.y);
    }
    const unsigned pv = notag(cvtpk(acc.x, acc.y), tg);
    const v4u pq = {dppu<0x00>(pv), dppu<0x55>(pv), dppu<0xAA>(pv), dppu<0xFF>(pv)};
    if ((l & 3) == 0) ST_SC1(pq, WS_P + (((kvh * 4 + sw) * 8 + b) * 4 + sp) * 256 + l * 4);
    STAMP(6);
    f32x4 ff = {0.f, 0.f, 0.f, 0.f};
    float gm = 1.f;
    if (ffn2) { ff = *(const LAS f32x4*)(L + L_FF + sw * 1024 + l * 16); gm = *(const LAS float*)(L + L_G + (l & 7) * 4); }
    const int po = WS_P + (oh * 8 + 2 * sw) * 1024 + l * 16;
    asm volatile("s_sleep %0" :: "n"(PDELAY1) : "memory");
    v4u p0 = LD_SC1(po), p1 = LD_SC1(po + 1024);
    if (LATEWO) {
#pragma nounroll
      for (int spin = 0; spin < SPIN_CAP * 16; ++spin) { if (*(volatile const LAS u64*)(L + L_FLAG + 24) == tag) break; }
      asm volatile("" ::: "memory");
    }
    v4u wf[4];
#pragma unroll
    for (int k = 0; k < 4; ++k) wf[k] = tile_frag(L + L_WO, l, k, sw);
    __builtin_amdgcn_sched_barrier(0);
    if ((p0.y == tg) | (p0.w == tg) | (p1.y == tg) | (p1.w == tg)) poll2(rws, po, po + 1024, tg, p0, p1);
    *(LAS v4u*)(L + L_PG + (8 * sw + g) * 272 + (l & 15) * 16) = p0;
    *(LAS v4u*)(L + L_PG + (8 * sw + 4 + g) * 272 + (l & 15) * 16) = p1;
    STAMP(7);
    LDSWAIT();
    WGBAR();
    const int pb = l & 7, sl = (l >> 3) & 1;
    v4u pa[8];
#pragma unroll
    for (int k = 0; k < 8; ++k) pa[k] = *(const LAS v4u*)(L + L_PG + (pb * 4 + (k >> 2) * 2 + sl) * 272 + (k & 3) * 64 + g * 16);
    __builtin_amdgcn_sched_barrier(0);
    f32x4 da = {0.f, 0.f, 0.f, 0.f}, db = {0.f, 0.f, 0.f, 0.f};
#pragma unroll
    for (int k = 0; k < 4; ++k) { da = mfma32(wf[k], pa[k], da); db = mfma32(wf[k], pa[4 + k], db); }
    v4u r;
#pragma unroll
    for (int j = 0; j < 4; ++j) { const float t = da[j] + db[j]; r[j] = notag(__float_as_uint(fmaf(t + dpp<0x128>(t), gm, ff[j])), tg); }
    if (sl == 0) ST_SC1(r, WS_O + (((on * 4 + sw) * 4 + (pb >> 1)) * 32 + oh) * 128 + (pb & 1) * 64 + g * 16);
    STAMP(8);
    if (sw == 0) {
      const f32x4 fin = *(const LAS f32x4*)(L + L_FIN + (l & 3) * 16);
      const float gt = *(const LAS float*)(L + L_FIN + 64);
      const float g0 = (l & 12) == 12 ? 1.f : gt, g1 = l < 48 ? g0 : gt;
      const int oo = WS_O + (rr >> 1) * 4096 + (l >> 2) * 128 + (rr & 1) * 64 + (l & 3) * 16;
      v4u r0, r1;
      asm volatile("s_sleep %0" :: "n"(PDELAY2) : "memory");
      poll2(rws, oo, oo + 2048, tg, r0, r1);
      STAMP(9);
      f32x4 av = __builtin_bit_cast(f32x4, r0) * g0 + __builtin_bit_cast(f32x4, r1) * g1;
#pragma unroll
      for (int j = 0; j < 4; ++j) av[j] += dpp<0x124>(av[j]);
#pragma unroll
      for (int j = 0; j < 4; ++j) av[j] += dpp<0x128>(av[j]);
      {
        auto s0 = __builtin_amdgcn_permlane16_swap(__float_as_uint(av[0]), __float_as_uint(av[1]), false, false);
        auto s1 = __builtin_amdgcn_permlane16_swap(__float_as_uint(av[2]), __float_as_uint(av[3]), false, false);
        const float u0 = __uint_as_float(s0[0]) + __uint_as_float(s0[1]), u1 = __uint_as_float(s1[0]) + __uint_as_float(s1[1]);
        auto s2 = __builtin_amdgcn_permlane32_swap(__float_as_uint(u0), __float_as_uint(u1), false, false);
        const float v = __uint_as_float(s2[0]) + __uint_as_float(s2[1]);
        const float o = v + (g == 0 ? fin[0] : g == 1 ? fin[1] : g == 2 ? fin[2] : fin[3]);
        if ((l & 15) < 4) *(GAS unsigned short*)(a.out + rb * DM + rc * 16 + 4 * (l & 3) + g) = (unsigned short)cvtpk(o, o);
      }
      if (c == 0 && l == 0) *(GAS u64*)(ws + WS_E) = tag;
      STAMP(10);
      { unsigned m = 0;
        for (int i = PW0; i < PW1; ++i) m = max(m, *(volatile LAS unsigned*)(L + L_END + i * 4));
        m = __builtin_amdgcn_readfirstlane(m);
        unsigned tq = (t_in + 4000 + 4 * m) << 4; WAIT_NEXT(tq, dt); }
    }
  }
  FLUSH();
}

extern "C" void kernel_launch(void* const* d_in, const int* in_sizes, int n_in, void* d_out, int out_size, void* d_ws, size_t ws_size,
                              hipStream_t stream) {
  Args a;
  a.x = (const unsigned short*)d_in[0];
  a.kc = (const unsigned char*)d_in[1];
  a.vc = (const unsigned char*)d_in[2];
  a.wq = (const unsigned short*)d_in[3];
  a.wo = (const unsigned short*)d_in[4];
  a.w1 = (const unsigned short*)d_in[6];
  a.b1 = (const unsigned short*)d_in[7];
  a.w2 = (const unsigned short*)d_in[8];
  a.b2 = (const unsigned short*)d_in[9];
  a.gw = (const unsigned short*)d_in[10];
  a.ws = (unsigned char*)d_ws;
  a.out = (unsigned short*)d_out;
  fused_decode<<<256, NWAVE * 64, 0, stream>>>(a);
}
```

```cpp
#define DT 465
#define DTV 440
#include <hip/hip_runtime.h>
#include <hip/hip_bf16.h>
#include <stdint.h>

typedef unsigned v4u __attribute__((ext_vector_type(4)));
typedef unsigned v2u __attribute__((ext_vector_type(2)));
typedef float f32x4 __attribute__((ext_vector_type(4)));
typedef float f32x2 __attribute__((ext_vector_type(2)));
typedef __bf16 bf16x8 __attribute__((ext_vector_type(8)));
typedef __bf16 bf16x2 __attribute__((ext_vector_type(2)));
typedef short s16x4 __attribute__((ext_vector_type(4)));
typedef unsigned long long u64;
#define GAS __attribute__((address_space(1)))
#define LAS __attribute__((address_space(3)))

constexpr int DM = 512, HKV = 8, SK = 4096, FF = 1792;
constexpr int AUX_SC1 = 16;
#define T0 130
#define WIN 5
#define TQ 560
#define TH 480
#define TF 1500
#define GQ 6
#define GH 3
#define SHIFT 1
#define PDELAY1 13
#define PDELAY2 15
#define SKEW 20
#define LATEWO 1
#define WOD 35
constexpr int NS0 = LATEWO ? 0 : 2;
#define RMAP 0
#define EAL 18
#define EAS 18
#define CODEKB 16
#define CODEB 15360
#define TOUCH 0
#define WOS 6
#define SLIP 40
#define GSLIP 1120
#define GEND 0
#define WOPRE 16
#define ROTW 0
#define ROTF (16*(sw&1)+8*((sw>>1)&1)+4*(sw>>2)+9*(sp&1)+28*(sp>>1)+30*(b&1))
#define ROTX 1
#define ROTC 0
#define STAG 1
constexpr int NWAVE = 10;

constexpr int WS_E = 0;
constexpr int WS_F = 2048;
constexpr int WS_QP = 4096;
constexpr int WS_HP = WS_QP + 8 * 8 * 2 * 512 * 8;
constexpr int WS_X = WS_HP + 7 * 8 * 2 * 256 * 8;
constexpr int WS_LSE = WS_X + 256 * 128;
constexpr int WS_END = WS_LSE + 256 * 128;
constexpr int WS_O = WS_QP;
constexpr int WS_P = WS_HP;
static_assert(WS_LSE - WS_P == 32 * 8 * 4 * 256, "the partials fill exactly the lines of HP and X");
constexpr int WS_ST = 8 << 20;
constexpr int L_WO = 0, L_WT = 16384, L_MRG = 49152, L_ML = L_MRG + 16384, L_CF = L_ML + 512, L_Q = L_CF + 128, L_X = L_Q + 1024, L_H = L_X + 8192, L_NIL = L_H + 8192, L_FF = L_NIL + 1024,
              L_G = L_FF + 4096, L_FIN = L_G + 32, L_FLAG = L_FIN + 96, L_DONE = L_FLAG + 32, L_END = L_DONE + 64;
constexpr int L_PG = L_WT;

__device__ __forceinline__ f32x4 mfma32(v4u a, v4u b, f32x4 c) {
  return __builtin_amdgcn_mfma_f32_16x16x32_bf16(__builtin_bit_cast(bf16x8, a), __builtin_bit_cast(bf16x8, b), c, 0, 0, 0);
}
__device__ __forceinline__ f32x4 mfma16(v2u a, v2u b, f32x4 c) {
  return __builtin_amdgcn_mfma_f32_16x16x16bf16_1k(__builtin_bit_cast(s16x4, a), __builtin_bit_cast(s16x4, b), c, 0, 0, 0);
}
__device__ __forceinline__ unsigned cvtpk(float lo, float hi) { bf16x2 r = {(__bf16)lo, (__bf16)hi}; return __builtin_bit_cast(unsigned, r); }
__device__ __forceinline__ float bf_lo(unsigned u) { return __builtin_bit_cast(float, u << 16); }
__device__ __forceinline__ float bf_hi(unsigned u) { return __builtin_bit_cast(float, u & 0xffff0000u); }
__device__ __forceinline__ float rbf(float x) { return (float)(__bf16)x; }
template <int CTRL> __device__ __forceinline__ float dpp(float x) { return __builtin_bit_cast(float, __builtin_amdgcn_mov_dpp(__builtin_bit_cast(int, x), CTRL, 0xf, 0xf, true)); }
template <int CTRL> __device__ __forceinline__ unsigned dppu(unsigned x) { return (unsigned)__builtin_amdgcn_mov_dpp((int)x, CTRL, 0xf, 0xf, true); }
__device__ __forceinline__ float xrow_max(float x) {
  auto s = __builtin_amdgcn_permlane16_swap(__float_as_uint(x), __float_as_uint(x), false, false);
  x = fmaxf(__uint_as_float(s[0]), __uint_as_float(s[1]));
  auto t = __builtin_amdgcn_permlane32_swap(__float_as_uint(x), __float_as_uint(x), false, false);
  return fmaxf(__uint_as_float(t[0]), __uint_as_float(t[1]));
}
__device__ __forceinline__ float xrow_sum(float x) {
  auto s = __builtin_amdgcn_permlane16_swap(__float_as_uint(x), __float_as_uint(x), false, false);
  x = __uint_as_float(s[0]) + __uint_as_float(s[1]);
  auto t = __builtin_amdgcn_permlane32_swap(__float_as_uint(x), __float_as_uint(x), false, false);
  return __uint_as_float(t[0]) + __uint_as_float(t[1]);
}
__device__ __forceinline__ v2u tr_read(const LAS unsigned char* p) { return __builtin_bit_cast(v2u, __builtin_amdgcn_ds_read_tr16_b64_v4i16((LAS s16x4*)p)); }
__device__ __forceinline__ v4u make_srd(const void* p, unsigned bytes) {
  const u64 a = (u64)p;
  return v4u{(unsigned)a, (unsigned)(a >> 32) & 0xffffu, bytes, 0x00020000u};
}
__device__ __forceinline__ void dma_rows(v4u srd, unsigned voff, unsigned soff, unsigned lds_addr) {
  unsigned keep;
  asm volatile("s_mov_b32 %0, m0\n\ts_mov_b32 m0, %1\n\ts_nop 0\n\tbuffer_load_dwordx4 %2, %3, %4 offen nt lds\n\ts_mov_b32 m0, %0"
               : "=&s"(keep) : "s"(lds_addr), "v"(voff), "s"(srd), "s"(soff) : "memory");
}
#define DMA_LOOP(POL, srd, voff, step, lds_addr, n) do { unsigned keep_, so_ = 0, n_ = (n); \
  asm volatile("s_cmp_eq_u32 %2, 0\n\ts_cbranch_scc1 2f\n\ts_mov_b32 %0, m0\n\ts_mov_b32 m0, %3\n\ts_nop 0\n1:\n\tbuffer_load_dwordx4 %4, %5, %1 offen " POL " lds\n\t" \
               "s_add_u32 m0, m0, 0x400\n\ts_add_u32 %1, %1, %6\n\ts_sub_u32 %2, %2, 1\n\ts_cmp_lg_u32 %2, 0\n\ts_cbranch_scc1 1b\n\ts_mov_b32 m0, %0\n2:" \
               : "=&s"(keep_), "+&s"(so_), "+&s"(n_) : "s"(lds_addr), "v"(voff), "s"(srd), "s"(step) : "scc", "memory"); } while (0)
__device__ __forceinline__ v4u tile_frag(const LAS unsigned char* tile, int l, int k, int ct) {
  const LAS unsigned char* p = tile + (8 * (l >> 4) + ((l & 15) >> 2)) * 128 + (l & 3) * 8 + k * 4096 + ct * 32;
  const v2u t0 = tr_read(p), t1 = tr_read(p + 512);
  return v4u{t0.x, t0.y, t1.x, t1.y};
}
#define DRAIN() asm volatile("s_waitcnt vmcnt(0)" ::: "memory")
#define LDSWAIT() asm volatile("s_waitcnt lgkmcnt(0)" ::: "memory")
#define WGBAR() do { __builtin_amdgcn_sched_barrier(0); __builtin_amdgcn_s_barrier(); __builtin_amdgcn_sched_barrier(0); asm volatile("" ::: "memory"); } while (0)
__device__ __forceinline__ u64 read_tag(const unsigned char* ws) {
  u64 t;
  asm volatile("s_load_dwordx2 %0, %1, 0x0\n\ts_waitcnt lgkmcnt(0)" : "=s"(t) : "s"(ws) : "memory");
  const unsigned old = (unsigned)t;
  t += 0x9E3779B97F4A7C15ull;
  t = (t ^ (t >> 30)) * 0xBF58476D1CE4E5B9ull; t = (t ^ (t >> 27)) * 0x94D049BB133111EBull;
  t ^= t >> 31;
  return (unsigned)t == old ? t ^ 1u : t;
}
__device__ __forceinline__ unsigned now_ticks() {
  u64 t;
  asm volatile("s_memrealtime %0\n\ts_waitcnt lgkmcnt(0)" : "=s"(t) :: "memory");
  return (unsigned)t;
}
#define WAIT_TICK(base, delta) \
  asm volatile("s_branch 2f\n1:\n\ts_sleep 1\n2:\n\ts_memrealtime vcc\n\ts_waitcnt lgkmcnt(0)\n\ts_sub_u32 vcc_lo, vcc_lo, %0\n\ts_cmpk_lt_i32 vcc_lo, %1\n\ts_cbranch_scc1 1b" \
               :: "s"(base), "n"(delta) : "scc", "vcc", "memory")
#define WAIT_NEXT(acc, step) \
  asm volatile("s_branch 2f\n1:\n\ts_sleep 1\n2:\n\ts_memrealtime vcc\n\ts_waitcnt lgkmcnt(0)\n\ts_lshl_b32 vcc_lo, vcc_lo, 4\n\ts_sub_u32 vcc_lo, vcc_lo, %0\n\ts_cmp_lt_i32 vcc_lo, 0\n\ts_cbranch_scc1 1b\n\ts_add_u32 %0, %0, %1" \
               : "+s"(acc) : "s"(step) : "scc", "vcc", "memory")
#define WAIT_SLIP(acc, step) \
  asm volatile("s_branch 2f\n1:\n\ts_sleep 1\n2:\n\ts_memrealtime vcc\n\ts_waitcnt lgkmcnt(0)\n\ts_lshl_b32 vcc_lo, vcc_lo, 4\n\ts_sub_u32 vcc_lo, vcc_lo, %0\n\ts_cmp_lt_i32 vcc_lo, 0\n\ts_cbranch_scc1 1b\n\t" \
               "s_addk_i32 vcc_lo, %2\n\ts_max_i32 vcc_lo, vcc_lo, 0\n\ts_add_u32 %0, %0, %1\n\ts_add_u32 %0, %0, vcc_lo" \
               : "+s"(acc) : "s"(step), "n"(-(GSLIP)) : "scc", "vcc", "memory")
constexpr int SPIN_CAP = 1 << 18;
#define LD_SC1(off) __builtin_amdgcn_raw_buffer_load_b128(rws, (off), 0, AUX_SC1)
#define ST_SC1(v, off) __builtin_amdgcn_raw_buffer_store_b128((v), rws, (off), 0, AUX_SC1)
__device__ __forceinline__ v4u gran2(float a, float b, unsigned tg) { return v4u{__float_as_uint(a), tg, __float_as_uint(b), tg}; }
__device__ __forceinline__ unsigned gbad(v4u g, unsigned tg) { return (g.y ^ tg) | (g.w ^ tg); }
__device__ __forceinline__ f32x2 gval(v4u g) { return f32x2{__uint_as_float(g.x), __uint_as_float(g.z)}; }
__device__ __forceinline__ v4u gran_d(f32x4 d, int l, unsigned tg) { return gran2((l & 8) ? d[2] : d[0], (l & 8) ? d[3] : d[1], tg); }

#define STAMP(k) do { } while (0)
#define FLUSH() do { } while (0)

#define PSTAMP(k) do { } while (0)
__device__ __forceinline__ void poll2(__amdgpu_buffer_rsrc_t rws, int o0, int o1, unsigned tg, v4u& r0, v4u& r1) {
  bool need = true;
#pragma nounroll
  for (int spin = 0; spin < SPIN_CAP && need; ++spin) {
    r0 = LD_SC1(o0); r1 = LD_SC1(o1);
    need = (r0.y == tg) | (r0.w == tg) | (r1.y == tg) | (r1.w == tg);
    asm volatile("" ::: "memory");
  }
}
__device__ __forceinline__ unsigned notag(unsigned x, unsigned tg) { return x == tg ? x ^ 1u : x; }
__device__ __forceinline__ float gate_of(v4u xv, v4u gv) {
  float gs = 0.f;
#pragma unroll
  for (int j = 0; j < 4; ++j) { gs += rbf(bf_lo(xv[j]) * bf_lo(gv[j])); gs += rbf(bf_hi(xv[j]) * bf_hi(gv[j])); }
  gs += dpp<0xB1>(gs); gs += dpp<0x4E>(gs); gs += dpp<0x124>(gs); gs += dpp<0x128>(gs); gs = xrow_sum(gs);
  return rbf(1.f / (1.f + __expf(-rbf(gs))));
}

struct Args {
  const unsigned short *x, *wq, *wo, *w1, *b1, *w2, *b2, *gw;
  const unsigned char *kc, *vc;
  unsigned char* ws;
  unsigned short* out;
};

typedef unsigned v16u __attribute__((ext_vector_type(16)));
typedef unsigned v8u __attribute__((ext_vector_type(8)));

__global__ void __launch_bounds__(NWAVE * 64) fused_decode(Args a_) {
  v16u ka0; v8u ka1; u64 t_in64;
  asm volatile("s_load_dwordx16 %0, %3, 0x0\n\ts_load_dwordx8 %1, %3, 0x40\n\ts_memrealtime %2" : "=&s"(ka0), "=&s"(ka1), "=&s"(t_in64) : "s"(__builtin_amdgcn_kernarg_segment_ptr()));
  __shared__ __attribute__((aligned(16))) unsigned char smem[L_END + 64];
  LAS unsigned char* L = (LAS unsigned char*)smem;
  const unsigned lbase = (unsigned)(uintptr_t)L;
  const int tid = threadIdx.x, l = tid & 63, w = __builtin_amdgcn_readfirstlane(tid >> 6);
  const int c = blockIdx.x, xr = c >> 3, sp = xr & 3, kvh = xr >> 2, b = (c - kvh) & 7, i32 = b * 4 + sp;
  {
    u64 pc;
    asm volatile("s_getpc_b64 %0" : "=s"(pc));
    const v4u rc = make_srd((const void*)(pc & ~255ull), CODEB);
    DMA_LOOP("", rc, ((xr * 2 + w) & (CODEKB - 1)) * 1024 + l * 16, 0, lbase + L_NIL, (12u >> w) & 1u);
  }
  const int g = l >> 4;
  const int oh = (c & 7) * 4 + (xr >> 3), on = xr & 7;
  const bool ffn2 = xr >= 24 && (c & 7) < 7;
  const bool isq = xr < 16, side = xr < 23 || ffn2;
  const int tl = isq ? ((xr >> 1) * 8 + ((c + xr) & 7)) * 2 + (xr & 1) : (c & 7) * 7 + xr - 16;
  const int cb = ffn2 ? on : tl >> 1, kh = ffn2 ? c & 7 : tl & 1;
#define KP(v, i) ((u64)v[i] | ((u64)v[(i) + 1] << 32))
  if (__builtin_expect(w == 0, 1)) {
    const unsigned ldb = isq ? 8192u : ffn2 ? 1024u : (unsigned)FF * 2u;
    unsigned woff = (unsigned)kh * 256u * ldb + cb * 128, wlen = 255u * ldb + 128u, wstep = 8u * ldb, wvo = (unsigned)(l >> 3) * ldb + (unsigned)(l & 7) * 16u;
    unsigned nw = side ? 32 : 0;
    asm volatile("s_waitcnt lgkmcnt(0)" : "+s"(ka0), "+s"(woff), "+s"(wlen), "+s"(wstep), "+s"(nw), "+v"(wvo) :: "memory");
    u64 pq = KP(ka0, 2), p1 = KP(ka0, 6), p2 = KP(ka0, 10);
    asm volatile("" : "+s"(pq), "+s"(p1), "+s"(p2));
    DMA_LOOP("nt", make_srd((const unsigned char*)(isq ? pq : ffn2 ? p2 : p1) + woff, wlen), wvo, wstep, lbase + L_WT, nw);
    DMA_LOOP("", make_srd((const void*)KP(ka0, 0), 8192), l * 16, 1024, lbase + L_X, nw >> 2);
  }
  asm volatile("s_waitcnt lgkmcnt(0)" : "+s"(t_in64), "+s"(ka0), "+s"(ka1) :: "memory");
  const unsigned t_in = (unsigned)t_in64;
  Args a;
  a.x = (const unsigned short*)KP(ka0, 0); a.wq = (const unsigned short*)KP(ka0, 2); a.wo = (const unsigned short*)KP(ka0, 4); a.w1 = (const unsigned short*)KP(ka0, 6);
  a.b1 = (const unsigned short*)KP(ka0, 8); a.w2 = (const unsigned short*)KP(ka0, 10); a.b2 = (const unsigned short*)KP(ka0, 12); a.gw = (const unsigned short*)KP(ka0, 14);
  a.kc = (const unsigned char*)KP(ka1, 0); a.vc = (const unsigned char*)KP(ka1, 2); a.ws = (unsigned char*)KP(ka1, 4); a.out = (unsigned short*)KP(ka1, 6);
  unsigned char* ws = a.ws;
  PSTAMP(21);
  const __amdgpu_buffer_rsrc_t rws = __builtin_amdgcn_make_buffer_rsrc((void*)ws, 0, WS_END, 0x00020000);
  const int rr = c, rb = rr & 7, rc = rr >> 3;

  if (w == 0) {
    if (side) {
      const u64 tag = read_tag(ws);
      const unsigned tg = (unsigned)tag;
      float og = 0.f;
      if (ffn2) {
        const unsigned bb0 = *(const GAS unsigned*)(a.b1 + kh * 256 + l * 2), bb1 = *(const GAS unsigned*)(a.b1 + kh * 256 + 128 + l * 2);
        const v4u gv = *(const GAS v4u*)(a.gw + l * 8);
        DRAIN();
        float gt = 0.f;
#pragma nounroll
        for (int j = 0; j < 8; ++j) { const float t = gate_of(*(const LAS v4u*)(L + L_X + j * 1024 + l * 16), gv); gt = (l & 7) == j ? t : gt; }
        if (l < 8) *(LAS float*)(L + L_G + l * 4) = gt;
        og = rbf(1.f - gt);
        WAIT_TICK(t_in, TH);
        int spin = 0;
#pragma nounroll
        for (int hf = 0; hf < 2;) {
          v4u t[16];
#pragma unroll
          for (int i = 0; i < 16; ++i) {
            t[i] = __builtin_amdgcn_raw_buffer_load_b128(rws, WS_HP + kh * 32768 + hf * 16384 + l * 16, i * 1024, EAL);
            asm volatile("s_sleep %0" :: "n"(GH) : "memory");
          }
          unsigned bad = 0;
#pragma unroll
          for (int j = 0; j < 8; ++j) {
            const int i0 = (j >> 1) * 4 + (j & 1);
            const unsigned bb = (j & 1) ? bb1 : bb0;
            bad |= gbad(t[i0], tg) | gbad(t[i0 + 2], tg);
            const f32x2 z = gval(t[i0]) + gval(t[i0 + 2]);
            const float h0 = rbf(rbf(z.x) + bf_lo(bb)), h1 = rbf(rbf(z.y) + bf_hi(bb));
            const float r0 = h0 > 0.f ? h0 : 0.f, r1 = h1 > 0.f ? h1 : 0.f;
            *(LAS unsigned*)(L + L_H + (hf * 4 + (j >> 1)) * 1024 + (j & 1) * 256 + l * 4) = cvtpk(r0 * r0, r1 * r1);
          }
          if (__builtin_amdgcn_ballot_w64(bad != 0) == 0 || ++spin > SPIN_CAP) ++hf;
        }
        LDSWAIT();
        if (l == 0) *(volatile LAS u64*)(L + L_FLAG + 8) = tag;
      }
      DRAIN();
      STAMP(1);
      PSTAMP(23);
      v4u xb[8];
      const LAS unsigned char* xp = L + (ffn2 ? L_H : L_X + kh * 512) + (l & 7) * 1024 + g * 16;
#pragma unroll
      for (int k = 0; k < 8; ++k) xb[k] = *(const LAS v4u*)(xp + k * 64);
      const int off = (isq ? WS_QP + (cb >> 3) * 65536 + (l & 7) * 8192 + kh * 4096 + (cb & 7) * 512 : WS_HP + (cb >> 2) * 32768 + (l & 7) * 4096 + kh * 2048 + (cb & 3) * 512)
                      + g * 32 + (l & 8) * 2;
#pragma nounroll
      for (int ct = 0; ct < 4; ++ct) {
        f32x4 d = {0.f, 0.f, 0.f, 0.f};
#pragma unroll
        for (int k = 0; k < 8; ++k) d = mfma32(tile_frag(L + L_WT, l, k, ct), xb[k], d);
        if (ffn2) *(LAS f32x4*)(L + L_FF + ct * 1024 + l * 16) = d * og;
        else __builtin_amdgcn_raw_buffer_store_b128(gran_d(d, l, tg), rws, off + ct * 128, 0, EAS);
      }
      LDSWAIT();
      PSTAMP(24);
    }
    STAMP(2);
    FLUSH();
    return;
  }
  if (w == 1) {
    const v4u xv = *(const GAS v4u*)(a.x + rb * DM + l * 8), gv = *(const GAS v4u*)(a.gw + l * 8);
    const v2u bb = *(const GAS v2u*)(a.b2 + rc * 16 + 4 * (l & 3));
    if (TOUCH) {
      const v4u rt = make_srd(a.wo + oh * 128 * DM + on * 64, 16);
      DMA_LOOP("nt", rt, l * 16, 0, lbase + L_NIL, 1);
      const v4u ru = make_srd(a.out, 16);
      DMA_LOOP("nt", ru, l * 16, 0, lbase + L_NIL, 1);
    }
    const u64 tag = read_tag(ws);
    const unsigned tg = (unsigned)tag, th = (unsigned)(tag >> 32);
    if (l < 8) __builtin_amdgcn_raw_buffer_store_b128(v4u{t_in, tg, t_in, tg}, rws, WS_X + c * 128 + l * 16, 0, EAS);
    {
      const float gt = gate_of(xv, gv), og = rbf(1.f - gt);
      if (l < 4) *(LAS f32x4*)(L + L_FIN + l * 16) = f32x4{bf_lo(bb.x), bf_hi(bb.x), bf_lo(bb.y), bf_hi(bb.y)} * og;
      if (l == 0) *(LAS float*)(L + L_FIN + 64) = gt;
    }
    WAIT_TICK(t_in, TQ);
    const v2u st = __builtin_amdgcn_raw_buffer_load_b64(rws, WS_X + (l & 7) * 128, 0, AUX_SC1);
#pragma nounroll
    for (int spin = 0; spin < SPIN_CAP; ++spin) {
      v4u t[8];
#pragma unroll
      for (int i = 0; i < 8; ++i) { asm volatile("s_sleep %0" :: "n"(GQ) : "memory"); t[i] = __builtin_amdgcn_raw_buffer_load_b128(rws, WS_QP + kvh * 65536 + b * 8192 + i * 1024 + l * 16, 0, EAL); }
      unsigned bad = 0;
#pragma unroll
      for (int j = 0; j < 4; ++j) {
        bad |= gbad(t[j], tg) | gbad(t[4 + j], tg);
        const f32x2 q = gval(t[j]) + gval(t[4 + j]);
        *(LAS unsigned*)(L + L_Q + j * 256 + l * 4) = cvtpk(q.x, q.y);
      }
      if (__builtin_amdgcn_ballot_w64(bad != 0) == 0) break;
      asm volatile("s_sleep 8" ::: "memory");
    }
    unsigned dtv = DTV;
    {
      int late = st.y == tg ? (int)(t_in - st.x) : 0;
      late = max(late, (int)dppu<0xB1>((unsigned)late)); late = max(late, (int)dppu<0x4E>((unsigned)late)); late = max(late, (int)dppu<0x141>((unsigned)late));
      late = min(late, 100);
      if (SKEW >= 0) dtv = __builtin_amdgcn_readfirstlane(DTV - (late - SKEW) / 2);
      if (l == 0) *(LAS unsigned*)(L + L_FLAG + 16) = dtv;
    }
    LDSWAIT();
    if (l == 0) *(volatile LAS u64*)(L + L_FLAG) = tag;
    STAMP(3);
    if (ffn2) {
#pragma nounroll
      for (int spin = 0; spin < SPIN_CAP * 16; ++spin) { if (*(volatile const LAS u64*)(L + L_FLAG + 8) == tag) break; asm volatile("s_sleep 8" ::: "memory"); }
    }
    DRAIN();
    if (l == 0) __builtin_amdgcn_raw_buffer_store_b64(v2u{tg, th}, rws, WS_F + c * 8, 0, AUX_SC1);
    STAMP(5);
    v4u ml;
#pragma nounroll
    for (int spin = 0; spin < SPIN_CAP * 16; ++spin) {
      ml = *(volatile const LAS v4u*)(L + L_ML + (l & 31) * 16);
      if (__builtin_amdgcn_ballot_w64(((ml.z ^ tg) | (ml.w ^ th)) != 0) == 0) break;
      asm volatile("s_sleep 4" ::: "memory");
    }
    const float mw = __uint_as_float(ml.x);
    float M = fmaxf(mw, dpp<0xB1>(mw)); M = fmaxf(M, dpp<0x4E>(M)); M = fmaxf(M, dpp<0x141>(M));
    float Ls = __builtin_amdgcn_exp2f(mw - M) * __uint_as_float(ml.y);
    Ls += dpp<0xB1>(Ls); Ls += dpp<0x4E>(Ls); Ls += dpp<0x141>(Ls);
    const float lse = M + __builtin_amdgcn_logf(Ls);
    const v4u lg = gran2(lse, dpp<0x128>(lse), tg);
    if (l < 32 && (l & 15) == 0) ST_SC1(lg, WS_LSE + ((b * 8 + kvh) * 4 + sp) * 128 + l);
    STAMP(6);
    v2u ls;
    asm volatile("s_sleep 16" ::: "memory");
#pragma nounroll
    for (int spin = 0; spin < SPIN_CAP; ++spin) {
      ls = __builtin_amdgcn_raw_buffer_load_b64(rws, WS_LSE + ((b * 8 + kvh) * 4 + (l & 3)) * 128 + ((l >> 3) & 3) * 8, 0, AUX_SC1);
      if (__builtin_amdgcn_ballot_w64(ls.y != tg) == 0) break;
      asm volatile("s_sleep 8" ::: "memory");
    }
    const float lf = __uint_as_float(ls.x);
    float M2 = fmaxf(lf, dpp<0xB1>(lf)); M2 = fmaxf(M2, dpp<0x4E>(M2));
    float E = __builtin_amdgcn_exp2f(lf - M2);
    E += dpp<0xB1>(E); E += dpp<0x4E>(E);
    if (l < 32) *(LAS float*)(L + L_CF + l * 4) = __builtin_amdgcn_exp2f(mw - M2) * __builtin_amdgcn_rcpf(E);
    WAIT_TICK(t_in, TF);
#pragma nounroll
    for (int spin = 0; spin < SPIN_CAP; ++spin) {
      const v4u f0 = LD_SC1(WS_F + l * 16), f1 = LD_SC1(WS_F + 1024 + l * 16);
      if (__builtin_amdgcn_ballot_w64(((f0.x ^ tg) | (f0.y ^ th) | (f0.z ^ tg) | (f0.w ^ th) | (f1.x ^ tg) | (f1.y ^ th) | (f1.z ^ tg) | (f1.w ^ th)) != 0) == 0) break;
      asm volatile("s_sleep 16" ::: "memory");
    }
    LDSWAIT();
    STAMP(7);
    if (LATEWO) {
      const v4u rwo = make_srd(a.wo + oh * 128 * DM + on * 64, 127 * 1024 + 128);
      const unsigned wov = (unsigned)(l >> 3) * 1024u + (unsigned)(l & 7) * 16u;
      if (WOPRE > 0) {
#pragma nounroll
        for (int spin = 0; spin < SPIN_CAP * 16; ++spin) {
          if (__builtin_amdgcn_ballot_w64(*(volatile const LAS u64*)(L + L_DONE + (l & 7) * 8) != tag) == 0) break;
          asm volatile("s_sleep 1" ::: "memory");
        }
        asm volatile("s_sleep %0" :: "n"(WOS) : "memory");
        DMA_LOOP("nt", rwo, wov, 8192, lbase + L_WO, WOPRE);
      }
      WGBAR();
      if (WOPRE < 16) DMA_LOOP("nt", rwo, wov + WOPRE * 8192, 8192, lbase + L_WO + WOPRE * 1024, 16 - WOPRE);
      DRAIN();
      if (l == 0) *(volatile LAS u64*)(L + L_FLAG + 24) = tag;
      LDSWAIT();
      STAMP(8);
    }
    FLUSH();
    return;
  }

  const int sw = w - 2;
  unsigned tnext = ((t_in + T0) << 4) + (STAG == 1 ? sw * (DT / 8) : STAG == 2 ? (xr & 7) * (DT / 8) : STAG == 3 ? ((xr >> 2) & 7) * (DT / 8) : 0), dt = DT;
#define PACE_TO(j) do { asm volatile("s_waitcnt vmcnt(%0)" :: "n"(WIN - 1) : "memory"); if ((j) < GEND) WAIT_SLIP(tnext, dt); else WAIT_NEXT(tnext, dt); } while (0)
  const size_t base = ((size_t)(b * HKV + kvh) * SK + sp * 1024 + sw * 128) * 256;
  const v4u rk = make_srd(a.kc + base, 32768), rv = make_srd(a.vc + base, 32768);
  const int ko = ((l >> 2) & 3) * 256 + g * 64 + (l & 3) * 16, vo = l * 16;
  const int rot = (ROTW * sw + (ROTF)) & 31;
#define OFF(i) ((((i) + rot) & 31) * 1024)
#define LOAD_V(i) asm volatile("buffer_load_dwordx4 %0, %1, %2, %3 offen nt" : "+v"(kv[i]) : "v"(vo), "s"(rv), "s"(OFF(i)) : "memory")
  if (!LATEWO) {
    const v4u ro = make_srd(a.wo + (oh * 128 + sw * 16) * DM + on * 64, 15 * 1024 + 128);
    const unsigned oo = (unsigned)(l >> 3) * 1024u + (unsigned)(l & 7) * 16u;
    PACE_TO(0); dma_rows(ro, oo, 0, lbase + L_WO + sw * 2048);
    PACE_TO(1); dma_rows(ro, oo, 8192, lbase + L_WO + sw * 2048 + 1024);
  }
  if (TOUCH) {
    v4u r1 = rk, r2 = rv;
    r1.z = 16; r2.z = 16;
    DMA_LOOP("nt", r1, l * 16, 0, lbase + L_NIL, 1);
    DMA_LOOP("nt", r2, l * 16, 0, lbase + L_NIL, 1);
  }
  v4u kv[32];
  __builtin_amdgcn_sched_barrier(0);
#pragma unroll
  for (int i = 0; i < 32; ++i) {
    PACE_TO(2 + i);
    asm volatile("buffer_load_dwordx4 %0, %1, %2, %3 offen nt" : "=v"(kv[i]) : "v"(ko), "s"(rk), "s"(OFF(i)) : "memory");
    PSTAMP(100 + i);
    if (SLIP >= 0 && (i == WIN || i == 2 * WIN)) { const unsigned t1 = (now_ticks() - SLIP) << 4; if ((int)(t1 - tnext) > 0) tnext = t1 + sw * (DT / 16); }
    __builtin_amdgcn_sched_barrier(0);
  }
  STAMP(1);
  const u64 tag = read_tag(ws);
  const unsigned tg = (unsigned)tag;
#pragma nounroll
  for (int spin = 0; spin < SPIN_CAP * 16; ++spin) { if (*(volatile const LAS u64*)(L + L_FLAG) == tag) break; asm volatile("s_sleep 2" ::: "memory"); }
  asm volatile("" ::: "memory");
  STAMP(2);
  const v4u qf = *(const LAS v4u*)(L + L_Q + ((l >> 2) & 3) * 256 + g * 64 + (l & 3) * 16);
  dt = __builtin_amdgcn_readfirstlane(*(const LAS unsigned*)(L + L_FLAG + 16));
  __builtin_amdgcn_sched_barrier(0);
  constexpr float C2 = 0.088388347648318440f * 1.4426950408889634f;
  float sc[8];
  const int s = l & 3;
#pragma unroll
  for (int m = 0; m < 8; ++m) {
    asm volatile("s_waitcnt vmcnt(%4)" : "+v"(kv[4 * m]), "+v"(kv[4 * m + 1]), "+v"(kv[4 * m + 2]), "+v"(kv[4 * m + 3]) : "n"(28 - 4 * m + m) : "memory");
    f32x4 d[4];
#pragma unroll
    for (int r = 0; r < 4; ++r) d[r] = mfma32(kv[4 * m + r], qf, f32x4{0.f, 0.f, 0.f, 0.f});
#pragma unroll
    for (int r = 0; r < 4; ++r) {
      float t = dpp<0x00>(d[r][0]); t = dpp<0x55>(d[r][1]) + t; t = dpp<0xAA>(d[r][2]) + t; t = dpp<0xFF>(d[r][3]) + t;
      asm volatile("" : "+v"(t));
      sc[m] = (r == 0 || s == r) ? t : sc[m];
    }
    asm volatile("" : "+v"(sc[m]));
    __builtin_amdgcn_sched_barrier(0);
    PACE_TO(34 + m);
    LOAD_V(m);
    __builtin_amdgcn_sched_barrier(0);
  }
  STAMP(3);
  float mx = sc[0];
#pragma unroll
  for (int m = 1; m < 8; ++m) mx = fmaxf(mx, sc[m]);
  mx = fmaxf(mx, dpp<0xB1>(mx)); mx = fmaxf(mx, dpp<0x4E>(mx)); mx = xrow_max(mx);
  const float mneg = -mx * C2;
  float lsum = 0.f;
  unsigned pk[8];
#pragma unroll
  for (int m = 0; m < 8; ++m) { const float p = __builtin_amdgcn_exp2f(fmaf(sc[m], C2, mneg)); pk[m] = cvtpk(p, p); lsum += bf_lo(pk[m]); }
  lsum += dpp<0xB1>(lsum); lsum += dpp<0x4E>(lsum); lsum = xrow_sum(lsum);
  if (l < 16 && (l & 3) == 0) *(LAS v4u*)(L + L_ML + ((l >> 2) * 8 + sw) * 16) = v4u{__float_as_uint(mx * C2), __float_as_uint(lsum), tg, (unsigned)(tag >> 32)};
  const unsigned half = (l & 1) ? 0xffff0000u : 0x0000ffffu;
  const unsigned m0 = (l & 2) ? 0u : half, m1 = (l & 2) ? half : 0u;
  f32x4 o0 = {0.f, 0.f, 0.f, 0.f}, o1 = {0.f, 0.f, 0.f, 0.f};
#pragma unroll
  for (int j = 8; j < 40; ++j) {
    if (j < 32) { PACE_TO(34 + j); LOAD_V(j); __builtin_amdgcn_sched_barrier(0); }
    if (j == 31) { if (l == 0) *(volatile LAS u64*)(L + L_DONE + sw * 8) = tag; STAMP(11); }
    const int i = j - 8;
    const unsigned pb = (i & 3) == 0 ? dppu<0x00>(pk[i >> 2]) : (i & 3) == 1 ? dppu<0x55>(pk[i >> 2]) : (i & 3) == 2 ? dppu<0xAA>(pk[i >> 2]) : dppu<0xFF>(pk[i >> 2]);
    const v2u av = {pb & m0, pb & m1};
    asm volatile("s_waitcnt vmcnt(%1)" : "+v"(kv[i]) : "n"((j < 32 ? j : 31) - i) : "memory");
    o0 = mfma16(av, v2u{kv[i].x, kv[i].y}, o0);
    o1 = mfma16(av, v2u{kv[i].z, kv[i].w}, o1);
    __builtin_amdgcn_sched_barrier(0);
  }
  *(LAS f32x4*)(L + L_MRG + (sw * 4 + g) * 512 + (l & 15) * 32) = o0;
  *(LAS f32x4*)(L + L_MRG + (sw * 4 + g) * 512 + (l & 15) * 32 + 16) = o1;
  STAMP(4);
  LDSWAIT();
  WGBAR();
  if (sw < 4) {
    STAMP(5);
    const f32x4 c0 = *(const LAS f32x4*)(L + L_CF + sw * 32), c1 = *(const LAS f32x4*)(L + L_CF + sw * 32 + 16);
    f32x2 acc = {0.f, 0.f};
#pragma unroll
    for (int j = 0; j < 8; ++j) {
      const f32x2 ov = *(const LAS f32x2*)(L + L_MRG + (j * 4 + sw) * 512 + l * 8);
      const float e = j < 4 ? c0[j & 3] : c1[j & 3];
      acc.x = fmaf(e, ov.x, acc.x); acc.y = fmaf(e, ov.y, acc.y);
    }
    const unsigned pv = notag(cvtpk(acc.x, acc.y), tg);
    const v4u pq = {dppu<0x00>(pv), dppu<0x55>(pv), dppu<0xAA>(pv), dppu<0xFF>(pv)};
    if ((l & 3) == 0) ST_SC1(pq, WS_P + (((kvh * 4 + sw) * 8 + b) * 4 + sp) * 256 + l * 4);
    STAMP(6);
    f32x4 ff = {0.f, 0.f, 0.f, 0.f};
    float gm = 1.f;
    if (ffn2) { ff = *(const LAS f32x4*)(L + L_FF + sw * 1024 + l * 16); gm = *(const LAS float*)(L + L_G + (l & 7) * 4); }
    const int po = WS_P + (oh * 8 + 2 * sw) * 1024 + l * 16;
    asm volatile("s_sleep %0" :: "n"(PDELAY1) : "memory");
    v4u p0 = LD_SC1(po), p1 = LD_SC1(po + 1024);
    if (LATEWO) {
#pragma nounroll
      for (int spin = 0; spin < SPIN_CAP * 16; ++spin) { if (*(volatile const LAS u64*)(L + L_FLAG + 24) == tag) break; }
      asm volatile("" ::: "memory");
    }
    v4u wf[4];
#pragma unroll
    for (int k = 0; k < 4; ++k) wf[k] = tile_frag(L + L_WO, l, k, sw);
    __builtin_amdgcn_sched_barrier(0);
    if ((p0.y == tg) | (p0.w == tg) | (p1.y == tg) | (p1.w == tg)) poll2(rws, po, po + 1024, tg, p0, p1);
    *(LAS v4u*)(L + L_PG + (8 * sw + g) * 272 + (l & 15) * 16) = p0;
    *(LAS v4u*)(L + L_PG + (8 * sw + 4 + g) * 272 + (l & 15) * 16) = p1;
    STAMP(7);
    LDSWAIT();
    WGBAR();
    const int pb = l & 7, sl = (l >> 3) & 1;
    v4u pa[8];
#pragma unroll
    for (int k = 0; k < 8; ++k) pa[k] = *(const LAS v4u*)(L + L_PG + (pb * 4 + (k >> 2) * 2 + sl) * 272 + (k & 3) * 64 + g * 16);
    __builtin_amdgcn_sched_barrier(0);
    f32x4 da = {0.f, 0.f, 0.f, 0.f}, db = {0.f, 0.f, 0.f, 0.f};
#pragma unroll
    for (int k = 0; k < 4; ++k) { da = mfma32(wf[k], pa[k], da); db = mfma32(wf[k], pa[4 + k], db); }
    v4u r;
#pragma unroll
    for (int j = 0; j < 4; ++j) { const float t = da[j] + db[j]; r[j] = notag(__float_as_uint(fmaf(t + dpp<0x128>(t), gm, ff[j])), tg); }
    if (sl == 0) ST_SC1(r, WS_O + (((on * 4 + sw) * 4 + (pb >> 1)) * 32 + oh) * 128 + (pb & 1) * 64 + g * 16);
    STAMP(8);
    if (sw == 0) {
      const f32x4 fin = *(const LAS f32x4*)(L + L_FIN + (l & 3) * 16);
      const float gt = *(const LAS float*)(L + L_FIN + 64);
      const float g0 = (l & 12) == 12 ? 1.f : gt, g1 = l < 48 ? g0 : gt;
      const int oo = WS_O + (rr >> 1) * 4096 + (l >> 2) * 128 + (rr & 1) * 64 + (l & 3) * 16;
      v4u r0, r1;
      asm volatile("s_sleep %0" :: "n"(PDELAY2) : "memory");
      poll2(rws, oo, oo + 2048, tg, r0, r1);
      STAMP(9);
      f32x4 av = __builtin_bit_cast(f32x4, r0) * g0 + __builtin_bit_cast(f32x4, r1) * g1;
#pragma unroll
      for (int j = 0; j < 4; ++j) av[j] += dpp<0x124>(av[j]);
#pragma unroll
      for (int j = 0; j < 4; ++j) av[j] += dpp<0x128>(av[j]);
      {
        auto s0 = __builtin_amdgcn_permlane16_swap(__float_as_uint(av[0]), __float_as_uint(av[1]), false, false);
        auto s1 = __builtin_amdgcn_permlane16_swap(__float_as_uint(av[2]), __float_as_uint(av[3]), false, false);
        const float u0 = __uint_as_float(s0[0]) + __uint_as_float(s0[1]), u1 = __uint_as_float(s1[0]) + __uint_as_float(s1[1]);
        auto s2 = __builtin_amdgcn_permlane32_swap(__float_as_uint(u0), __float_as_uint(u1), false, false);
        const float v = __uint_as_float(s2[0]) + __uint_as_float(s2[1]);
        const float o = v + (g == 0 ? fin[0] : g == 1 ? fin[1] : g == 2 ? fin[2] : fin[3]);
        if ((l & 15) < 4) *(GAS unsigned short*)(a.out + rb * DM + rc * 16 + 4 * (l & 3) + g) = (unsigned short)cvtpk(o, o);
      }
      if (c == 0 && l == 0) *(GAS u64*)(ws + WS_E) = tag;
      STAMP(10);
    }
  }
  FLUSH();
}

extern "C" void kernel_launch(void* const* d_in, const int* in_sizes, int n_in, void* d_out, int out_size, void* d_ws, size_t ws_size,
                              hipStream_t stream) {
  Args a;
  a.x = (const unsigned short*)d_in[0];
  a.kc = (const unsigned char*)d_in[1];
  a.vc = (const unsigned char*)d_in[2];
  a.wq = (const unsigned short*)d_in[3];
  a.wo = (const unsigned short*)d_in[4];
  a.w1 = (const unsigned short*)d_in[6];
  a.b1 = (const unsigned short*)d_in[7];
  a.w2 = (const unsigned short*)d_in[8];
  a.b2 = (const unsigned short*)d_in[9];
  a.gw = (const unsigned short*)d_in[10];
  a.ws = (unsigned char*)d_ws;
  a.out = (unsigned short*)d_out;
  fused_decode<<<256, NWAVE * 64, 0, stream>>>(a);
}
```

```cpp
#define PSLOW 2420
#define PROBE 21
#define PW0 2
#define PW1 3
#include <hip/hip_runtime.h>
#include <hip/hip_bf16.h>
#include <stdint.h>

typedef unsigned v4u __attribute__((ext_vector_type(4)));
typedef unsigned v2u __attribute__((ext_vector_type(2)));
typedef float f32x4 __attribute__((ext_vector_type(4)));
typedef float f32x2 __attribute__((ext_vector_type(2)));
typedef __bf16 bf16x8 __attribute__((ext_vector_type(8)));
typedef __bf16 bf16x2 __attribute__((ext_vector_type(2)));
typedef short s16x4 __attribute__((ext_vector_type(4)));
typedef unsigned long long u64;
#define GAS __attribute__((address_space(1)))
#define LAS __attribute__((address_space(3)))

constexpr int DM = 512, HKV = 8, SK = 4096, FF = 1792;
constexpr int AUX_SC1 = 16;
#define DT 465
#define T0 130
#define WIN 5
#define TQ 560
#define TH 480
#define TF 1500
#define GQ 6
#define GH 3
#define SHIFT 1
#define PDELAY1 13
#define PDELAY2 15
#define SKEW 20
#define LATEWO 1
#define WOD 35
constexpr int NS0 = LATEWO ? 0 : 2;
#define RMAP 0
#define EAL 18
#define EAS 18
#define CODEKB 16
#define CODEB 15360
#define TOUCH 0
#define WOS 6
#define SLIP 40
#define GSLIP 1120
#define GEND 0
#define WOPRE 16
#define ROTW 0
#define ROTF (16*(sw&1)+8*((sw>>1)&1)+4*(sw>>2)+9*(sp&1)+28*(sp>>1)+30*(b&1))
#define ROTX 1
#define ROTC 0
#define STAG 1
#define DTV 440
constexpr int NWAVE = 10;

constexpr int WS_E = 0;
constexpr int WS_F = 2048;
constexpr int WS_QP = 4096;
constexpr int WS_HP = WS_QP + 8 * 8 * 2 * 512 * 8;
constexpr int WS_X = WS_HP + 7 * 8 * 2 * 256 * 8;
constexpr int WS_LSE = WS_X + 256 * 128;
constexpr int WS_END = WS_LSE + 256 * 128;
constexpr int WS_O = WS_QP;
constexpr int WS_P = WS_HP;
static_assert(WS_LSE - WS_P == 32 * 8 * 4 * 256, "the partials fill exactly the lines of HP and X");
constexpr int WS_ST = 8 << 20;
constexpr int L_WO = 0, L_WT = 16384, L_MRG = 49152, L_ML = L_MRG + 16384, L_CF = L_ML + 512, L_Q = L_CF + 128, L_X = L_Q + 1024, L_H = L_X + 8192, L_NIL = L_H + 8192, L_FF = L_NIL + 1024,
              L_G = L_FF + 4096, L_FIN = L_G + 32, L_FLAG = L_FIN + 96, L_DONE = L_FLAG + 32, L_END = L_DONE + 64;
constexpr int L_PG = L_WT;

__device__ __forceinline__ f32x4 mfma32(v4u a, v4u b, f32x4 c) {
  return __builtin_amdgcn_mfma_f32_16x16x32_bf16(__builtin_bit_cast(bf16x8, a), __builtin_bit_cast(bf16x8, b), c, 0, 0, 0);
}
__device__ __forceinline__ f32x4 mfma16(v2u a, v2u b, f32x4 c) {
  return __builtin_amdgcn_mfma_f32_16x16x16bf16_1k(__builtin_bit_cast(s16x4, a), __builtin_bit_cast(s16x4, b), c, 0, 0, 0);
}
__device__ __forceinline__ unsigned cvtpk(float lo, float hi) { bf16x2 r = {(__bf16)lo, (__bf16)hi}; return __builtin_bit_cast(unsigned, r); }
__device__ __forceinline__ float bf_lo(unsigned u) { return __builtin_bit_cast(float, u << 16); }
__device__ __forceinline__ float bf_hi(unsigned u) { return __builtin_bit_cast(float, u & 0xffff0000u); }
__device__ __forceinline__ float rbf(float x) { return (float)(__bf16)x; }
template <int CTRL> __device__ __forceinline__ float dpp(float x) { return __builtin_bit_cast(float, __builtin_amdgcn_mov_dpp(__builtin_bit_cast(int, x), CTRL, 0xf, 0xf, true)); }
template <int CTRL> __device__ __forceinline__ unsigned dppu(unsigned x) { return (unsigned)__builtin_amdgcn_mov_dpp((int)x, CTRL, 0xf, 0xf, true); }
__device__ __forceinline__ float xrow_max(float x) {
  auto s = __builtin_amdgcn_permlane16_swap(__float_as_uint(x), __float_as_uint(x), false, false);
  x = fmaxf(__uint_as_float(s[0]), __uint_as_float(s[1]));
  auto t = __builtin_amdgcn_permlane32_swap(__float_as_uint(x), __float_as_uint(x), false, false);
  return fmaxf(__uint_as_float(t[0]), __uint_as_float(t[1]));
}
__device__ __forceinline__ float xrow_sum(float x) {
  auto s = __builtin_amdgcn_permlane16_swap(__float_as_uint(x), __float_as_uint(x), false, false);
  x = __uint_as_float(s[0]) + __uint_as_float(s[1]);
  auto t = __builtin_amdgcn_permlane32_swap(__float_as_uint(x), __float_as_uint(x), false, false);
  return __uint_as_float(t[0]) + __uint_as_float(t[1]);
}
__device__ __forceinline__ v2u tr_read(const LAS unsigned char* p) { return __builtin_bit_cast(v2u, __builtin_amdgcn_ds_read_tr16_b64_v4i16((LAS s16x4*)p)); }
__device__ __forceinline__ v4u make_srd(const void* p, unsigned bytes) {
  const u64 a = (u64)p;
  return v4u{(unsigned)a, (unsigned)(a >> 32) & 0xffffu, bytes, 0x00020000u};
}
__device__ __forceinline__ void dma_rows(v4u srd, unsigned voff, unsigned soff, unsigned lds_addr) {
  unsigned keep;
  asm volatile("s_mov_b32 %0, m0\n\ts_mov_b32 m0, %1\n\ts_nop 0\n\tbuffer_load_dwordx4 %2, %3, %4 offen nt lds\n\ts_mov_b32 m0, %0"
               : "=&s"(keep) : "s"(lds_addr), "v"(voff), "s"(srd), "s"(soff) : "memory");
}
#define DMA_LOOP(POL, srd, voff, step, lds_addr, n) do { unsigned keep_, so_ = 0, n_ = (n); \
  asm volatile("s_cmp_eq_u32 %2, 0\n\ts_cbranch_scc1 2f\n\ts_mov_b32 %0, m0\n\ts_mov_b32 m0, %3\n\ts_nop 0\n1:\n\tbuffer_load_dwordx4 %4, %5, %1 offen " POL " lds\n\t" \
               "s_add_u32 m0, m0, 0x400\n\ts_add_u32 %1, %1, %6\n\ts_sub_u32 %2, %2, 1\n\ts_cmp_lg_u32 %2, 0\n\ts_cbranch_scc1 1b\n\ts_mov_b32 m0, %0\n2:" \
               : "=&s"(keep_), "+&s"(so_), "+&s"(n_) : "s"(lds_addr), "v"(voff), "s"(srd), "s"(step) : "scc", "memory"); } while (0)
__device__ __forceinline__ v4u tile_frag(const LAS unsigned char* tile, int l, int k, int ct) {
  const LAS unsigned char* p = tile + (8 * (l >> 4) + ((l & 15) >> 2)) * 128 + (l & 3) * 8 + k * 4096 + ct * 32;
  const v2u t0 = tr_read(p), t1 = tr_read(p + 512);
  return v4u{t0.x, t0.y, t1.x, t1.y};
}
#define DRAIN() asm volatile("s_waitcnt vmcnt(0)" ::: "memory")
#define LDSWAIT() asm volatile("s_waitcnt lgkmcnt(0)" ::: "memory")
#define WGBAR() do { __builtin_amdgcn_sched_barrier(0); __builtin_amdgcn_s_barrier(); __builtin_amdgcn_sched_barrier(0); asm volatile("" ::: "memory"); } while (0)
__device__ __forceinline__ u64 read_tag(const unsigned char* ws) {
  u64 t;
  asm volatile("s_load_dwordx2 %0, %1, 0x0\n\ts_waitcnt lgkmcnt(0)" : "=s"(t) : "s"(ws) : "memory");
  const unsigned old = (unsigned)t;
  t += 0x9E3779B97F4A7C15ull;
  t = (t ^ (t >> 30)) * 0xBF58476D1CE4E5B9ull; t = (t ^ (t >> 27)) * 0x94D049BB133111EBull;
  t ^= t >> 31;
  return (unsigned)t == old ? t ^ 1u : t;
}
__device__ __forceinline__ unsigned now_ticks() {
  u64 t;
  asm volatile("s_memrealtime %0\n\ts_waitcnt lgkmcnt(0)" : "=s"(t) :: "memory");
  return (unsigned)t;
}
#define WAIT_TICK(base, delta) \
  asm volatile("s_branch 2f\n1:\n\ts_sleep 1\n2:\n\ts_memrealtime vcc\n\ts_waitcnt lgkmcnt(0)\n\ts_sub_u32 vcc_lo, vcc_lo, %0\n\ts_cmpk_lt_i32 vcc_lo, %1\n\ts_cbranch_scc1 1b" \
               :: "s"(base), "n"(delta) : "scc", "vcc", "memory")
#define WAIT_NEXT(acc, step) \
  asm volatile("s_branch 2f\n1:\n\ts_sleep 1\n2:\n\ts_memrealtime vcc\n\ts_waitcnt lgkmcnt(0)\n\ts_lshl_b32 vcc_lo, vcc_lo, 4\n\ts_sub_u32 vcc_lo, vcc_lo, %0\n\ts_cmp_lt_i32 vcc_lo, 0\n\ts_cbranch_scc1 1b\n\ts_add_u32 %0, %0, %1" \
               : "+s"(acc) : "s"(step) : "scc", "vcc", "memory")
#define WAIT_SLIP(acc, step) \
  asm volatile("s_branch 2f\n1:\n\ts_sleep 1\n2:\n\ts_memrealtime vcc\n\ts_waitcnt lgkmcnt(0)\n\ts_lshl_b32 vcc_lo, vcc_lo, 4\n\ts_sub_u32 vcc_lo, vcc_lo, %0\n\ts_cmp_lt_i32 vcc_lo, 0\n\ts_cbranch_scc1 1b\n\t" \
               "s_addk_i32 vcc_lo, %2\n\ts_max_i32 vcc_lo, vcc_lo, 0\n\ts_add_u32 %0, %0, %1\n\ts_add_u32 %0, %0, vcc_lo" \
               : "+s"(acc) : "s"(step), "n"(-(GSLIP)) : "scc", "vcc", "memory")
constexpr int SPIN_CAP = 1 << 18;
#define LD_SC1(off) __builtin_amdgcn_raw_buffer_load_b128(rws, (off), 0, AUX_SC1)
#define ST_SC1(v, off) __builtin_amdgcn_raw_buffer_store_b128((v), rws, (off), 0, AUX_SC1)
__device__ __forceinline__ v4u gran2(float a, float b, unsigned tg) { return v4u{__float_as_uint(a), tg, __float_as_uint(b), tg}; }
__device__ __forceinline__ unsigned gbad(v4u g, unsigned tg) { return (g.y ^ tg) | (g.w ^ tg); }
__device__ __forceinline__ f32x2 gval(v4u g) { return f32x2{__uint_as_float(g.x), __uint_as_float(g.z)}; }
__device__ __forceinline__ v4u gran_d(f32x4 d, int l, unsigned tg) { return gran2((l & 8) ? d[2] : d[0], (l & 8) ? d[3] : d[1], tg); }

#define PSTAMP(k) STAMP(k)
#define STAMP(k) do { if ((k) == PROBE && w >= PW0 && w < PW1) { const unsigned t_ = now_ticks() - t_in; if (l == 0) *(volatile LAS unsigned*)(L + L_END + w * 4) = t_; LDSWAIT(); } } while (0)
#define FLUSH() do { } while (0)

__device__ __forceinline__ void poll2(__amdgpu_buffer_rsrc_t rws, int o0, int o1, unsigned tg, v4u& r0, v4u& r1) {
  bool need = true;
#pragma nounroll
  for (int spin = 0; spin < SPIN_CAP && need; ++spin) {
    r0 = LD_SC1(o0); r1 = LD_SC1(o1);
    need = (r0.y == tg) | (r0.w == tg) | (r1.y == tg) | (r1.w == tg);
    asm volatile("" ::: "memory");
  }
}
__device__ __forceinline__ unsigned notag(unsigned x, unsigned tg) { return x == tg ? x ^ 1u : x; }
__device__ __forceinline__ float gate_of(v4u xv, v4u gv) {
  float gs = 0.f;
#pragma unroll
  for (int j = 0; j < 4; ++j) { gs += rbf(bf_lo(xv[j]) * bf_lo(gv[j])); gs += rbf(bf_hi(xv[j]) * bf_hi(gv[j])); }
  gs += dpp<0xB1>(gs); gs += dpp<0x4E>(gs); gs += dpp<0x124>(gs); gs += dpp<0x128>(gs); gs = xrow_sum(gs);
  return rbf(1.f / (1.f + __expf(-rbf(gs))));
}

struct Args {
  const unsigned short *x, *wq, *wo, *w1, *b1, *w2, *b2, *gw;
  const unsigned char *kc, *vc;
  unsigned char* ws;
  unsigned short* out;
};

typedef unsigned v16u __attribute__((ext_vector_type(16)));
typedef unsigned v8u __attribute__((ext_vector_type(8)));

__global__ void __launch_bounds__(NWAVE * 64) fused_decode(Args a_) {
  v16u ka0; v8u ka1; u64 t_in64;
  asm volatile("s_load_dwordx16 %0, %3, 0x0\n\ts_load_dwordx8 %1, %3, 0x40\n\ts_memrealtime %2" : "=&s"(ka0), "=&s"(ka1), "=&s"(t_in64) : "s"(__builtin_amdgcn_kernarg_segment_ptr()));
  __shared__ __attribute__((aligned(16))) unsigned char smem[L_END + 64];
  LAS unsigned char* L = (LAS unsigned char*)smem;
  const unsigned lbase = (unsigned)(uintptr_t)L;
  const int tid = threadIdx.x, l = tid & 63, w = __builtin_amdgcn_readfirstlane(tid >> 6);
  const int c = blockIdx.x, xr = c >> 3, sp = xr & 3, kvh = xr >> 2, b = (c - kvh) & 7, i32 = b * 4 + sp;
  {
    u64 pc;
    asm volatile("s_getpc_b64 %0" : "=s"(pc));
    const v4u rc = make_srd((const void*)(pc & ~255ull), CODEB);
    DMA_LOOP("", rc, ((xr * 2 + w) & (CODEKB - 1)) * 1024 + l * 16, 0, lbase + L_NIL, (12u >> w) & 1u);
  }
  const int g = l >> 4;
  const int oh = (c & 7) * 4 + (xr >> 3), on = xr & 7;
  const bool ffn2 = xr >= 24 && (c & 7) < 7;
  const bool isq = xr < 16, side = xr < 23 || ffn2;
  const int tl = isq ? ((xr >> 1) * 8 + ((c + xr) & 7)) * 2 + (xr & 1) : (c & 7) * 7 + xr - 16;
  const int cb = ffn2 ? on : tl >> 1, kh = ffn2 ? c & 7 : tl & 1;
#define KP(v, i) ((u64)v[i] | ((u64)v[(i) + 1] << 32))
  if (__builtin_expect(w == 0, 1)) {
    const unsigned ldb = isq ? 8192u : ffn2 ? 1024u : (unsigned)FF * 2u;
    unsigned woff = (unsigned)kh * 256u * ldb + cb * 128, wlen = 255u * ldb + 128u, wstep = 8u * ldb, wvo = (unsigned)(l >> 3) * ldb + (unsigned)(l & 7) * 16u;
    unsigned nw = side ? 32 : 0;
    asm volatile("s_waitcnt lgkmcnt(0)" : "+s"(ka0), "+s"(woff), "+s"(wlen), "+s"(wstep), "+s"(nw), "+v"(wvo) :: "memory");
    u64 pq = KP(ka0, 2), p1 = KP(ka0, 6), p2 = KP(ka0, 10);
    asm volatile("" : "+s"(pq), "+s"(p1), "+s"(p2));
    DMA_LOOP("nt", make_srd((const unsigned char*)(isq ? pq : ffn2 ? p2 : p1) + woff, wlen), wvo, wstep, lbase + L_WT, nw);
    DMA_LOOP("", make_srd((const void*)KP(ka0, 0), 8192), l * 16, 1024, lbase + L_X, nw >> 2);
  }
  asm volatile("s_waitcnt lgkmcnt(0)" : "+s"(t_in64), "+s"(ka0), "+s"(ka1) :: "memory");
  const unsigned t_in = (unsigned)t_in64;
  Args a;
  a.x = (const unsigned short*)KP(ka0, 0); a.wq = (const unsigned short*)KP(ka0, 2); a.wo = (const unsigned short*)KP(ka0, 4); a.w1 = (const unsigned short*)KP(ka0, 6);
  a.b1 = (const unsigned short*)KP(ka0, 8); a.w2 = (const unsigned short*)KP(ka0, 10); a.b2 = (const unsigned short*)KP(ka0, 12); a.gw = (const unsigned short*)KP(ka0, 14);
  a.kc = (const unsigned char*)KP(ka1, 0); a.vc = (const unsigned char*)KP(ka1, 2); a.ws = (unsigned char*)KP(ka1, 4); a.out = (unsigned short*)KP(ka1, 6);
  unsigned char* ws = a.ws;
  if (l == 0) *(volatile LAS unsigned*)(L + L_END + w * 4) = 0;
  PSTAMP(21);
  const __amdgpu_buffer_rsrc_t rws = __builtin_amdgcn_make_buffer_rsrc((void*)ws, 0, WS_END, 0x00020000);
  const int rr = c, rb = rr & 7, rc = rr >> 3;

  if (w == 0) {
    if (side) {
      const u64 tag = read_tag(ws);
      const unsigned tg = (unsigned)tag;
      float og = 0.f;
      if (ffn2) {
        const unsigned bb0 = *(const GAS unsigned*)(a.b1 + kh * 256 + l * 2), bb1 = *(const GAS unsigned*)(a.b1 + kh * 256 + 128 + l * 2);
        const v4u gv = *(const GAS v4u*)(a.gw + l * 8);
        DRAIN();
        float gt = 0.f;
#pragma nounroll
        for (int j = 0; j < 8; ++j) { const float t = gate_of(*(const LAS v4u*)(L + L_X + j * 1024 + l * 16), gv); gt = (l & 7) == j ? t : gt; }
        if (l < 8) *(LAS float*)(L + L_G + l * 4) = gt;
        og = rbf(1.f - gt);
        WAIT_TICK(t_in, TH);
        int spin = 0;
#pragma nounroll
        for (int hf = 0; hf < 2;) {
          v4u t[16];
#pragma unroll
          for (int i = 0; i < 16; ++i) {
            t[i] = __builtin_amdgcn_raw_buffer_load_b128(rws, WS_HP + kh * 32768 + hf * 16384 + l * 16, i * 1024, EAL);
            asm volatile("s_sleep %0" :: "n"(GH) : "memory");
          }
          unsigned bad = 0;
#pragma unroll
          for (int j = 0; j < 8; ++j) {
            const int i0 = (j >> 1) * 4 + (j & 1);
            const unsigned bb = (j & 1) ? bb1 : bb0;
            bad |= gbad(t[i0], tg) | gbad(t[i0 + 2], tg);
            const f32x2 z = gval(t[i0]) + gval(t[i0 + 2]);
            const float h0 = rbf(rbf(z.x) + bf_lo(bb)), h1 = rbf(rbf(z.y) + bf_hi(bb));
            const float r0 = h0 > 0.f ? h0 : 0.f, r1 = h1 > 0.f ? h1 : 0.f;
            *(LAS unsigned*)(L + L_H + (hf * 4 + (j >> 1)) * 1024 + (j & 1) * 256 + l * 4) = cvtpk(r0 * r0, r1 * r1);
          }
          if (__builtin_amdgcn_ballot_w64(bad != 0) == 0 || ++spin > SPIN_CAP) ++hf;
        }
        LDSWAIT();
        if (l == 0) *(volatile LAS u64*)(L + L_FLAG + 8) = tag;
      }
      DRAIN();
      STAMP(1);
      PSTAMP(23);
      v4u xb[8];
      const LAS unsigned char* xp = L + (ffn2 ? L_H : L_X + kh * 512) + (l & 7) * 1024 + g * 16;
#pragma unroll
      for (int k = 0; k < 8; ++k) xb[k] = *(const LAS v4u*)(xp + k * 64);
      const int off = (isq ? WS_QP + (cb >> 3) * 65536 + (l & 7) * 8192 + kh * 4096 + (cb & 7) * 512 : WS_HP + (cb >> 2) * 32768 + (l & 7) * 4096 + kh * 2048 + (cb & 3) * 512)
                      + g * 32 + (l & 8) * 2;
#pragma nounroll
      for (int ct = 0; ct < 4; ++ct) {
        f32x4 d = {0.f, 0.f, 0.f, 0.f};
#pragma unroll
        for (int k = 0; k < 8; ++k) d = mfma32(tile_frag(L + L_WT, l, k, ct), xb[k], d);
        if (ffn2) *(LAS f32x4*)(L + L_FF + ct * 1024 + l * 16) = d * og;
        else __builtin_amdgcn_raw_buffer_store_b128(gran_d(d, l, tg), rws, off + ct * 128, 0, EAS);
      }
      LDSWAIT();
      PSTAMP(24);
    }
    STAMP(2);
    FLUSH();
    return;
  }
  if (w == 1) {
    const v4u xv = *(const GAS v4u*)(a.x + rb * DM + l * 8), gv = *(const GAS v4u*)(a.gw + l * 8);
    const v2u bb = *(const GAS v2u*)(a.b2 + rc * 16 + 4 * (l & 3));
    if (TOUCH) {
      const v4u rt = make_srd(a.wo + oh * 128 * DM + on * 64, 16);
      DMA_LOOP("nt", rt, l * 16, 0, lbase + L_NIL, 1);
      const v4u ru = make_srd(a.out, 16);
      DMA_LOOP("nt", ru, l * 16, 0, lbase + L_NIL, 1);
    }
    const u64 tag = read_tag(ws);
    const unsigned tg = (unsigned)tag, th = (unsigned)(tag >> 32);
    if (l < 8) __builtin_amdgcn_raw_buffer_store_b128(v4u{t_in, tg, t_in, tg}, rws, WS_X + c * 128 + l * 16, 0, EAS);
    {
      const float gt = gate_of(xv, gv), og = rbf(1.f - gt);
      if (l < 4) *(LAS f32x4*)(L + L_FIN + l * 16) = f32x4{bf_lo(bb.x), bf_hi(bb.x), bf_lo(bb.y), bf_hi(bb.y)} * og;
      if (l == 0) *(LAS float*)(L + L_FIN + 64) = gt;
    }
    WAIT_TICK(t_in, TQ);
    const v2u st = __builtin_amdgcn_raw_buffer_load_b64(rws, WS_X + (l & 7) * 128, 0, AUX_SC1);
#pragma nounroll
    for (int spin = 0; spin < SPIN_CAP; ++spin) {
      v4u t[8];
#pragma unroll
      for (int i = 0; i < 8; ++i) { asm volatile("s_sleep %0" :: "n"(GQ) : "memory"); t[i] = __builtin_amdgcn_raw_buffer_load_b128(rws, WS_QP + kvh * 65536 + b * 8192 + i * 1024 + l * 16, 0, EAL); }
      unsigned bad = 0;
#pragma unroll
      for (int j = 0; j < 4; ++j) {
        bad |= gbad(t[j], tg) | gbad(t[4 + j], tg);
        const f32x2 q = gval(t[j]) + gval(t[4 + j]);
        *(LAS unsigned*)(L + L_Q + j * 256 + l * 4) = cvtpk(q.x, q.y);
      }
      if (__builtin_amdgcn_ballot_w64(bad != 0) == 0) break;
      asm volatile("s_sleep 8" ::: "memory");
    }
    unsigned dtv = DTV;
    {
      int late = st.y == tg ? (int)(t_in - st.x) : 0;
      late = max(late, (int)dppu<0xB1>((unsigned)late)); late = max(late, (int)dppu<0x4E>((unsigned)late)); late = max(late, (int)dppu<0x141>((unsigned)late));
      late = min(late, 100);
      if (SKEW >= 0) dtv = __builtin_amdgcn_readfirstlane(DTV - (late - SKEW) / 2);
      if (l == 0) *(LAS unsigned*)(L + L_FLAG + 16) = dtv;
    }
    LDSWAIT();
    if (l == 0) *(volatile LAS u64*)(L + L_FLAG) = tag;
    STAMP(3);
    if (ffn2) {
#pragma nounroll
      for (int spin = 0; spin < SPIN_CAP * 16; ++spin) { if (*(volatile const LAS u64*)(L + L_FLAG + 8) == tag) break; asm volatile("s_sleep 8" ::: "memory"); }
    }
    DRAIN();
    if (l == 0) __builtin_amdgcn_raw_buffer_store_b64(v2u{tg, th}, rws, WS_F + c * 8, 0, AUX_SC1);
    STAMP(5);
    v4u ml;
#pragma nounroll
    for (int spin = 0; spin < SPIN_CAP * 16; ++spin) {
      ml = *(volatile const LAS v4u*)(L + L_ML + (l & 31) * 16);
      if (__builtin_amdgcn_ballot_w64(((ml.z ^ tg) | (ml.w ^ th)) != 0) == 0) break;
      asm volatile("s_sleep 4" ::: "memory");
    }
    const float mw = __uint_as_float(ml.x);
    float M = fmaxf(mw, dpp<0xB1>(mw)); M = fmaxf(M, dpp<0x4E>(M)); M = fmaxf(M, dpp<0x141>(M));
    float Ls = __builtin_amdgcn_exp2f(mw - M) * __uint_as_float(ml.y);
    Ls += dpp<0xB1>(Ls); Ls += dpp<0x4E>(Ls); Ls += dpp<0x141>(Ls);
    const float lse = M + __builtin_amdgcn_logf(Ls);
    const v4u lg = gran2(lse, dpp<0x128>(lse), tg);
    if (l < 32 && (l & 15) == 0) ST_SC1(lg, WS_LSE + ((b * 8 + kvh) * 4 + sp) * 128 + l);
    STAMP(6);
    v2u ls;
    asm volatile("s_sleep 16" ::: "memory");
#pragma nounroll
    for (int spin = 0; spin < SPIN_CAP; ++spin) {
      ls = __builtin_amdgcn_raw_buffer_load_b64(rws, WS_LSE + ((b * 8 + kvh) * 4 + (l & 3)) * 128 + ((l >> 3) & 3) * 8, 0, AUX_SC1);
      if (__builtin_amdgcn_ballot_w64(ls.y != tg) == 0) break;
      asm volatile("s_sleep 8" ::: "memory");
    }
    const float lf = __uint_as_float(ls.x);
    float M2 = fmaxf(lf, dpp<0xB1>(lf)); M2 = fmaxf(M2, dpp<0x4E>(M2));
    float E = __builtin_amdgcn_exp2f(lf - M2);
    E += dpp<0xB1>(E); E += dpp<0x4E>(E);
    if (l < 32) *(LAS float*)(L + L_CF + l * 4) = __builtin_amdgcn_exp2f(mw - M2) * __builtin_amdgcn_rcpf(E);
    WAIT_TICK(t_in, TF);
#pragma nounroll
    for (int spin = 0; spin < SPIN_CAP; ++spin) {
      const v4u f0 = LD_SC1(WS_F + l * 16), f1 = LD_SC1(WS_F + 1024 + l * 16);
      if (__builtin_amdgcn_ballot_w64(((f0.x ^ tg) | (f0.y ^ th) | (f0.z ^ tg) | (f0.w ^ th) | (f1.x ^ tg) | (f1.y ^ th) | (f1.z ^ tg) | (f1.w ^ th)) != 0) == 0) break;
      asm volatile("s_sleep 16" ::: "memory");
    }
    LDSWAIT();
    STAMP(7);
    if (LATEWO) {
      const v4u rwo = make_srd(a.wo + oh * 128 * DM + on * 64, 127 * 1024 + 128);
      const unsigned wov = (unsigned)(l >> 3) * 1024u + (unsigned)(l & 7) * 16u;
      if (WOPRE > 0) {
#pragma nounroll
        for (int spin = 0; spin < SPIN_CAP * 16; ++spin) {
          if (__builtin_amdgcn_ballot_w64(*(volatile const LAS u64*)(L + L_DONE + (l & 7) * 8) != tag) == 0) break;
          asm volatile("s_sleep 1" ::: "memory");
        }
        asm volatile("s_sleep %0" :: "n"(WOS) : "memory");
        DMA_LOOP("nt", rwo, wov, 8192, lbase + L_WO, WOPRE);
      }
      WGBAR();
      if (WOPRE < 16) DMA_LOOP("nt", rwo, wov + WOPRE * 8192, 8192, lbase + L_WO + WOPRE * 1024, 16 - WOPRE);
      DRAIN();
      if (l == 0) *(volatile LAS u64*)(L + L_FLAG + 24) = tag;
      LDSWAIT();
      STAMP(8);
    }
    FLUSH();
    return;
  }

  const int sw = w - 2;
  unsigned tnext = ((t_in + T0) << 4) + (STAG == 1 ? sw * (DT / 8) : STAG == 2 ? (xr & 7) * (DT / 8) : STAG == 3 ? ((xr >> 2) & 7) * (DT / 8) : 0), dt = DT;
#define PACE_TO(j) do { asm volatile("s_waitcnt vmcnt(%0)" :: "n"(WIN - 1) : "memory"); if ((j) < GEND) WAIT_SLIP(tnext, dt); else WAIT_NEXT(tnext, dt); } while (0)
  const size_t base = ((size_t)(b * HKV + kvh) * SK + sp * 1024 + sw * 128) * 256;
  const v4u rk = make_srd(a.kc + base, 32768), rv = make_srd(a.vc + base, 32768);
  const int ko = ((l >> 2) & 3) * 256 + g * 64 + (l & 3) * 16, vo = l * 16;
  const int rot = (ROTW * sw + (ROTF)) & 31;
#define OFF(i) ((((i) + rot) & 31) * 1024)
#define LOAD_V(i) asm volatile("buffer_load_dwordx4 %0, %1, %2, %3 offen nt" : "+v"(kv[i]) : "v"(vo), "s"(rv), "s"(OFF(i)) : "memory")
  if (!LATEWO) {
    const v4u ro = make_srd(a.wo + (oh * 128 + sw * 16) * DM + on * 64, 15 * 1024 + 128);
    const unsigned oo = (unsigned)(l >> 3) * 1024u + (unsigned)(l & 7) * 16u;
    PACE_TO(0); dma_rows(ro, oo, 0, lbase + L_WO + sw * 2048);
    PACE_TO(1); dma_rows(ro, oo, 8192, lbase + L_WO + sw * 2048 + 1024);
  }
  if (TOUCH) {
    v4u r1 = rk, r2 = rv;
    r1.z = 16; r2.z = 16;
    DMA_LOOP("nt", r1, l * 16, 0, lbase + L_NIL, 1);
    DMA_LOOP("nt", r2, l * 16, 0, lbase + L_NIL, 1);
  }
  v4u kv[32];
  __builtin_amdgcn_sched_barrier(0);
#pragma unroll
  for (int i = 0; i < 32; ++i) {
    PACE_TO(2 + i);
    asm volatile("buffer_load_dwordx4 %0, %1, %2, %3 offen nt" : "=v"(kv[i]) : "v"(ko), "s"(rk), "s"(OFF(i)) : "memory");
    PSTAMP(100 + i);
    if (SLIP >= 0 && (i == WIN || i == 2 * WIN)) { const unsigned t1 = (now_ticks() - SLIP) << 4; if ((int)(t1 - tnext) > 0) tnext = t1 + sw * (DT / 16); }
    __builtin_amdgcn_sched_barrier(0);
  }
  STAMP(1);
  const u64 tag = read_tag(ws);
  const unsigned tg = (unsigned)tag;
#pragma nounroll
  for (int spin = 0; spin < SPIN_CAP * 16; ++spin) { if (*(volatile const LAS u64*)(L + L_FLAG) == tag) break; asm volatile("s_sleep 2" ::: "memory"); }
  asm volatile("" ::: "memory");
  STAMP(2);
  const v4u qf = *(const LAS v4u*)(L + L_Q + ((l >> 2) & 3) * 256 + g * 64 + (l & 3) * 16);
  dt = __builtin_amdgcn_readfirstlane(*(const LAS unsigned*)(L + L_FLAG + 16));
  __builtin_amdgcn_sched_barrier(0);
  constexpr float C2 = 0.088388347648318440f * 1.4426950408889634f;
  float sc[8];
  const int s = l & 3;
#pragma unroll
  for (int m = 0; m < 8; ++m) {
    asm volatile("s_waitcnt vmcnt(%4)" : "+v"(kv[4 * m]), "+v"(kv[4 * m + 1]), "+v"(kv[4 * m + 2]), "+v"(kv[4 * m + 3]) : "n"(28 - 4 * m + m) : "memory");
    f32x4 d[4];
#pragma unroll
    for (int r = 0; r < 4; ++r) d[r] = mfma32(kv[4 * m + r], qf, f32x4{0.f, 0.f, 0.f, 0.f});
#pragma unroll
    for (int r = 0; r < 4; ++r) {
      float t = dpp<0x00>(d[r][0]); t = dpp<0x55>(d[r][1]) + t; t = dpp<0xAA>(d[r][2]) + t; t = dpp<0xFF>(d[r][3]) + t;
      asm volatile("" : "+v"(t));
      sc[m] = (r == 0 || s == r) ? t : sc[m];
    }
    asm volatile("" : "+v"(sc[m]));
    __builtin_amdgcn_sched_barrier(0);
    PACE_TO(34 + m);
    LOAD_V(m);
    __builtin_amdgcn_sched_barrier(0);
  }
  STAMP(3);
  float mx = sc[0];
#pragma unroll
  for (int m = 1; m < 8; ++m) mx = fmaxf(mx, sc[m]);
  mx = fmaxf(mx, dpp<0xB1>(mx)); mx = fmaxf(mx, dpp<0x4E>(mx)); mx = xrow_max(mx);
  const float mneg = -mx * C2;
  float lsum = 0.f;
  unsigned pk[8];
#pragma unroll
  for (int m = 0; m < 8; ++m) { const float p = __builtin_amdgcn_exp2f(fmaf(sc[m], C2, mneg)); pk[m] = cvtpk(p, p); lsum += bf_lo(pk[m]); }
  lsum += dpp<0xB1>(lsum); lsum += dpp<0x4E>(lsum); lsum = xrow_sum(lsum);
  if (l < 16 && (l & 3) == 0) *(LAS v4u*)(L + L_ML + ((l >> 2) * 8 + sw) * 16) = v4u{__float_as_uint(mx * C2), __float_as_uint(lsum), tg, (unsigned)(tag >> 32)};
  const unsigned half = (l & 1) ? 0xffff0000u : 0x0000ffffu;
  const unsigned m0 = (l & 2) ? 0u : half, m1 = (l & 2) ? half : 0u;
  f32x4 o0 = {0.f, 0.f, 0.f, 0.f}, o1 = {0.f, 0.f, 0.f, 0.f};
#pragma unroll
  for (int j = 8; j < 40; ++j) {
    if (j < 32) { PACE_TO(34 + j); LOAD_V(j); __builtin_amdgcn_sched_barrier(0); }
    if (j == 31) { if (l == 0) *(volatile LAS u64*)(L + L_DONE + sw * 8) = tag; STAMP(11); }
    const int i = j - 8;
    const unsigned pb = (i & 3) == 0 ? dppu<0x00>(pk[i >> 2]) : (i & 3) == 1 ? dppu<0x55>(pk[i >> 2]) : (i & 3) == 2 ? dppu<0xAA>(pk[i >> 2]) : dppu<0xFF>(pk[i >> 2]);
    const v2u av = {pb & m0, pb & m1};
    asm volatile("s_waitcnt vmcnt(%1)" : "+v"(kv[i]) : "n"((j < 32 ? j : 31) - i) : "memory");
    o0 = mfma16(av, v2u{kv[i].x, kv[i].y}, o0);
    o1 = mfma16(av, v2u{kv[i].z, kv[i].w}, o1);
    __builtin_amdgcn_sched_barrier(0);
  }
  *(LAS f32x4*)(L + L_MRG + (sw * 4 + g) * 512 + (l & 15) * 32) = o0;
  *(LAS f32x4*)(L + L_MRG + (sw * 4 + g) * 512 + (l & 15) * 32 + 16) = o1;
  STAMP(4);
  LDSWAIT();
  WGBAR();
  if (sw < 4) {
    STAMP(5);
    const f32x4 c0 = *(const LAS f32x4*)(L + L_CF + sw * 32), c1 = *(const LAS f32x4*)(L + L_CF + sw * 32 + 16);
    f32x2 acc = {0.f, 0.f};
#pragma unroll
    for (int j = 0; j < 8; ++j) {
      const f32x2 ov = *(const LAS f32x2*)(L + L_MRG + (j * 4 + sw) * 512 + l * 8);
      const float e = j < 4 ? c0[j & 3] : c1[j & 3];
      acc.x = fmaf(e, ov.x, acc.x); acc.y = fmaf(e, ov.y, acc.y);
    }
    const unsigned pv = notag(cvtpk(acc.x, acc.y), tg);
    const v4u pq = {dppu<0x00>(pv), dppu<0x55>(pv), dppu<0xAA>(pv), dppu<0xFF>(pv)};
    if ((l & 3) == 0) ST_SC1(pq, WS_P + (((kvh * 4 + sw) * 8 + b) * 4 + sp) * 256 + l * 4);
    STAMP(6);
    f32x4 ff = {0.f, 0.f, 0.f, 0.f};
    float gm = 1.f;
    if (ffn2) { ff = *(const LAS f32x4*)(L + L_FF + sw * 1024 + l * 16); gm = *(const LAS float*)(L + L_G + (l & 7) * 4); }
    const int po = WS_P + (oh * 8 + 2 * sw) * 1024 + l * 16;
    asm volatile("s_sleep %0" :: "n"(PDELAY1) : "memory");
    v4u p0 = LD_SC1(po), p1 = LD_SC1(po + 1024);
    if (LATEWO) {
#pragma nounroll
      for (int spin = 0; spin < SPIN_CAP * 16; ++spin) { if (*(volatile const LAS u64*)(L + L_FLAG + 24) == tag) break; }
      asm volatile("" ::: "memory");
    }
    v4u wf[4];
#pragma unroll
    for (int k = 0; k < 4; ++k) wf[k] = tile_frag(L + L_WO, l, k, sw);
    __builtin_amdgcn_sched_barrier(0);
    if ((p0.y == tg) | (p0.w == tg) | (p1.y == tg) | (p1.w == tg)) poll2(rws, po, po + 1024, tg, p0, p1);
    *(LAS v4u*)(L + L_PG + (8 * sw + g) * 272 + (l & 15) * 16) = p0;
    *(LAS v4u*)(L + L_PG + (8 * sw + 4 + g) * 272 + (l & 15) * 16) = p1;
    STAMP(7);
    LDSWAIT();
    WGBAR();
    const int pb = l & 7, sl = (l >> 3) & 1;
    v4u pa[8];
#pragma unroll
    for (int k = 0; k < 8; ++k) pa[k] = *(const LAS v4u*)(L + L_PG + (pb * 4 + (k >> 2) * 2 + sl) * 272 + (k & 3) * 64 + g * 16);
    __builtin_amdgcn_sched_barrier(0);
    f32x4 da = {0.f, 0.f, 0.f, 0.f}, db = {0.f, 0.f, 0.f, 0.f};
#pragma unroll
    for (int k = 0; k < 4; ++k) { da = mfma32(wf[k], pa[k], da); db = mfma32(wf[k], pa[4 + k], db); }
    v4u r;
#pragma unroll
    for (int j = 0; j < 4; ++j) { const float t = da[j] + db[j]; r[j] = notag(__float_as_uint(fmaf(t + dpp<0x128>(t), gm, ff[j])), tg); }
    if (sl == 0) ST_SC1(r, WS_O + (((on * 4 + sw) * 4 + (pb >> 1)) * 32 + oh) * 128 + (pb & 1) * 64 + g * 16);
    STAMP(8);
    if (sw == 0) {
      const f32x4 fin = *(const LAS f32x4*)(L + L_FIN + (l & 3) * 16);
      const float gt = *(const LAS float*)(L + L_FIN + 64);
      const float g0 = (l & 12) == 12 ? 1.f : gt, g1 = l < 48 ? g0 : gt;
      const int oo = WS_O + (rr >> 1) * 4096 + (l >> 2) * 128 + (rr & 1) * 64 + (l & 3) * 16;
      v4u r0, r1;
      asm volatile("s_sleep %0" :: "n"(PDELAY2) : "memory");
      poll2(rws, oo, oo + 2048, tg, r0, r1);
      STAMP(9);
      f32x4 av = __builtin_bit_cast(f32x4, r0) * g0 + __builtin_bit_cast(f32x4, r1) * g1;
#pragma unroll
      for (int j = 0; j < 4; ++j) av[j] += dpp<0x124>(av[j]);
#pragma unroll
      for (int j = 0; j < 4; ++j) av[j] += dpp<0x128>(av[j]);
      {
        auto s0 = __builtin_amdgcn_permlane16_swap(__float_as_uint(av[0]), __float_as_uint(av[1]), false, false);
        auto s1 = __builtin_amdgcn_permlane16_swap(__float_as_uint(av[2]), __float_as_uint(av[3]), false, false);
        const float u0 = __uint_as_float(s0[0]) + __uint_as_float(s0[1]), u1 = __uint_as_float(s1[0]) + __uint_as_float(s1[1]);
        auto s2 = __builtin_amdgcn_permlane32_swap(__float_as_uint(u0), __float_as_uint(u1), false, false);
        const float v = __uint_as_float(s2[0]) + __uint_as_float(s2[1]);
        const float o = v + (g == 0 ? fin[0] : g == 1 ? fin[1] : g == 2 ? fin[2] : fin[3]);
        if ((l & 15) < 4) *(GAS unsigned short*)(a.out + rb * DM + rc * 16 + 4 * (l & 3) + g) = (unsigned short)cvtpk(o, o);
      }
      if (c == 0 && l == 0) *(GAS u64*)(ws + WS_E) = tag;
      STAMP(10);
      { unsigned m = 0;
        for (int i = PW0; i < PW1; ++i) m = max(m, *(volatile LAS unsigned*)(L + L_END + i * 4));
        m = __builtin_amdgcn_readfirstlane(m);
        if (now_ticks() - t_in > PSLOW) m += 2500;
        unsigned tq = (t_in + 4000 + 4 * m) << 4; WAIT_NEXT(tq, dt); }
    }
  }
  FLUSH();
}

extern "C" void kernel_launch(void* const* d_in, const int* in_sizes, int n_in, void* d_out, int out_size, void* d_ws, size_t ws_size,
                              hipStream_t stream) {
  Args a;
  a.x = (const unsigned short*)d_in[0];
  a.kc = (const unsigned char*)d_in[1];
  a.vc = (const unsigned char*)d_in[2];
  a.wq = (const unsigned short*)d_in[3];
  a.wo = (const unsigned short*)d_in[4];
  a.w1 = (const unsigned short*)d_in[6];
  a.b1 = (const unsigned short*)d_in[7];
  a.w2 = (const unsigned short*)d_in[8];
  a.b2 = (const unsigned short*)d_in[9];
  a.gw = (const unsigned short*)d_in[10];
  a.ws = (unsigned char*)d_ws;
  a.out = (unsigned short*)d_out;
  fused_decode<<<256, NWAVE * 64, 0, stream>>>(a);
}
```

```cpp
#define UP1 0
#define UP2 30
#include <hip/hip_runtime.h>
#include <hip/hip_bf16.h>
#include <stdint.h>

typedef unsigned v4u __attribute__((ext_vector_type(4)));
typedef unsigned v2u __attribute__((ext_vector_type(2)));
typedef float f32x4 __attribute__((ext_vector_type(4)));
typedef float f32x2 __attribute__((ext_vector_type(2)));
typedef __bf16 bf16x8 __attribute__((ext_vector_type(8)));
typedef __bf16 bf16x2 __attribute__((ext_vector_type(2)));
typedef short s16x4 __attribute__((ext_vector_type(4)));
typedef unsigned long long u64;
#define GAS __attribute__((address_space(1)))
#define LAS __attribute__((address_space(3)))

constexpr int DM = 512, HKV = 8, SK = 4096, FF = 1792;
constexpr int AUX_SC1 = 16;
#define DT 465
#define DTV 440
#define T0 130
#define WIN 5
#define SLIP 40
#define DN2 40
#define JA 24
#define TQ 560
#define TH 480
#define TF 1500
#define GQ 6
#define GH 3
#define WOS 6
#define PDELAY1 13
#define PDELAY2 15
#define CODEB 15360
#define AUXE 18
constexpr int AUX_E = AUXE;
#define EBAR 1
#define EBAR0 ""
#define SLIP0 30
#define RELQ 1
constexpr int NWAVE = 10;

constexpr int WS_E = 0;
constexpr int WS_F = 2048;
constexpr int WS_QP = 4096;
constexpr int WS_HP = WS_QP + 8 * 8 * 2 * 512 * 8;
constexpr int WS_X = WS_HP + 7 * 8 * 2 * 256 * 8;
constexpr int WS_LSE = WS_X + 256 * 128;
constexpr int WS_END = WS_LSE + 256 * 128;
constexpr int WS_O = WS_QP;
constexpr int WS_P = WS_HP;
static_assert(WS_LSE - WS_P == 32 * 8 * 4 * 256, "the partials fill exactly the lines of HP and X");
constexpr int WS_ST = 8 << 20;
constexpr int L_WO = 0, L_WT = 16384, L_MRG = 49152, L_Q = L_MRG + 16384, L_X = L_Q + 1024, L_H = L_X + 8192, L_NIL = L_H + 8192, L_FF = L_NIL + 1024,
              L_CF = L_FF + 4096, L_G = L_CF + 128, L_FIN = L_G + 32,
              L_SYNC = L_FIN + 96,
              L_ML = L_SYNC, L_QRDY = L_ML + 512, L_TEND = L_QRDY + 4, L_HRDY = L_QRDY + 8, L_WRDY = L_QRDY + 12, L_ORG = L_QRDY + 16, L_DONE = L_QRDY + 32,
              L_END = L_SYNC + 1024;
constexpr int L_PG = L_WT;

__device__ __forceinline__ f32x4 mfma32(v4u a, v4u b, f32x4 c) {
  return __builtin_amdgcn_mfma_f32_16x16x32_bf16(__builtin_bit_cast(bf16x8, a), __builtin_bit_cast(bf16x8, b), c, 0, 0, 0);
}
__device__ __forceinline__ f32x4 mfma16(v2u a, v2u b, f32x4 c) {
  return __builtin_amdgcn_mfma_f32_16x16x16bf16_1k(__builtin_bit_cast(s16x4, a), __builtin_bit_cast(s16x4, b), c, 0, 0, 0);
}
__device__ __forceinline__ unsigned cvtpk(float lo, float hi) { bf16x2 r = {(__bf16)lo, (__bf16)hi}; return __builtin_bit_cast(unsigned, r); }
__device__ __forceinline__ float bf_lo(unsigned u) { return __builtin_bit_cast(float, u << 16); }
__device__ __forceinline__ float bf_hi(unsigned u) { return __builtin_bit_cast(float, u & 0xffff0000u); }
__device__ __forceinline__ float rbf(float x) { return (float)(__bf16)x; }
template <int CTRL> __device__ __forceinline__ float dpp(float x) { return __builtin_bit_cast(float, __builtin_amdgcn_mov_dpp(__builtin_bit_cast(int, x), CTRL, 0xf, 0xf, true)); }
template <int CTRL> __device__ __forceinline__ unsigned dppu(unsigned x) { return (unsigned)__builtin_amdgcn_mov_dpp((int)x, CTRL, 0xf, 0xf, true); }
__device__ __forceinline__ float xrow_max(float x) {
  auto s = __builtin_amdgcn_permlane16_swap(__float_as_uint(x), __float_as_uint(x), false, false);
  x = fmaxf(__uint_as_float(s[0]), __uint_as_float(s[1]));
  auto t = __builtin_amdgcn_permlane32_swap(__float_as_uint(x), __float_as_uint(x), false, false);
  return fmaxf(__uint_as_float(t[0]), __uint_as_float(t[1]));
}
__device__ __forceinline__ float xrow_sum(float x) {
  auto s = __builtin_amdgcn_permlane16_swap(__float_as_uint(x), __float_as_uint(x), false, false);
  x = __uint_as_float(s[0]) + __uint_as_float(s[1]);
  auto t = __builtin_amdgcn_permlane32_swap(__float_as_uint(x), __float_as_uint(x), false, false);
  return __uint_as_float(t[0]) + __uint_as_float(t[1]);
}
__device__ __forceinline__ v2u tr_read(const LAS unsigned char* p) { return __builtin_bit_cast(v2u, __builtin_amdgcn_ds_read_tr16_b64_v4i16((LAS s16x4*)p)); }
__device__ __forceinline__ v4u make_srd(const void* p, unsigned bytes) {
  const u64 a = (u64)p;
  return v4u{(unsigned)a, (unsigned)(a >> 32) & 0xffffu, bytes, 0x00020000u};
}
#define DMA_LOOP(POL, srd, voff, step, lds_addr, n) do { unsigned keep_, so_ = 0, n_ = (n); \
  asm volatile("s_cmp_eq_u32 %2, 0\n\ts_cbranch_scc1 2f\n\ts_mov_b32 %0, m0\n\ts_mov_b32 m0, %3\n\ts_nop 0\n1:\n\tbuffer_load_dwordx4 %4, %5, %1 offen " POL " lds\n\t" \
               "s_add_u32 m0, m0, 0x400\n\ts_add_u32 %1, %1, %6\n\ts_sub_u32 %2, %2, 1\n\ts_cmp_lg_u32 %2, 0\n\ts_cbranch_scc1 1b\n\ts_mov_b32 m0, %0\n2:" \
               : "=&s"(keep_), "+&s"(so_), "+&s"(n_) : "s"(lds_addr), "v"(voff), "s"(srd), "s"(step) : "scc", "memory"); } while (0)
__device__ __forceinline__ v4u tile_frag(const LAS unsigned char* tile, int l, int k, int ct) {
  const LAS unsigned char* p = tile + (8 * (l >> 4) + ((l & 15) >> 2)) * 128 + (l & 3) * 8 + k * 4096 + ct * 32;
  const v2u t0 = tr_read(p), t1 = tr_read(p + 512);
  return v4u{t0.x, t0.y, t1.x, t1.y};
}
#define DRAIN() asm volatile("s_waitcnt vmcnt(0)" ::: "memory")
#define LDSWAIT() asm volatile("s_waitcnt lgkmcnt(0)" ::: "memory")
#define WGBAR() do { __builtin_amdgcn_sched_barrier(0); __builtin_amdgcn_s_barrier(); __builtin_amdgcn_sched_barrier(0); asm volatile("" ::: "memory"); } while (0)
__device__ __forceinline__ u64 read_tag(const unsigned char* ws) {
  u64 t;
  asm volatile("s_load_dwordx2 %0, %1, 0x0\n\ts_waitcnt lgkmcnt(0)" : "=s"(t) : "s"(ws) : "memory");
  const unsigned old = (unsigned)t;
  t += 0x9E3779B97F4A7C15ull;
  t = (t ^ (t >> 30)) * 0xBF58476D1CE4E5B9ull; t = (t ^ (t >> 27)) * 0x94D049BB133111EBull;
  t ^= t >> 31;
  return (unsigned)t == old ? t ^ 1u : t;
}
__device__ __forceinline__ unsigned now_ticks() {
  u64 t;
  asm volatile("s_memrealtime %0\n\ts_waitcnt lgkmcnt(0)" : "=s"(t) :: "memory");
  return (unsigned)t;
}
#define WAIT_TICK(base, delta) \
  asm volatile("s_branch 2f\n1:\n\ts_sleep 1\n2:\n\ts_memrealtime vcc\n\ts_waitcnt lgkmcnt(0)\n\ts_sub_u32 vcc_lo, vcc_lo, %0\n\ts_cmpk_lt_i32 vcc_lo, %1\n\ts_cbranch_scc1 1b" \
               :: "s"(base), "n"(delta) : "scc", "vcc", "memory")
#define WAIT_NEXT(acc, step) \
  asm volatile("s_branch 2f\n1:\n\ts_sleep 1\n2:\n\ts_memrealtime vcc\n\ts_waitcnt lgkmcnt(0)\n\ts_lshl_b32 vcc_lo, vcc_lo, 4\n\ts_sub_u32 vcc_lo, vcc_lo, %0\n\ts_cmp_lt_i32 vcc_lo, 0\n\ts_cbranch_scc1 1b\n\ts_add_u32 %0, %0, %1" \
               : "+s"(acc) : "s"(step) : "scc", "vcc", "memory")
template <int LO, int HI> __device__ __forceinline__ int sclamp(int x) {
  asm("s_max_i32 %0, %0, %1\n\ts_min_i32 %0, %0, %2" : "+s"(x) : "n"(LO), "n"(HI) : "scc");
  return x;
}
constexpr int SPIN_CAP = 1 << 18;
#define LD_SC1(off) __builtin_amdgcn_raw_buffer_load_b128(rws, (off), 0, AUX_SC1)
#define ST_SC1(v, off) __builtin_amdgcn_raw_buffer_store_b128((v), rws, (off), 0, AUX_SC1)
__device__ __forceinline__ v4u gran2(float a, float b, unsigned tg) { return v4u{__float_as_uint(a), tg, __float_as_uint(b), tg}; }
__device__ __forceinline__ unsigned gbad(v4u g, unsigned tg) { return (g.y ^ tg) | (g.w ^ tg); }
__device__ __forceinline__ f32x2 gval(v4u g) { return f32x2{__uint_as_float(g.x), __uint_as_float(g.z)}; }
__device__ __forceinline__ v4u gran_d(f32x4 d, int l, unsigned tg) { return gran2((l & 8) ? d[2] : d[0], (l & 8) ? d[3] : d[1], tg); }

#define STAMP(k) do { } while (0)
#define FLUSH() do { } while (0)

#define PSTAMP(k) do { } while (0)
__device__ __forceinline__ void watch(__amdgpu_buffer_rsrc_t rws, int miss, unsigned tg, int& spin) {
#pragma nounroll
  while (miss >= 0 && spin < SPIN_CAP) {
    asm volatile("s_sleep 24" ::: "memory");
    if (gbad(__builtin_amdgcn_raw_buffer_load_b128(rws, miss, 0, AUX_E), tg) == 0) miss = -1;
    ++spin;
  }
  spin = __builtin_amdgcn_ballot_w64(spin >= SPIN_CAP) != 0 ? SPIN_CAP : __builtin_amdgcn_readfirstlane(spin) + 1;
}
template <int SLEEP> __device__ __forceinline__ void lds_wait(const LAS unsigned char* p) {
#pragma nounroll
  for (int spin = 0; spin < SPIN_CAP * 16; ++spin) {
    if (__builtin_amdgcn_ballot_w64(*(volatile const LAS unsigned*)p == 0u) == 0) break;
    if (SLEEP) asm volatile("s_sleep %0" :: "n"(SLEEP) : "memory");
  }
  asm volatile("" ::: "memory");
}
#define LDS_SET(off) (*(volatile LAS unsigned*)(L + (off)) = 1u)
__device__ __forceinline__ void poll2(__amdgpu_buffer_rsrc_t rws, int o0, int o1, unsigned tg, v4u& r0, v4u& r1) {
  bool need = true;
#pragma nounroll
  for (int spin = 0; spin < SPIN_CAP && need; ++spin) {
    r0 = LD_SC1(o0); r1 = LD_SC1(o1);
    need = (r0.y == tg) | (r0.w == tg) | (r1.y == tg) | (r1.w == tg);
    asm volatile("" ::: "memory");
  }
}
__device__ __forceinline__ unsigned notag(unsigned x, unsigned tg) { return x == tg ? x ^ 1u : x; }
__device__ __forceinline__ float gate_of(v4u xv, v4u gv) {
  float gs = 0.f;
#pragma unroll
  for (int j = 0; j < 4; ++j) { gs += rbf(bf_lo(xv[j]) * bf_lo(gv[j])); gs += rbf(bf_hi(xv[j]) * bf_hi(gv[j])); }
  gs += dpp<0xB1>(gs); gs += dpp<0x4E>(gs); gs += dpp<0x124>(gs); gs += dpp<0x128>(gs); gs = xrow_sum(gs);
  return rbf(1.f / (1.f + __expf(-rbf(gs))));
}

struct Args {
  const unsigned short *x, *wq, *wo, *w1, *b1, *w2, *b2, *gw;
  const unsigned char *kc, *vc;
  unsigned char* ws;
  unsigned short* out;
};

typedef unsigned v16u __attribute__((ext_vector_type(16)));
typedef unsigned v8u __attribute__((ext_vector_type(8)));

__global__ void __launch_bounds__(NWAVE * 64) fused_decode(Args a_) {
  v16u ka0; v8u ka1; u64 t_in64;
  asm volatile("s_load_dwordx16 %0, %3, 0x0\n\ts_load_dwordx8 %1, %3, 0x40\n\ts_memrealtime %2" : "=&s"(ka0), "=&s"(ka1), "=&s"(t_in64) : "s"(__builtin_amdgcn_kernarg_segment_ptr()));
  __shared__ __attribute__((aligned(16))) unsigned char smem[L_END + 64];
  LAS unsigned char* L = (LAS unsigned char*)smem;
  const unsigned lbase = (unsigned)(uintptr_t)L;
  const int tid = threadIdx.x, l = tid & 63, w = __builtin_amdgcn_readfirstlane(tid >> 6);
  const int c = blockIdx.x, xr = c >> 3, sp = xr & 3, kvh = xr >> 2, b = (c - kvh) & 7, i32 = b * 4 + sp;
  {
    u64 pc;
    asm volatile("s_getpc_b64 %0" : "=s"(pc));
    const v4u rc = make_srd((const void*)(pc & ~255ull), CODEB);
    DMA_LOOP("", rc, ((xr * 2 + w) & 15) * 1024 + l * 16, 0, lbase + L_NIL, (12u >> w) & 1u);
  }
  const int g = l >> 4;
  const int oh = (c & 7) * 4 + (xr >> 3), on = xr & 7;
  const bool ffn2 = xr >= 24 && (c & 7) < 7;
  const bool isq = xr < 16, side = xr < 23 || ffn2;
  const int tl = isq ? ((xr >> 1) * 8 + ((c + xr) & 7)) * 2 + (xr & 1) : (c & 7) * 7 + xr - 16;
  const int cb = ffn2 ? on : tl >> 1, kh = ffn2 ? c & 7 : tl & 1;
#define KP(v, i) ((u64)v[i] | ((u64)v[(i) + 1] << 32))
  if (w == 1) *(LAS v4u*)(L + L_SYNC + l * 16) = v4u{0u, 0u, 0u, 0u};
  if (__builtin_expect(w == 0, 1)) {
    const unsigned ldb = isq ? 8192u : ffn2 ? 1024u : (unsigned)FF * 2u;
    unsigned woff = (unsigned)kh * 256u * ldb + cb * 128, wlen = 255u * ldb + 128u, wstep = 8u * ldb, wvo = (unsigned)(l >> 3) * ldb + (unsigned)(l & 7) * 16u;
    unsigned nw = side ? 32 : 0;
    asm volatile("s_waitcnt lgkmcnt(0)" EBAR0 : "+s"(ka0), "+s"(woff), "+s"(wlen), "+s"(wstep), "+s"(nw), "+v"(wvo) :: "memory");
    u64 pq = KP(ka0, 2), p1 = KP(ka0, 6), p2 = KP(ka0, 10);
    asm volatile("" : "+s"(pq), "+s"(p1), "+s"(p2));
    DMA_LOOP("nt", make_srd((const unsigned char*)(isq ? pq : ffn2 ? p2 : p1) + woff, wlen), wvo, wstep, lbase + L_WT, nw);
    DMA_LOOP("", make_srd((const void*)KP(ka0, 0), 8192), l * 16, 1024, lbase + L_X, nw >> 2);
  }
  asm volatile("s_waitcnt lgkmcnt(0)" : "+s"(t_in64), "+s"(ka0), "+s"(ka1) :: "memory");
  if (EBAR || w != 0) WGBAR();
  const unsigned t_in = (unsigned)t_in64;
  Args a;
  a.x = (const unsigned short*)KP(ka0, 0); a.wq = (const unsigned short*)KP(ka0, 2); a.wo = (const unsigned short*)KP(ka0, 4); a.w1 = (const unsigned short*)KP(ka0, 6);
  a.b1 = (const unsigned short*)KP(ka0, 8); a.w2 = (const unsigned short*)KP(ka0, 10); a.b2 = (const unsigned short*)KP(ka0, 12); a.gw = (const unsigned short*)KP(ka0, 14);
  a.kc = (const unsigned char*)KP(ka1, 0); a.vc = (const unsigned char*)KP(ka1, 2); a.ws = (unsigned char*)KP(ka1, 4); a.out = (unsigned short*)KP(ka1, 6);
  unsigned char* ws = a.ws;
  PSTAMP(21);
  const __amdgpu_buffer_rsrc_t rws = __builtin_amdgcn_make_buffer_rsrc((void*)ws, 0, WS_END, 0x00020000);
  const int rr = c, rb = rr & 7, rc = rr >> 3;

  if (w == 0) {
    if (side) {
      const unsigned tg = (unsigned)read_tag(ws);
      float og = 0.f;
      if (ffn2) {
        const unsigned bb0 = *(const GAS unsigned*)(a.b1 + kh * 256 + l * 2), bb1 = *(const GAS unsigned*)(a.b1 + kh * 256 + 128 + l * 2);
        const v4u gv = *(const GAS v4u*)(a.gw + l * 8);
        DRAIN();
        const unsigned t_w = now_ticks();
        float gt = 0.f;
#pragma nounroll
        for (int j = 0; j < 8; ++j) { const float t = gate_of(*(const LAS v4u*)(L + L_X + j * 1024 + l * 16), gv); gt = (l & 7) == j ? t : gt; }
        if (l < 8) *(LAS float*)(L + L_G + l * 4) = gt;
        og = rbf(1.f - gt);
        WAIT_TICK(t_in, TH);
        if (RELQ) WAIT_TICK(t_w, 250);
        int spin = 0;
#pragma nounroll
        for (int hf = 0; hf < 2;) {
          v4u t[16];
#pragma unroll
          for (int i = 0; i < 16; ++i) {
            t[i] = __builtin_amdgcn_raw_buffer_load_b128(rws, WS_HP + kh * 32768 + hf * 16384 + l * 16, i * 1024, AUX_E);
            asm volatile("s_sleep %0" :: "n"(GH) : "memory");
          }
          int miss = -1;
#pragma unroll
          for (int j = 0; j < 8; ++j) {
            const int i0 = (j >> 1) * 4 + (j & 1);
            const unsigned bb = (j & 1) ? bb1 : bb0;
            miss = gbad(t[i0], tg) ? i0 : gbad(t[i0 + 2], tg) ? i0 + 2 : miss;
            const f32x2 z = gval(t[i0]) + gval(t[i0 + 2]);
            const float h0 = rbf(rbf(z.x) + bf_lo(bb)), h1 = rbf(rbf(z.y) + bf_hi(bb));
            const float r0 = h0 > 0.f ? h0 : 0.f, r1 = h1 > 0.f ? h1 : 0.f;
            *(LAS unsigned*)(L + L_H + (hf * 4 + (j >> 1)) * 1024 + (j & 1) * 256 + l * 4) = cvtpk(r0 * r0, r1 * r1);
          }
          if (__builtin_amdgcn_ballot_w64(miss >= 0) == 0 || spin >= SPIN_CAP) ++hf;
          else watch(rws, miss < 0 ? -1 : WS_HP + kh * 32768 + hf * 16384 + l * 16 + miss * 1024, tg, spin);
        }
        LDSWAIT();
        if (l == 0) LDS_SET(L_HRDY);
      }
      DRAIN();
      STAMP(1);
      PSTAMP(23);
      v4u xb[8];
      const LAS unsigned char* xp = L + (ffn2 ? L_H : L_X + kh * 512) + (l & 7) * 1024 + g * 16;
#pragma unroll
      for (int k = 0; k < 8; ++k) xb[k] = *(const LAS v4u*)(xp + k * 64);
      const int off = (isq ? WS_QP + (cb >> 3) * 65536 + (l & 7) * 8192 + kh * 4096 + (cb & 7) * 512 : WS_HP + (cb >> 2) * 32768 + (l & 7) * 4096 + kh * 2048 + (cb & 3) * 512)
                      + g * 32 + (l & 8) * 2;
#pragma nounroll
      for (int ct = 0; ct < 4; ++ct) {
        f32x4 d = {0.f, 0.f, 0.f, 0.f};
#pragma unroll
        for (int k = 0; k < 8; ++k) d = mfma32(tile_frag(L + L_WT, l, k, ct), xb[k], d);
        if (ffn2) *(LAS f32x4*)(L + L_FF + ct * 1024 + l * 16) = d * og;
        else __builtin_amdgcn_raw_buffer_store_b128(gran_d(d, l, tg), rws, off + ct * 128, 0, AUX_E);
      }
      LDSWAIT();
      PSTAMP(24);
    }
    STAMP(2);
    FLUSH();
    return;
  }
  if (w == 1) {
    const v4u xv = *(const GAS v4u*)(a.x + rb * DM + l * 8), gv = *(const GAS v4u*)(a.gw + l * 8);
    const v2u bb = *(const GAS v2u*)(a.b2 + rc * 16 + 4 * (l & 3));
    const u64 tag = read_tag(ws);
    const unsigned tg = (unsigned)tag, th = (unsigned)(tag >> 32);
    {
      const float gt = gate_of(xv, gv), og = rbf(1.f - gt);
      if (l < 4) *(LAS f32x4*)(L + L_FIN + l * 16) = f32x4{bf_lo(bb.x), bf_hi(bb.x), bf_lo(bb.y), bf_hi(bb.y)} * og;
      if (l == 0) *(LAS float*)(L + L_FIN + 64) = gt;
    }
    lds_wait<4>(L + L_ORG + 4);
    const unsigned org = *(volatile const LAS unsigned*)(L + L_ORG);
    if (l < 8) __builtin_amdgcn_raw_buffer_store_b128(v4u{org, tg, org, tg}, rws, WS_X + c * 128 + l * 16, 0, AUX_E);
    if (RELQ) { unsigned tq = __builtin_amdgcn_readfirstlane(org) + (TQ - T0) * 16; WAIT_NEXT(tq, 0); } else WAIT_TICK(t_in, TQ);
    v2u st;
#pragma nounroll
    for (int spin = 0; spin < SPIN_CAP;) {
      v4u t[8];
#pragma unroll
      for (int i = 0; i < 8; ++i) { t[i] = __builtin_amdgcn_raw_buffer_load_b128(rws, WS_QP + kvh * 65536 + b * 8192 + l * 16, i * 1024, AUX_E); asm volatile("s_sleep %0" :: "n"(GQ) : "memory"); }
      st = __builtin_amdgcn_raw_buffer_load_b64(rws, WS_X + ((l & 7) + 16 + ((l >> 3) & 3) * 64) * 128, 0, AUX_SC1);
      int miss = -1;
#pragma unroll
      for (int j = 0; j < 4; ++j) {
        miss = gbad(t[j], tg) ? j : gbad(t[4 + j], tg) ? 4 + j : miss;
        const f32x2 q = gval(t[j]) + gval(t[4 + j]);
        *(LAS unsigned*)(L + L_Q + j * 256 + l * 4) = cvtpk(q.x, q.y);
      }
      if (__builtin_amdgcn_ballot_w64(miss >= 0) == 0) break;
      watch(rws, miss < 0 ? -1 : WS_QP + kvh * 65536 + b * 8192 + l * 16 + miss * 1024, tg, spin);
    }
    {
      unsigned rel = (st.y == tg ? st.x : (now_ticks() << 4) - ((2 * WIN + 1) * DT + 2400)) - org;
      rel += dppu<0xB1>(rel); rel += dppu<0x4E>(rel); rel += dppu<0x141>(rel); rel += dppu<0x140>(rel);
      const int sum = __builtin_amdgcn_readlane((int)rel, 0) + __builtin_amdgcn_readlane((int)rel, 16);
      if (l == 0) *(LAS unsigned*)(L + L_TEND) = org + (sum >> 5) + 32 * (DT + DTV);
    }
    LDSWAIT();
    if (l == 0) LDS_SET(L_QRDY);
    STAMP(3);
    if (ffn2) lds_wait<8>(L + L_HRDY);
    DRAIN();
    if (l == 0) __builtin_amdgcn_raw_buffer_store_b64(v2u{tg, th}, rws, WS_F + c * 8, 0, AUX_SC1);
    STAMP(5);
    lds_wait<4>(L + L_ML + (l & 31) * 16 + 8);
    const v2u ml = *(volatile const LAS v2u*)(L + L_ML + (l & 31) * 16);
    const float mw = __uint_as_float(ml.x);
    float M = fmaxf(mw, dpp<0xB1>(mw)); M = fmaxf(M, dpp<0x4E>(M)); M = fmaxf(M, dpp<0x141>(M));
    float Ls = __builtin_amdgcn_exp2f(mw - M) * __uint_as_float(ml.y);
    Ls += dpp<0xB1>(Ls); Ls += dpp<0x4E>(Ls); Ls += dpp<0x141>(Ls);
    const float lse = M + __builtin_amdgcn_logf(Ls);
    const v4u lg = gran2(lse, dpp<0x128>(lse), tg);
    if (l < 32 && (l & 15) == 0) ST_SC1(lg, WS_LSE + ((b * 8 + kvh) * 4 + sp) * 128 + l);
    STAMP(6);
    v2u ls;
    asm volatile("s_sleep 16" ::: "memory");
#pragma nounroll
    for (int spin = 0; spin < SPIN_CAP; ++spin) {
      ls = __builtin_amdgcn_raw_buffer_load_b64(rws, WS_LSE + ((b * 8 + kvh) * 4 + (l & 3)) * 128 + ((l >> 3) & 3) * 8, 0, AUX_SC1);
      if (__builtin_amdgcn_ballot_w64(ls.y != tg) == 0) break;
      asm volatile("s_sleep 8" ::: "memory");
    }
    const float lf = __uint_as_float(ls.x);
    float M2 = fmaxf(lf, dpp<0xB1>(lf)); M2 = fmaxf(M2, dpp<0x4E>(M2));
    float E = __builtin_amdgcn_exp2f(lf - M2);
    E += dpp<0xB1>(E); E += dpp<0x4E>(E);
    if (l < 32) *(LAS float*)(L + L_CF + l * 4) = __builtin_amdgcn_exp2f(mw - M2) * __builtin_amdgcn_rcpf(E);
    WAIT_TICK(t_in, TF);
#pragma nounroll
    for (int spin = 0; spin < SPIN_CAP; ++spin) {
      const v4u f0 = LD_SC1(WS_F + l * 16), f1 = LD_SC1(WS_F + 1024 + l * 16);
      if (__builtin_amdgcn_ballot_w64(((f0.x ^ tg) | (f0.y ^ th) | (f0.z ^ tg) | (f0.w ^ th) | (f1.x ^ tg) | (f1.y ^ th) | (f1.z ^ tg) | (f1.w ^ th)) != 0) == 0) break;
      asm volatile("s_sleep 16" ::: "memory");
    }
    LDSWAIT();
    STAMP(7);
    lds_wait<1>(L + L_DONE + (l & 7) * 4);
    asm volatile("s_sleep %0" :: "n"(WOS) : "memory");
    DMA_LOOP("nt", make_srd(a.wo + oh * 128 * DM + on * 64, 127 * 1024 + 128), (unsigned)(l >> 3) * 1024u + (unsigned)(l & 7) * 16u, 8192, lbase + L_WO, 16);
    WGBAR();
    DRAIN();
    if (l == 0) LDS_SET(L_WRDY);
    LDSWAIT();
    STAMP(8);
    FLUSH();
    return;
  }

  const int sw = w - 2;
  const unsigned stag = sw * (DT / 8);
  unsigned tnext = ((t_in + T0) << 4) + stag, dt = DT;
#define PACE() do { asm volatile("s_waitcnt vmcnt(%0)" :: "n"(WIN - 1) : "memory"); WAIT_NEXT(tnext, dt); } while (0)
#define RESLIP() do { const unsigned t1_ = (now_ticks() - SLIP) << 4; if ((int)(t1_ - tnext) > 0) tnext = t1_ + (stag >> 1); } while (0)
#define REPLAN(done, nominal) do { RESLIP(); \
    const int ex_ = (int)(tnext + ((done) < 32 ? (32 - (done)) * DT + 32 * DTV : (64 - (done)) * DTV) - stag - tend); \
    dt = (nominal) - sclamp<-DN2, UP2>((ex_ * (65536 / (64 - (done)))) >> 16); } while (0)
  const size_t base = ((size_t)(b * HKV + kvh) * SK + sp * 1024 + sw * 128) * 256;
  const v4u rk = make_srd(a.kc + base, 32768), rv = make_srd(a.vc + base, 32768);
  const int ko = ((l >> 2) & 3) * 256 + g * 64 + (l & 3) * 16, vo = l * 16;
    const int rot = (16 * (sw & 1) + 8 * ((sw >> 1) & 1) + 4 * (sw >> 2) + 9 * (sp & 1) + 28 * (sp >> 1) + 30 * (b & 1)) & 31;
#define OFF(i) ((((i) + rot) & 31) * 1024)
#define LOAD_V(i) asm volatile("buffer_load_dwordx4 %0, %1, %2, %3 offen nt" : "+v"(kv[i]) : "v"(vo), "s"(rv), "s"(OFF(i)) : "memory")
  const u64 tag = read_tag(ws);
  const unsigned tg = (unsigned)tag;
  unsigned tend = 0;
  { const unsigned t1 = ((now_ticks() - SLIP0) << 4) + stag; if ((int)(t1 - tnext) > 0) tnext = t1; }
  v4u kv[32];
  __builtin_amdgcn_sched_barrier(0);
#pragma unroll
  for (int i = 0; i < 32; ++i) {
    PACE();
    asm volatile("buffer_load_dwordx4 %0, %1, %2, %3 offen nt" : "=v"(kv[i]) : "v"(ko), "s"(rk), "s"(OFF(i)) : "memory");
    PSTAMP(100 + i);
    if (i == WIN) RESLIP();
    if (i == 2 * WIN) {
      RESLIP();
      const unsigned org = tnext - stag - (2 * WIN + 1) * DT;
      if (sw == 3 && l == 0) { *(volatile LAS unsigned*)(L + L_ORG) = org; LDS_SET(L_ORG + 4); }
      dt = DT - sclamp<0, UP1>((int)(org - ((t_in + T0) << 4)) >> 6);
    }
    if (i == JA) {
      const unsigned f = __builtin_amdgcn_readfirstlane(*(volatile const LAS unsigned*)(L + L_QRDY));
      tend = __builtin_amdgcn_readfirstlane(*(volatile const LAS unsigned*)(L + L_TEND));
      if (f) REPLAN(JA + 1, DT);
    }
    __builtin_amdgcn_sched_barrier(0);
  }
  STAMP(1);
  lds_wait<2>(L + L_QRDY);
  STAMP(2);
  const v4u qf = *(const LAS v4u*)(L + L_Q + ((l >> 2) & 3) * 256 + g * 64 + (l & 3) * 16);
  tend = __builtin_amdgcn_readfirstlane(*(const LAS unsigned*)(L + L_TEND));
  REPLAN(32, DTV);
  __builtin_amdgcn_sched_barrier(0);
  constexpr float C2 = 0.088388347648318440f * 1.4426950408889634f;
  float sc[8];
  const int s = l & 3;
#pragma unroll
  for (int m = 0; m < 8; ++m) {
    asm volatile("s_waitcnt vmcnt(%4)" : "+v"(kv[4 * m]), "+v"(kv[4 * m + 1]), "+v"(kv[4 * m + 2]), "+v"(kv[4 * m + 3]) : "n"(28 - 4 * m + m) : "memory");
    f32x4 d[4];
#pragma unroll
    for (int r = 0; r < 4; ++r) d[r] = mfma32(kv[4 * m + r], qf, f32x4{0.f, 0.f, 0.f, 0.f});
#pragma unroll
    for (int r = 0; r < 4; ++r) {
      float t = dpp<0x00>(d[r][0]); t = dpp<0x55>(d[r][1]) + t; t = dpp<0xAA>(d[r][2]) + t; t = dpp<0xFF>(d[r][3]) + t;
      asm volatile("" : "+v"(t));
      sc[m] = (r == 0 || s == r) ? t : sc[m];
    }
    asm volatile("" : "+v"(sc[m]));
    __builtin_amdgcn_sched_barrier(0);
    PACE();
    LOAD_V(m);
    __builtin_amdgcn_sched_barrier(0);
  }
  STAMP(3);
  float mx = sc[0];
#pragma unroll
  for (int m = 1; m < 8; ++m) mx = fmaxf(mx, sc[m]);
  mx = fmaxf(mx, dpp<0xB1>(mx)); mx = fmaxf(mx, dpp<0x4E>(mx)); mx = xrow_max(mx);
  const float mneg = -mx * C2;
  float lsum = 0.f;
  unsigned pk[8];
#pragma unroll
  for (int m = 0; m < 8; ++m) { const float p = __builtin_amdgcn_exp2f(fmaf(sc[m], C2, mneg)); pk[m] = cvtpk(p, p); lsum += bf_lo(pk[m]); }
  lsum += dpp<0xB1>(lsum); lsum += dpp<0x4E>(lsum); lsum = xrow_sum(lsum);
  if (l < 16 && (l & 3) == 0) {
    *(volatile LAS v2u*)(L + L_ML + ((l >> 2) * 8 + sw) * 16) = v2u{__float_as_uint(mx * C2), __float_as_uint(lsum)};
    LDS_SET(L_ML + ((l >> 2) * 8 + sw) * 16 + 8);
  }
  const unsigned half = (l & 1) ? 0xffff0000u : 0x0000ffffu;
  const unsigned m0 = (l & 2) ? 0u : half, m1 = (l & 2) ? half : 0u;
  f32x4 o0 = {0.f, 0.f, 0.f, 0.f}, o1 = {0.f, 0.f, 0.f, 0.f};
#pragma unroll
  for (int j = 8; j < 40; ++j) {
    if (j < 32) { PACE(); LOAD_V(j); if (j == 15) REPLAN(48, DTV); __builtin_amdgcn_sched_barrier(0); }
    if (j == 31) { if (l == 0) LDS_SET(L_DONE + sw * 4); STAMP(11); }
    const int i = j - 8;
    const unsigned pb = (i & 3) == 0 ? dppu<0x00>(pk[i >> 2]) : (i & 3) == 1 ? dppu<0x55>(pk[i >> 2]) : (i & 3) == 2 ? dppu<0xAA>(pk[i >> 2]) : dppu<0xFF>(pk[i >> 2]);
    const v2u av = {pb & m0, pb & m1};
    asm volatile("s_waitcnt vmcnt(%1)" : "+v"(kv[i]) : "n"((j < 32 ? j : 31) - i) : "memory");
    o0 = mfma16(av, v2u{kv[i].x, kv[i].y}, o0);
    o1 = mfma16(av, v2u{kv[i].z, kv[i].w}, o1);
    __builtin_amdgcn_sched_barrier(0);
  }
  *(LAS f32x4*)(L + L_MRG + (sw * 4 + g) * 512 + (l & 15) * 32) = o0;
  *(LAS f32x4*)(L + L_MRG + (sw * 4 + g) * 512 + (l & 15) * 32 + 16) = o1;
  STAMP(4);
  LDSWAIT();
  WGBAR();
  if (sw < 4) {
    STAMP(5);
    const f32x4 c0 = *(const LAS f32x4*)(L + L_CF + sw * 32), c1 = *(const LAS f32x4*)(L + L_CF + sw * 32 + 16);
    f32x2 acc = {0.f, 0.f};
#pragma unroll
    for (int j = 0; j < 8; ++j) {
      const f32x2 ov = *(const LAS f32x2*)(L + L_MRG + (j * 4 + sw) * 512 + l * 8);
      const float e = j < 4 ? c0[j & 3] : c1[j & 3];
      acc.x = fmaf(e, ov.x, acc.x); acc.y = fmaf(e, ov.y, acc.y);
    }
    const unsigned pv = notag(cvtpk(acc.x, acc.y), tg);
    const v4u pq = {dppu<0x00>(pv), dppu<0x55>(pv), dppu<0xAA>(pv), dppu<0xFF>(pv)};
    if ((l & 3) == 0) ST_SC1(pq, WS_P + (((kvh * 4 + sw) * 8 + b) * 4 + sp) * 256 + l * 4);
    STAMP(6);
    f32x4 ff = {0.f, 0.f, 0.f, 0.f};
    float gm = 1.f;
    if (ffn2) { ff = *(const LAS f32x4*)(L + L_FF + sw * 1024 + l * 16); gm = *(const LAS float*)(L + L_G + (l & 7) * 4); }
    const int po = WS_P + (oh * 8 + 2 * sw) * 1024 + l * 16;
    asm volatile("s_sleep %0" :: "n"(PDELAY1) : "memory");
    v4u p0 = LD_SC1(po), p1 = LD_SC1(po + 1024);
    lds_wait<0>(L + L_WRDY);
    v4u wf[4];
#pragma unroll
    for (int k = 0; k < 4; ++k) wf[k] = tile_frag(L + L_WO, l, k, sw);
    __builtin_amdgcn_sched_barrier(0);
    if ((p0.y == tg) | (p0.w == tg) | (p1.y == tg) | (p1.w == tg)) poll2(rws, po, po + 1024, tg, p0, p1);
    *(LAS v4u*)(L + L_PG + (8 * sw + g) * 272 + (l & 15) * 16) = p0;
    *(LAS v4u*)(L + L_PG + (8 * sw + 4 + g) * 272 + (l & 15) * 16) = p1;
    STAMP(7);
    LDSWAIT();
    WGBAR();
    const int pb = l & 7, sl = (l >> 3) & 1;
    v4u pa[8];
#pragma unroll
    for (int k = 0; k < 8; ++k) pa[k] = *(const LAS v4u*)(L + L_PG + (pb * 4 + (k >> 2) * 2 + sl) * 272 + (k & 3) * 64 + g * 16);
    __builtin_amdgcn_sched_barrier(0);
    f32x4 da = {0.f, 0.f, 0.f, 0.f}, db = {0.f, 0.f, 0.f, 0.f};
#pragma unroll
    for (int k = 0; k < 4; ++k) { da = mfma32(wf[k], pa[k], da); db = mfma32(wf[k], pa[4 + k], db); }
    v4u r;
#pragma unroll
    for (int j = 0; j < 4; ++j) { const float t = da[j] + db[j]; r[j] = notag(__float_as_uint(fmaf(t + dpp<0x128>(t), gm, ff[j])), tg); }
    if (sl == 0) ST_SC1(r, WS_O + (((on * 4 + sw) * 4 + (pb >> 1)) * 32 + oh) * 128 + (pb & 1) * 64 + g * 16);
    STAMP(8);
    if (sw == 0) {
      const f32x4 fin = *(const LAS f32x4*)(L + L_FIN + (l & 3) * 16);
      const float gt = *(const LAS float*)(L + L_FIN + 64);
      const float g0 = (l & 12) == 12 ? 1.f : gt, g1 = l < 48 ? g0 : gt;
      const int oo = WS_O + (rr >> 1) * 4096 + (l >> 2) * 128 + (rr & 1) * 64 + (l & 3) * 16;
      v4u r0, r1;
      asm volatile("s_sleep %0" :: "n"(PDELAY2) : "memory");
      poll2(rws, oo, oo + 2048, tg, r0, r1);
      STAMP(9);
      f32x4 av = __builtin_bit_cast(f32x4, r0) * g0 + __builtin_bit_cast(f32x4, r1) * g1;
#pragma unroll
      for (int j = 0; j < 4; ++j) av[j] += dpp<0x124>(av[j]);
#pragma unroll
      for (int j = 0; j < 4; ++j) av[j] += dpp<0x128>(av[j]);
      {
        auto s0 = __builtin_amdgcn_permlane16_swap(__float_as_uint(av[0]), __float_as_uint(av[1]), false, false);
        auto s1 = __builtin_amdgcn_permlane16_swap(__float_as_uint(av[2]), __float_as_uint(av[3]), false, false);
        const float u0 = __uint_as_float(s0[0]) + __uint_as_float(s0[1]), u1 = __uint_as_float(s1[0]) + __uint_as_float(s1[1]);
        auto s2 = __builtin_amdgcn_permlane32_swap(__float_as_uint(u0), __float_as_uint(u1), false, false);
        const float v = __uint_as_float(s2[0]) + __uint_as_float(s2[1]);
        const float o = v + (g == 0 ? fin[0] : g == 1 ? fin[1] : g == 2 ? fin[2] : fin[3]);
        if ((l & 15) < 4) *(GAS unsigned short*)(a.out + rb * DM + rc * 16 + 4 * (l & 3) + g) = (unsigned short)cvtpk(o, o);
      }
      if (c == 0 && l == 0) *(GAS u64*)(ws + WS_E) = tag;
      STAMP(10);
    }
  }
  FLUSH();
}

extern "C" void kernel_launch(void* const* d_in, const int* in_sizes, int n_in, void* d_out, int out_size, void* d_ws, size_t ws_size,
                              hipStream_t stream) {
  Args a;
  a.x = (const unsigned short*)d_in[0];
  a.kc = (const unsigned char*)d_in[1];
  a.vc = (const unsigned char*)d_in[2];
  a.wq = (const unsigned short*)d_in[3];
  a.wo = (const unsigned short*)d_in[4];
  a.w1 = (const unsigned short*)d_in[6];
  a.b1 = (const unsigned short*)d_in[7];
  a.w2 = (const unsigned short*)d_in[8];
  a.b2 = (const unsigned short*)d_in[9];
  a.gw = (const unsigned short*)d_in[10];
  a.ws = (unsigned char*)d_ws;
  a.out = (unsigned short*)d_out;
  fused_decode<<<256, NWAVE * 64, 0, stream>>>(a);
}
```

```cpp
#include <hip/hip_runtime.h>
#include <hip/hip_bf16.h>
#include <stdint.h>

typedef unsigned v4u __attribute__((ext_vector_type(4)));
typedef unsigned v2u __attribute__((ext_vector_type(2)));
typedef float f32x4 __attribute__((ext_vector_type(4)));
typedef float f32x2 __attribute__((ext_vector_type(2)));
typedef __bf16 bf16x8 __attribute__((ext_vector_type(8)));
typedef __bf16 bf16x2 __attribute__((ext_vector_type(2)));
typedef short s16x4 __attribute__((ext_vector_type(4)));
typedef unsigned long long u64;
#define GAS __attribute__((address_space(1)))
#define LAS __attribute__((address_space(3)))

constexpr int DM = 512, HKV = 8, SK = 4096, FF = 1792;
constexpr int AUX_SC1 = 16;
#define DT 465
#define DTV 440
#define T0 130
#define WIN 5
#define SLIP 40
#define UP1 0
#define UP2 30
#define DN2 40
#define JA 24
#define TQ 560
#define TH 480
#define TF 1500
#define GQ 6
#define GH 3
#define WOS 6
#define PDELAY1 19
#define PDELAY2 18
#define CODEB 17760
#define AUXE 18
constexpr int AUX_E = AUXE;
#define EBAR 1
#define EBAR0 ""
#define SLIP0 30
#define RELQ 1
#define SQZ 0
#define DT2 DT
#define DT0 DT
#define J1 10
#define WSP 0
#define OAUX 0
#define LNT 0
#define LSL 16
#define SAMP ((l & 7) + 16 + ((l >> 3) & 3) * 64)
constexpr int NWAVE = 10;

constexpr int WS_E = 0;
constexpr int WS_F = 2048;
constexpr int WS_QP = 4096;
constexpr int WS_HP = WS_QP + 8 * 8 * 2 * 512 * 8;
constexpr int WS_X = WS_HP + 7 * 8 * 2 * 256 * 8;
constexpr int WS_LSE = WS_X + 256 * 128;
constexpr int WS_END = WS_LSE + 256 * 128;
constexpr int WS_O = WS_QP;
constexpr int WS_P = WS_HP;
static_assert(WS_LSE - WS_P == 32 * 8 * 4 * 256, "the partials fill exactly the lines of HP and X");
constexpr int WS_ST = 8 << 20;
constexpr int L_WO = 0, L_WT = 16384, L_MRG = 49152, L_Q = L_MRG + 16384, L_X = L_Q + 1024, L_H = L_X + 8192, L_NIL = L_H + 8192, L_FF = L_NIL + 1024,
              L_CF = L_FF + 4096, L_G = L_CF + 128, L_FIN = L_G + 32,
              L_SYNC = L_FIN + 96,
              L_ML = L_SYNC, L_QRDY = L_ML + 512, L_TEND = L_QRDY + 4, L_HRDY = L_QRDY + 8, L_WRDY = L_QRDY + 12, L_ORG = L_QRDY + 16, L_DONE = L_QRDY + 32,
              L_END = L_SYNC + 1024;
constexpr int L_PG = L_WT;

__device__ __forceinline__ f32x4 mfma32(v4u a, v4u b, f32x4 c) {
  return __builtin_amdgcn_mfma_f32_16x16x32_bf16(__builtin_bit_cast(bf16x8, a), __builtin_bit_cast(bf16x8, b), c, 0, 0, 0);
}
__device__ __forceinline__ f32x4 mfma16(v2u a, v2u b, f32x4 c) {
  return __builtin_amdgcn_mfma_f32_16x16x16bf16_1k(__builtin_bit_cast(s16x4, a), __builtin_bit_cast(s16x4, b), c, 0, 0, 0);
}
__device__ __forceinline__ unsigned cvtpk(float lo, float hi) { bf16x2 r = {(__bf16)lo, (__bf16)hi}; return __builtin_bit_cast(unsigned, r); }
__device__ __forceinline__ float bf_lo(unsigned u) { return __builtin_bit_cast(float, u << 16); }
__device__ __forceinline__ float bf_hi(unsigned u) { return __builtin_bit_cast(float, u & 0xffff0000u); }
__device__ __forceinline__ float rbf(float x) { return (float)(__bf16)x; }
template <int CTRL> __device__ __forceinline__ float dpp(float x) { return __builtin_bit_cast(float, __builtin_amdgcn_mov_dpp(__builtin_bit_cast(int, x), CTRL, 0xf, 0xf, true)); }
template <int CTRL> __device__ __forceinline__ unsigned dppu(unsigned x) { return (unsigned)__builtin_amdgcn_mov_dpp((int)x, CTRL, 0xf, 0xf, true); }
__device__ __forceinline__ float xrow_max(float x) {
  auto s = __builtin_amdgcn_permlane16_swap(__float_as_uint(x), __float_as_uint(x), false, false);
  x = fmaxf(__uint_as_float(s[0]), __uint_as_float(s[1]));
  auto t = __builtin_amdgcn_permlane32_swap(__float_as_uint(x), __float_as_uint(x), false, false);
  return fmaxf(__uint_as_float(t[0]), __uint_as_float(t[1]));
}
__device__ __forceinline__ float xrow_sum(float x) {
  auto s = __builtin_amdgcn_permlane16_swap(__float_as_uint(x), __float_as_uint(x), false, false);
  x = __uint_as_float(s[0]) + __uint_as_float(s[1]);
  auto t = __builtin_amdgcn_permlane32_swap(__float_as_uint(x), __float_as_uint(x), false, false);
  return __uint_as_float(t[0]) + __uint_as_float(t[1]);
}
__device__ __forceinline__ v2u tr_read(const LAS unsigned char* p) { return __builtin_bit_cast(v2u, __builtin_amdgcn_ds_read_tr16_b64_v4i16((LAS s16x4*)p)); }
__device__ __forceinline__ v4u make_srd(const void* p, unsigned bytes) {
  const u64 a = (u64)p;
  return v4u{(unsigned)a, (unsigned)(a >> 32) & 0xffffu, bytes, 0x00020000u};
}
#define DMA_LOOP(POL, srd, voff, step, lds_addr, n) do { unsigned keep_, so_ = 0, n_ = (n); \
  asm volatile("s_cmp_eq_u32 %2, 0\n\ts_cbranch_scc1 2f\n\ts_mov_b32 %0, m0\n\ts_mov_b32 m0, %3\n\ts_nop 0\n1:\n\tbuffer_load_dwordx4 %4, %5, %1 offen " POL " lds\n\t" \
               "s_add_u32 m0, m0, 0x400\n\ts_add_u32 %1, %1, %6\n\ts_sub_u32 %2, %2, 1\n\ts_cmp_lg_u32 %2, 0\n\ts_cbranch_scc1 1b\n\ts_mov_b32 m0, %0\n2:" \
               : "=&s"(keep_), "+&s"(so_), "+&s"(n_) : "s"(lds_addr), "v"(voff), "s"(srd), "s"(step) : "scc", "memory"); } while (0)
__device__ __forceinline__ v4u tile_frag(const LAS unsigned char* tile, int l, int k, int ct) {
  const LAS unsigned char* p = tile + (8 * (l >> 4) + ((l & 15) >> 2)) * 128 + (l & 3) * 8 + k * 4096 + ct * 32;
  const v2u t0 = tr_read(p), t1 = tr_read(p + 512);
  return v4u{t0.x, t0.y, t1.x, t1.y};
}
#define DRAIN() asm volatile("s_waitcnt vmcnt(0)" ::: "memory")
#define LDSWAIT() asm volatile("s_waitcnt lgkmcnt(0)" ::: "memory")
#define WGBAR() do { __builtin_amdgcn_sched_barrier(0); __builtin_amdgcn_s_barrier(); __builtin_amdgcn_sched_barrier(0); asm volatile("" ::: "memory"); } while (0)
__device__ __forceinline__ u64 read_tag(const unsigned char* ws) {
  u64 t;
  asm volatile("s_load_dwordx2 %0, %1, 0x0\n\ts_waitcnt lgkmcnt(0)" : "=s"(t) : "s"(ws) : "memory");
  const unsigned old = (unsigned)t;
  t += 0x9E3779B97F4A7C15ull;
  t = (t ^ (t >> 30)) * 0xBF58476D1CE4E5B9ull; t = (t ^ (t >> 27)) * 0x94D049BB133111EBull;
  t ^= t >> 31;
  t |= 1u;
  return (unsigned)t == old ? t ^ 2u : t;
}
__device__ __forceinline__ unsigned now_ticks() {
  u64 t;
  asm volatile("s_memrealtime %0\n\ts_waitcnt lgkmcnt(0)" : "=s"(t) :: "memory");
  return (unsigned)t;
}
#define WAIT_TICK(base, delta) \
  asm volatile("s_branch 2f\n1:\n\ts_sleep 1\n2:\n\ts_memrealtime vcc\n\ts_waitcnt lgkmcnt(0)\n\ts_sub_u32 vcc_lo, vcc_lo, %0\n\ts_cmpk_lt_i32 vcc_lo, %1\n\ts_cbranch_scc1 1b" \
               :: "s"(base), "n"(delta) : "scc", "vcc", "memory")
#define WAIT_NEXT(acc, step) \
  asm volatile("s_branch 2f\n1:\n\ts_sleep 1\n2:\n\ts_memrealtime vcc\n\ts_waitcnt lgkmcnt(0)\n\ts_lshl_b32 vcc_lo, vcc_lo, 4\n\ts_sub_u32 vcc_lo, vcc_lo, %0\n\ts_cmp_lt_i32 vcc_lo, 0\n\ts_cbranch_scc1 1b\n\ts_add_u32 %0, %0, %1" \
               : "+s"(acc) : "s"(step) : "scc", "vcc", "memory")
template <int LO, int HI> __device__ __forceinline__ int sclamp(int x) {
  asm("s_max_i32 %0, %0, %1\n\ts_min_i32 %0, %0, %2" : "+s"(x) : "n"(LO), "n"(HI) : "scc");
  return x;
}
constexpr int SPIN_CAP = 1 << 18;
#define LD_SC1(off) __builtin_amdgcn_raw_buffer_load_b128(rws, (off), 0, AUX_SC1)
#define ST_SC1(v, off) __builtin_amdgcn_raw_buffer_store_b128((v), rws, (off), 0, AUX_SC1)
__device__ __forceinline__ v4u gran2(float a, float b, unsigned tg) { return v4u{__float_as_uint(a), tg, __float_as_uint(b), tg}; }
__device__ __forceinline__ unsigned gbad(v4u g, unsigned tg) { return (g.y ^ tg) | (g.w ^ tg); }
__device__ __forceinline__ f32x2 gval(v4u g) { return f32x2{__uint_as_float(g.x), __uint_as_float(g.z)}; }
__device__ __forceinline__ v4u gran_d(f32x4 d, int l, unsigned tg) { return gran2((l & 8) ? d[2] : d[0], (l & 8) ? d[3] : d[1], tg); }

#define STAMP(k) do { } while (0)
#define FLUSH() do { } while (0)

#define PSTAMP(k) do { } while (0)
__device__ __forceinline__ void watch(__amdgpu_buffer_rsrc_t rws, int miss, unsigned tg, int& spin) {
#pragma nounroll
  while (miss >= 0 && spin < SPIN_CAP) {
    asm volatile("s_sleep 24" ::: "memory");
    if (gbad(__builtin_amdgcn_raw_buffer_load_b128(rws, miss, 0, AUX_E), tg) == 0) miss = -1;
    ++spin;
  }
  spin = __builtin_amdgcn_ballot_w64(spin >= SPIN_CAP) != 0 ? SPIN_CAP : __builtin_amdgcn_readfirstlane(spin) + 1;
}
template <int SLEEP> __device__ __forceinline__ void lds_wait(const LAS unsigned char* p) {
#pragma nounroll
  for (int spin = 0; spin < SPIN_CAP * 16; ++spin) {
    if (__builtin_amdgcn_ballot_w64(*(volatile const LAS unsigned*)p == 0u) == 0) break;
    if (SLEEP) asm volatile("s_sleep %0" :: "n"(SLEEP) : "memory");
  }
  asm volatile("" ::: "memory");
}
#define LDS_SET(off) (*(volatile LAS unsigned*)(L + (off)) = 1u)
__device__ __forceinline__ void poll2(__amdgpu_buffer_rsrc_t rws, int o0, int o1, unsigned tg, v4u& r0, v4u& r1) {
#pragma nounroll
  for (int spin = 0; spin < SPIN_CAP; ++spin) {
    r0 = LD_SC1(o0); r1 = LD_SC1(o1);
    if (__builtin_amdgcn_ballot_w64(min(min(r0.y ^ tg, r0.w ^ tg), min(r1.y ^ tg, r1.w ^ tg)) == 0u) == 0) break;
    asm volatile("" ::: "memory");
  }
}
__device__ __forceinline__ unsigned notag(unsigned x, unsigned tg) { return x == tg ? x ^ 1u : x; }
__device__ __forceinline__ float gate_of(v4u xv, v4u gv) {
  float gs = 0.f;
#pragma unroll
  for (int j = 0; j < 4; ++j) { gs += rbf(bf_lo(xv[j]) * bf_lo(gv[j])); gs += rbf(bf_hi(xv[j]) * bf_hi(gv[j])); }
  gs += dpp<0xB1>(gs); gs += dpp<0x4E>(gs); gs += dpp<0x124>(gs); gs += dpp<0x128>(gs); gs = xrow_sum(gs);
  return rbf(1.f / (1.f + __expf(-rbf(gs))));
}

struct Args {
  const unsigned short *x, *wq, *wo, *w1, *b1, *w2, *b2, *gw;
  const unsigned char *kc, *vc;
  unsigned char* ws;
  unsigned short* out;
};

typedef unsigned v16u __attribute__((ext_vector_type(16)));
typedef unsigned v8u __attribute__((ext_vector_type(8)));

__global__ void __launch_bounds__(NWAVE * 64) fused_decode(Args a_) {
  v16u ka0; v8u ka1; u64 t_in64;
  asm volatile("s_load_dwordx16 %0, %3, 0x0\n\ts_load_dwordx8 %1, %3, 0x40\n\ts_memrealtime %2" : "=&s"(ka0), "=&s"(ka1), "=&s"(t_in64) : "s"(__builtin_amdgcn_kernarg_segment_ptr()));
  __shared__ __attribute__((aligned(16))) unsigned char smem[L_END + 64];
  LAS unsigned char* L = (LAS unsigned char*)smem;
  const unsigned lbase = (unsigned)(uintptr_t)L;
  const int tid = threadIdx.x, l = tid & 63, w = __builtin_amdgcn_readfirstlane(tid >> 6);
  const int c = blockIdx.x, xr = c >> 3, sp = xr & 3, kvh = xr >> 2, b = (c - kvh) & 7, i32 = b * 4 + sp;
  {
    u64 pc;
    asm volatile("s_getpc_b64 %0" : "=s"(pc));
    const v4u rc = make_srd((const void*)(pc & ~255ull), CODEB);
    DMA_LOOP("", rc, ((xr * 2 + w) & 31) * 1024 + l * 16, 0, lbase + L_NIL, (12u >> w) & 1u);
  }
  const int g = l >> 4;
  const int oh = (c & 7) * 4 + (xr >> 3), on = xr & 7;
  const bool ffn2 = xr >= 24 && (c & 7) < 7;
  const bool isq = xr < 16, side = xr < 23 || ffn2;
  const int tl = isq ? ((xr >> 1) * 8 + ((c + xr) & 7)) * 2 + (xr & 1) : (c & 7) * 7 + xr - 16;
  const int cb = ffn2 ? on : tl >> 1, kh = ffn2 ? c & 7 : tl & 1;
#define KP(v, i) ((u64)v[i] | ((u64)v[(i) + 1] << 32))
  if (w == 1) *(LAS v4u*)(L + L_SYNC + l * 16) = v4u{0u, 0u, 0u, 0u};
  if (__builtin_expect(w == 0, 1)) {
    const unsigned ldb = isq ? 8192u : ffn2 ? 1024u : (unsigned)FF * 2u;
    unsigned woff = (unsigned)kh * 256u * ldb + cb * 128, wlen = 255u * ldb + 128u, wstep = 8u * ldb, wvo = (unsigned)(l >> 3) * ldb + (unsigned)(l & 7) * 16u;
    unsigned nw = side ? 32 : 0;
    asm volatile("s_waitcnt lgkmcnt(0)" EBAR0 : "+s"(ka0), "+s"(woff), "+s"(wlen), "+s"(wstep), "+s"(nw), "+v"(wvo) :: "memory");
    u64 pq = KP(ka0, 2), p1 = KP(ka0, 6), p2 = KP(ka0, 10);
    asm volatile("" : "+s"(pq), "+s"(p1), "+s"(p2));
    DMA_LOOP("nt", make_srd((const unsigned char*)(isq ? pq : ffn2 ? p2 : p1) + woff, wlen), wvo, wstep, lbase + L_WT, nw);
    DMA_LOOP("", make_srd((const void*)KP(ka0, 0), 8192), l * 16, 1024, lbase + L_X, nw >> 2);
  }
  asm volatile("s_waitcnt lgkmcnt(0)" : "+s"(t_in64), "+s"(ka0), "+s"(ka1) :: "memory");
  if (EBAR || w != 0) WGBAR();
  const unsigned t_in = (unsigned)t_in64;
  Args a;
  a.x = (const unsigned short*)KP(ka0, 0); a.wq = (const unsigned short*)KP(ka0, 2); a.wo = (const unsigned short*)KP(ka0, 4); a.w1 = (const unsigned short*)KP(ka0, 6);
  a.b1 = (const unsigned short*)KP(ka0, 8); a.w2 = (const unsigned short*)KP(ka0, 10); a.b2 = (const unsigned short*)KP(ka0, 12); a.gw = (const unsigned short*)KP(ka0, 14);
  a.kc = (const unsigned char*)KP(ka1, 0); a.vc = (const unsigned char*)KP(ka1, 2); a.ws = (unsigned char*)KP(ka1, 4); a.out = (unsigned short*)KP(ka1, 6);
  unsigned char* ws = a.ws;
  PSTAMP(21);
  const __amdgpu_buffer_rsrc_t rws = __builtin_amdgcn_make_buffer_rsrc((void*)ws, 0, WS_END, 0x00020000);
  const int rr = c, rb = rr & 7, rc = rr >> 3;

  if (w == 0) {
    if (side) {
      const unsigned tg = (unsigned)read_tag(ws);
      float og = 0.f;
      if (ffn2) {
        const unsigned bb0 = *(const GAS unsigned*)(a.b1 + kh * 256 + l * 2), bb1 = *(const GAS unsigned*)(a.b1 + kh * 256 + 128 + l * 2);
        const v4u gv = *(const GAS v4u*)(a.gw + l * 8);
        DRAIN();
        const unsigned t_w = now_ticks();
        float gt = 0.f;
#pragma nounroll
        for (int j = 0; j < 8; ++j) { const float t = gate_of(*(const LAS v4u*)(L + L_X + j * 1024 + l * 16), gv); gt = (l & 7) == j ? t : gt; }
        if (l < 8) *(LAS float*)(L + L_G + l * 4) = gt;
        og = rbf(1.f - gt);
        WAIT_TICK(t_in, TH);
        if (RELQ) WAIT_TICK(t_w, 250);
        int spin = 0;
#pragma nounroll
        for (int hf = 0; hf < 2;) {
          v4u t[16];
#pragma unroll
          for (int i = 0; i < 16; ++i) {
            t[i] = __builtin_amdgcn_raw_buffer_load_b128(rws, WS_HP + kh * 32768 + hf * 16384 + l * 16, i * 1024, AUX_E);
            asm volatile("s_sleep %0" :: "n"(GH) : "memory");
          }
          int miss = -1;
#pragma unroll
          for (int j = 0; j < 8; ++j) {
            const int i0 = (j >> 1) * 4 + (j & 1);
            const unsigned bb = (j & 1) ? bb1 : bb0;
            miss = gbad(t[i0], tg) ? i0 : gbad(t[i0 + 2], tg) ? i0 + 2 : miss;
            const f32x2 z = gval(t[i0]) + gval(t[i0 + 2]);
            const float h0 = rbf(rbf(z.x) + bf_lo(bb)), h1 = rbf(rbf(z.y) + bf_hi(bb));
            const float r0 = h0 > 0.f ? h0 : 0.f, r1 = h1 > 0.f ? h1 : 0.f;
            *(LAS unsigned*)(L + L_H + (hf * 4 + (j >> 1)) * 1024 + (j & 1) * 256 + l * 4) = cvtpk(r0 * r0, r1 * r1);
          }
          if (__builtin_amdgcn_ballot_w64(miss >= 0) == 0 || spin >= SPIN_CAP) ++hf;
          else watch(rws, miss < 0 ? -1 : WS_HP + kh * 32768 + hf * 16384 + l * 16 + miss * 1024, tg, spin);
        }
        LDSWAIT();
        if (l == 0) LDS_SET(L_HRDY);
      }
      DRAIN();
      STAMP(1);
      PSTAMP(23);
      v4u xb[8];
      const LAS unsigned char* xp = L + (ffn2 ? L_H : L_X + kh * 512) + (l & 7) * 1024 + g * 16;
#pragma unroll
      for (int k = 0; k < 8; ++k) xb[k] = *(const LAS v4u*)(xp + k * 64);
      const int off = (isq ? WS_QP + (cb >> 3) * 65536 + (l & 7) * 8192 + kh * 4096 + (cb & 7) * 512 : WS_HP + (cb >> 2) * 32768 + (l & 7) * 4096 + kh * 2048 + (cb & 3) * 512)
                      + g * 32 + (l & 8) * 2;
#pragma nounroll
      for (int ct = 0; ct < 4; ++ct) {
        f32x4 d = {0.f, 0.f, 0.f, 0.f};
#pragma unroll
        for (int k = 0; k < 8; ++k) d = mfma32(tile_frag(L + L_WT, l, k, ct), xb[k], d);
        if (ffn2) *(LAS f32x4*)(L + L_FF + ct * 1024 + l * 16) = d * og;
        else { __builtin_amdgcn_raw_buffer_store_b128(gran_d(d, l, tg), rws, off + ct * 128, 0, AUX_E); if (WSP) asm volatile("s_sleep %0" :: "n"(WSP) : "memory"); }
      }
      LDSWAIT();
      PSTAMP(24);
    }
    STAMP(2);
    FLUSH();
    return;
  }
  if (w == 1) {
    const v4u xv = *(const GAS v4u*)(a.x + rb * DM + l * 8), gv = *(const GAS v4u*)(a.gw + l * 8);
    const v2u bb = *(const GAS v2u*)(a.b2 + rc * 16 + 4 * (l & 3));
    const u64 tag = read_tag(ws);
    const unsigned tg = (unsigned)tag, th = (unsigned)(tag >> 32);
    {
      const float gt = gate_of(xv, gv), og = rbf(1.f - gt);
      if (l < 4) *(LAS f32x4*)(L + L_FIN + l * 16) = f32x4{bf_lo(bb.x), bf_hi(bb.x), bf_lo(bb.y), bf_hi(bb.y)} * og;
      if (l == 0) *(LAS float*)(L + L_FIN + 64) = gt;
    }
    lds_wait<4>(L + L_ORG + 4);
    const unsigned org = *(volatile const LAS unsigned*)(L + L_ORG);
    if (l < 8) __builtin_amdgcn_raw_buffer_store_b128(v4u{org, tg, org, tg}, rws, WS_X + c * 128 + l * 16, 0, AUX_E);
    if (RELQ) { unsigned tq = __builtin_amdgcn_readfirstlane(org) + (TQ - T0) * 16; WAIT_NEXT(tq, 0); } else WAIT_TICK(t_in, TQ);
    v2u st;
#pragma nounroll
    for (int spin = 0; spin < SPIN_CAP;) {
      v4u t[8];
#pragma unroll
      for (int i = 0; i < 8; ++i) { t[i] = __builtin_amdgcn_raw_buffer_load_b128(rws, WS_QP + kvh * 65536 + b * 8192 + l * 16, i * 1024, AUX_E); asm volatile("s_sleep %0" :: "n"(GQ) : "memory"); }
      st = __builtin_amdgcn_raw_buffer_load_b64(rws, WS_X + (SAMP) * 128, 0, AUX_SC1);
      int miss = -1;
#pragma unroll
      for (int j = 0; j < 4; ++j) {
        miss = gbad(t[j], tg) ? j : gbad(t[4 + j], tg) ? 4 + j : miss;
        const f32x2 q = gval(t[j]) + gval(t[4 + j]);
        *(LAS unsigned*)(L + L_Q + j * 256 + l * 4) = cvtpk(q.x, q.y);
      }
      if (__builtin_amdgcn_ballot_w64(miss >= 0) == 0) break;
      watch(rws, miss < 0 ? -1 : WS_QP + kvh * 65536 + b * 8192 + l * 16 + miss * 1024, tg, spin);
    }
    {
      unsigned rel = (st.y == tg ? st.x : (now_ticks() << 4) - ((2 * WIN + 1) * DT0 + 2400)) - org;
      rel += dppu<0xB1>(rel); rel += dppu<0x4E>(rel); rel += dppu<0x141>(rel); rel += dppu<0x140>(rel);
      const int sum = __builtin_amdgcn_readlane((int)rel, 0) + __builtin_amdgcn_readlane((int)rel, 16);
      if (l == 0) *(LAS unsigned*)(L + L_TEND) = org + (sum >> 5) + (J1 + 1) * DT0 + (JA - J1) * DT + (31 - JA) * DT2 + 32 * DTV;
    }
    LDSWAIT();
    if (l == 0) LDS_SET(L_QRDY);
    STAMP(3);
    if (ffn2) lds_wait<8>(L + L_HRDY);
    DRAIN();
    if (l == 0) __builtin_amdgcn_raw_buffer_store_b64(v2u{tg, th}, rws, WS_F + c * 8, 0, AUX_SC1);
    STAMP(5);
    lds_wait<4>(L + L_ML + (l & 31) * 16 + 8);
    const v2u ml = *(volatile const LAS v2u*)(L + L_ML + (l & 31) * 16);
    const float mw = __uint_as_float(ml.x);
    float M = fmaxf(mw, dpp<0xB1>(mw)); M = fmaxf(M, dpp<0x4E>(M)); M = fmaxf(M, dpp<0x141>(M));
    float Ls = __builtin_amdgcn_exp2f(mw - M) * __uint_as_float(ml.y);
    Ls += dpp<0xB1>(Ls); Ls += dpp<0x4E>(Ls); Ls += dpp<0x141>(Ls);
    const float lse = M + __builtin_amdgcn_logf(Ls);
    const v4u lg = gran2(lse, dpp<0x128>(lse), tg);
    if (l < 32 && (l & 15) == 0) ST_SC1(lg, WS_LSE + ((b * 8 + kvh) * 4 + sp) * 128 + l);
    STAMP(6);
    v2u ls;
    asm volatile("s_sleep %0" :: "n"(LSL) : "memory");
#pragma nounroll
    for (int spin = 0; spin < SPIN_CAP; ++spin) {
      ls = __builtin_amdgcn_raw_buffer_load_b64(rws, WS_LSE + ((b * 8 + kvh) * 4 + (l & 3)) * 128 + ((l >> 3) & 3) * 8, 0, LNT ? AUX_E : AUX_SC1);
      if (__builtin_amdgcn_ballot_w64(ls.y != tg) == 0) break;
      asm volatile("s_sleep 8" ::: "memory");
    }
    const float lf = __uint_as_float(ls.x);
    float M2 = fmaxf(lf, dpp<0xB1>(lf)); M2 = fmaxf(M2, dpp<0x4E>(M2));
    float E = __builtin_amdgcn_exp2f(lf - M2);
    E += dpp<0xB1>(E); E += dpp<0x4E>(E);
    if (l < 32) *(LAS float*)(L + L_CF + l * 4) = __builtin_amdgcn_exp2f(mw - M2) * __builtin_amdgcn_rcpf(E);
    WAIT_TICK(t_in, TF);
#pragma nounroll
    for (int spin = 0; spin < SPIN_CAP; ++spin) {
      const v4u f0 = LD_SC1(WS_F + l * 16), f1 = LD_SC1(WS_F + 1024 + l * 16);
      if (__builtin_amdgcn_ballot_w64(((f0.x ^ tg) | (f0.y ^ th) | (f0.z ^ tg) | (f0.w ^ th) | (f1.x ^ tg) | (f1.y ^ th) | (f1.z ^ tg) | (f1.w ^ th)) != 0) == 0) break;
      asm volatile("s_sleep 16" ::: "memory");
    }
    LDSWAIT();
    STAMP(7);
    lds_wait<1>(L + L_DONE + (l & 7) * 4);
    asm volatile("s_sleep %0" :: "n"(WOS) : "memory");
    DMA_LOOP("nt", make_srd(a.wo + oh * 128 * DM + on * 64, 127 * 1024 + 128), (unsigned)(l >> 3) * 1024u + (unsigned)(l & 7) * 16u, 8192, lbase + L_WO, 16);
    WGBAR();
    DRAIN();
    if (l == 0) LDS_SET(L_WRDY);
    LDSWAIT();
    STAMP(8);
    FLUSH();
    return;
  }

  const int sw = w - 2;
  const unsigned stag = sw * (DT / 8);
  unsigned tnext = ((t_in + T0) << 4) + stag, dt = DT0;
#define PACE() do { asm volatile("s_waitcnt vmcnt(%0)" :: "n"(WIN - 1) : "memory"); WAIT_NEXT(tnext, dt); } while (0)
#define RESLIP() do { const unsigned t1_ = (now_ticks() - SLIP) << 4; if ((int)(t1_ - tnext) > 0) tnext = t1_ + (stag >> 1); } while (0)
#define REPLAN(done, nominal) do { RESLIP(); \
    const int ex_ = (int)(tnext + ((done) < 32 ? (32 - (done)) * DT2 + 32 * DTV : (64 - (done)) * DTV) - ((done) == 48 ? (stag >> SQZ) : stag) - tend); \
    dt = (nominal) - sclamp<-DN2, UP2>((ex_ * (65536 / (64 - (done)))) >> 16); } while (0)
  const size_t base = ((size_t)(b * HKV + kvh) * SK + sp * 1024 + sw * 128) * 256;
  const v4u rk = make_srd(a.kc + base, 32768), rv = make_srd(a.vc + base, 32768);
  const int ko = ((l >> 2) & 3) * 256 + g * 64 + (l & 3) * 16, vo = l * 16;
    const int rot = (16 * (sw & 1) + 8 * ((sw >> 1) & 1) + 4 * (sw >> 2) + 9 * (sp & 1) + 28 * (sp >> 1) + 30 * (b & 1)) & 31;
#define OFF(i) ((((i) + rot) & 31) * 1024)
#define LOAD_V(i) asm volatile("buffer_load_dwordx4 %0, %1, %2, %3 offen nt" : "+v"(kv[i]) : "v"(vo), "s"(rv), "s"(OFF(i)) : "memory")
  const u64 tag = read_tag(ws);
  const unsigned tg = (unsigned)tag;
  unsigned tend = 0;
  { const unsigned t1 = ((now_ticks() - SLIP0) << 4) + stag; if ((int)(t1 - tnext) > 0) tnext = t1; }
  v4u kv[32];
  __builtin_amdgcn_sched_barrier(0);
#pragma unroll
  for (int i = 0; i < 32; ++i) {
    PACE();
    asm volatile("buffer_load_dwordx4 %0, %1, %2, %3 offen nt" : "=v"(kv[i]) : "v"(ko), "s"(rk), "s"(OFF(i)) : "memory");
    PSTAMP(100 + i);
    if (i == WIN) RESLIP();
    if (i == 2 * WIN) {
      RESLIP();
      const unsigned org = tnext - stag - (2 * WIN + 1) * DT0;
      if (sw == 3 && l == 0) { *(volatile LAS unsigned*)(L + L_ORG) = org; LDS_SET(L_ORG + 4); }
    }
    if (i == J1) dt = DT;
    if (i == JA) {
      const unsigned f = __builtin_amdgcn_readfirstlane(*(volatile const LAS unsigned*)(L + L_QRDY));
      tend = __builtin_amdgcn_readfirstlane(*(volatile const LAS unsigned*)(L + L_TEND));
      if (f) REPLAN(JA + 1, DT2);
    }
    __builtin_amdgcn_sched_barrier(0);
  }
  STAMP(1);
  lds_wait<2>(L + L_QRDY);
  STAMP(2);
  const v4u qf = *(const LAS v4u*)(L + L_Q + ((l >> 2) & 3) * 256 + g * 64 + (l & 3) * 16);
  tend = __builtin_amdgcn_readfirstlane(*(const LAS unsigned*)(L + L_TEND));
  REPLAN(32, DTV);
  __builtin_amdgcn_sched_barrier(0);
  constexpr float C2 = 0.088388347648318440f * 1.4426950408889634f;
  float sc[8];
  const int s = l & 3;
#pragma unroll
  for (int m = 0; m < 8; ++m) {
    asm volatile("s_waitcnt vmcnt(%4)" : "+v"(kv[4 * m]), "+v"(kv[4 * m + 1]), "+v"(kv[4 * m + 2]), "+v"(kv[4 * m + 3]) : "n"(28 - 4 * m + m) : "memory");
    f32x4 d[4];
#pragma unroll
    for (int r = 0; r < 4; ++r) d[r] = mfma32(kv[4 * m + r], qf, f32x4{0.f, 0.f, 0.f, 0.f});
#pragma unroll
    for (int r = 0; r < 4; ++r) {
      float t = dpp<0x00>(d[r][0]); t = dpp<0x55>(d[r][1]) + t; t = dpp<0xAA>(d[r][2]) + t; t = dpp<0xFF>(d[r][3]) + t;
      asm volatile("" : "+v"(t));
      sc[m] = (r == 0 || s == r) ? t : sc[m];
    }
    asm volatile("" : "+v"(sc[m]));
    __builtin_amdgcn_sched_barrier(0);
    PACE();
    LOAD_V(m);
    __builtin_amdgcn_sched_barrier(0);
  }
  STAMP(3);
  float mx = sc[0];
#pragma unroll
  for (int m = 1; m < 8; ++m) mx = fmaxf(mx, sc[m]);
  mx = fmaxf(mx, dpp<0xB1>(mx)); mx = fmaxf(mx, dpp<0x4E>(mx)); mx = xrow_max(mx);
  const float mneg = -mx * C2;
  float lsum = 0.f;
  unsigned pk[8];
#pragma unroll
  for (int m = 0; m < 8; ++m) { const float p = __builtin_amdgcn_exp2f(fmaf(sc[m], C2, mneg)); pk[m] = cvtpk(p, p); lsum += bf_lo(pk[m]); }
  lsum += dpp<0xB1>(lsum); lsum += dpp<0x4E>(lsum); lsum = xrow_sum(lsum);
  if (l < 16 && (l & 3) == 0) {
    *(volatile LAS v2u*)(L + L_ML + ((l >> 2) * 8 + sw) * 16) = v2u{__float_as_uint(mx * C2), __float_as_uint(lsum)};
    LDS_SET(L_ML + ((l >> 2) * 8 + sw) * 16 + 8);
  }
  const unsigned half = (l & 1) ? 0xffff0000u : 0x0000ffffu;
  const unsigned m0 = (l & 2) ? 0u : half, m1 = (l & 2) ? half : 0u;
  f32x4 o0 = {0.f, 0.f, 0.f, 0.f}, o1 = {0.f, 0.f, 0.f, 0.f};
  const int s4 = sw & 3, pb8 = l & 7;
  unsigned a_mw, a_mr, a_g0, a_l0, a_l1, a_g1;
#pragma unroll
  for (int j = 8; j < 40; ++j) {
    if (j < 32) { PACE(); LOAD_V(j); if (j == 15) REPLAN(48, DTV); __builtin_amdgcn_sched_barrier(0); }
    if (j == 31) { if (l == 0) LDS_SET(L_DONE + sw * 4); STAMP(11); }
    if (j == 33) {
      a_mw = L_MRG + (sw * 4 + g) * 512 + (l & 15) * 32;
      a_mr = L_MRG + s4 * 512 + l * 8;
      if (sw < 4) {
        a_g0 = (l & 3) == 0 ? WS_P + (((kvh * 4 + s4) * 8 + b) * 4 + sp) * 256 + l * 4 : WS_END;
        a_l0 = L_WO + (8 * g + ((l & 15) >> 2)) * 128 + (l & 3) * 8 + s4 * 32;
        a_l1 = L_PG + (pb8 * 4 + ((l >> 3) & 1)) * 272 + g * 16;
        a_g1 = (l & 8) == 0 ? WS_O + (((on * 4 + s4) * 4 + (pb8 >> 1)) * 32 + oh) * 128 + (pb8 & 1) * 64 + g * 16 : WS_END;
      } else {
        a_g0 = WS_P + (oh * 8 + 2 * s4) * 1024 + l * 16;
        a_l0 = L_PG + (8 * s4 + g) * 272 + (l & 15) * 16;
        a_l1 = (l & 15) < 4 ? (rb * DM + rc * 16 + 4 * (l & 3) + g) * 2 : 8192;
        a_g1 = WS_O + (rr >> 1) * 4096 + (l >> 2) * 128 + (rr & 1) * 64 + (l & 3) * 16;
      }
      asm volatile("" : "+v"(a_mw), "+v"(a_mr), "+v"(a_g0), "+v"(a_l0), "+v"(a_l1), "+v"(a_g1));
      __builtin_amdgcn_sched_barrier(0);
    }
    const int i = j - 8;
    const unsigned pb = (i & 3) == 0 ? dppu<0x00>(pk[i >> 2]) : (i & 3) == 1 ? dppu<0x55>(pk[i >> 2]) : (i & 3) == 2 ? dppu<0xAA>(pk[i >> 2]) : dppu<0xFF>(pk[i >> 2]);
    const v2u av = {pb & m0, pb & m1};
    asm volatile("s_waitcnt vmcnt(%1)" : "+v"(kv[i]) : "n"((j < 32 ? j : 31) - i) : "memory");
    o0 = mfma16(av, v2u{kv[i].x, kv[i].y}, o0);
    o1 = mfma16(av, v2u{kv[i].z, kv[i].w}, o1);
    __builtin_amdgcn_sched_barrier(0);
  }
  *(LAS f32x4*)(L + a_mw) = o0;
  *(LAS f32x4*)(L + a_mw + 16) = o1;
  STAMP(4);
  if (sw < 4) {
    asm volatile("s_waitcnt lgkmcnt(0)\n\ts_barrier ; merge" ::: "memory");
    STAMP(5);
    const f32x4 c0 = *(const LAS f32x4*)(L + L_CF + sw * 32), c1 = *(const LAS f32x4*)(L + L_CF + sw * 32 + 16);
    f32x2 ac0 = {0.f, 0.f}, ac1 = {0.f, 0.f};
#pragma unroll
    for (int j = 0; j < 4; ++j) {
      ac0 += *(const LAS f32x2*)(L + a_mr + j * 2048) * c0[j];
      ac1 += *(const LAS f32x2*)(L + a_mr + (4 + j) * 2048) * c1[j];
    }
    ac0 += ac1;
    const unsigned pv = notag(cvtpk(ac0.x, ac0.y), tg);
    ST_SC1((v4u{pv, dppu<0x55>(pv), dppu<0xAA>(pv), dppu<0xFF>(pv)}), a_g0);
    STAMP(6);
    f32x4 ff = {0.f, 0.f, 0.f, 0.f};
    float gm = 1.f;
    if (ffn2) { ff = *(const LAS f32x4*)(L + L_FF + sw * 1024 + l * 16); gm = *(const LAS float*)(L + L_G + pb8 * 4); }
    lds_wait<0>(L + L_WRDY);
    v4u wf[4];
#pragma unroll
    for (int k = 0; k < 4; ++k) { const v2u t0 = tr_read(L + a_l0 + k * 4096), t1 = tr_read(L + a_l0 + k * 4096 + 512); wf[k] = v4u{t0.x, t0.y, t1.x, t1.y}; }
    asm volatile("" : "+v"(wf[0]), "+v"(wf[1]), "+v"(wf[2]), "+v"(wf[3]), "+v"(ff), "+v"(gm));
    WGBAR();
    v4u pa[8];
#pragma unroll
    for (int k = 0; k < 8; ++k) pa[k] = *(const LAS v4u*)(L + a_l1 + (k & 1) * 544 + (k >> 1) * 64);
    __builtin_amdgcn_sched_barrier(0);
    f32x4 da = {0.f, 0.f, 0.f, 0.f};
#pragma unroll
    for (int k = 0; k < 8; ++k) da = mfma32(wf[k >> 1], pa[k], da);
    v4u r;
#pragma unroll
    for (int j = 0; j < 4; ++j) r[j] = __float_as_uint(fmaf(da[j] + dpp<0x128>(da[j]), gm, ff[j]));
    r.y &= ~1u; r.w &= ~1u;
    ST_SC1(r, a_g1);
    STAMP(8);
  } else {
    v4u p0, p1;
    asm volatile("s_waitcnt lgkmcnt(0)\n\ts_barrier\n\ts_sleep %0" :: "n"(PDELAY1) : "memory");
    poll2(rws, a_g0, a_g0 + 1024, tg, p0, p1);
    *(LAS v4u*)(L + a_l0) = p0;
    *(LAS v4u*)(L + a_l0 + 4 * 272) = p1;
    STAMP(7);
    LDSWAIT();
    WGBAR();
    if (sw == 4) {
      float fin = *(const LAS float*)(L + L_FIN + (l & 3) * 16 + g * 4);
      const float gt = *(const LAS float*)(L + L_FIN + 64);
      float g0 = (l & 12) == 12 ? 1.f : gt, g1 = l < 48 ? g0 : gt;
      const __amdgpu_buffer_rsrc_t rout = __builtin_amdgcn_make_buffer_rsrc((void*)a.out, 0, 8192, 0x00020000);
      asm volatile("s_sleep %3" : "+v"(fin), "+v"(g0), "+v"(g1) : "n"(PDELAY2) : "memory");
      v4u r0, r1;
      poll2(rws, a_g1, a_g1 + 2048, tg, r0, r1);
      STAMP(9);
      f32x4 av = __builtin_bit_cast(f32x4, r0) * g0 + __builtin_bit_cast(f32x4, r1) * g1;
      auto s0 = __builtin_amdgcn_permlane16_swap(__float_as_uint(av[0]), __float_as_uint(av[1]), false, false);
      auto s1 = __builtin_amdgcn_permlane16_swap(__float_as_uint(av[2]), __float_as_uint(av[3]), false, false);
      const float u0 = __uint_as_float(s0[0]) + __uint_as_float(s0[1]), u1 = __uint_as_float(s1[0]) + __uint_as_float(s1[1]);
      auto s2 = __builtin_amdgcn_permlane32_swap(__float_as_uint(u0), __float_as_uint(u1), false, false);
      float v = __uint_as_float(s2[0]) + __uint_as_float(s2[1]);
      v += dpp<0x124>(v); v += dpp<0x128>(v);
      v += fin;
      __builtin_amdgcn_raw_buffer_store_b16((unsigned short)cvtpk(v, v), rout, a_l1, 0, OAUX);
      if (c == 0 && l == 0) __builtin_amdgcn_raw_buffer_store_b64(v2u{tg, (unsigned)(tag >> 32)}, rws, WS_E, 0, OAUX);
      STAMP(10);
    }
  }
  FLUSH();
}

extern "C" void kernel_launch(void* const* d_in, const int* in_sizes, int n_in, void* d_out, int out_size, void* d_ws, size_t ws_size,
                              hipStream_t stream) {
  Args a;
  a.x = (const unsigned short*)d_in[0];
  a.kc = (const unsigned char*)d_in[1];
  a.vc = (const unsigned char*)d_in[2];
  a.wq = (const unsigned short*)d_in[3];
  a.wo = (const unsigned short*)d_in[4];
  a.w1 = (const unsigned short*)d_in[6];
  a.b1 = (const unsigned short*)d_in[7];
  a.w2 = (const unsigned short*)d_in[8];
  a.b2 = (const unsigned short*)d_in[9];
  a.gw = (const unsigned short*)d_in[10];
  a.ws = (unsigned char*)d_ws;
  a.out = (unsigned short*)d_out;
  fused_decode<<<256, NWAVE * 64, 0, stream>>>(a);
}
```

```cpp
#define PROBE 4
#define PW0 2
#define PW1 10
#include <hip/hip_runtime.h>
#include <hip/hip_bf16.h>
#include <stdint.h>

typedef unsigned v4u __attribute__((ext_vector_type(4)));
typedef unsigned v2u __attribute__((ext_vector_type(2)));
typedef float f32x4 __attribute__((ext_vector_type(4)));
typedef float f32x2 __attribute__((ext_vector_type(2)));
typedef __bf16 bf16x8 __attribute__((ext_vector_type(8)));
typedef __bf16 bf16x2 __attribute__((ext_vector_type(2)));
typedef short s16x4 __attribute__((ext_vector_type(4)));
typedef unsigned long long u64;
#define GAS __attribute__((address_space(1)))
#define LAS __attribute__((address_space(3)))

constexpr int DM = 512, HKV = 8, SK = 4096, FF = 1792;
constexpr int AUX_SC1 = 16;
#define DT 465
#define DTV 440
#define T0 130
#define WIN 5
#define SLIP 40
#define UP1 0
#define UP2 30
#define DN2 40
#define JA 24
#define TQ 560
#define TH 480
#define TF 1500
#define GQ 6
#define GH 3
#define WOS 6
#define PDELAY1 19
#define PDELAY2 18
#define CODEB 17760
#define AUXE 18
constexpr int AUX_E = AUXE;
#define EBAR 1
#define EBAR0 ""
#define SLIP0 30
#define RELQ 1
#define SQZ 0
#define DT2 DT
#define DT0 DT
#define J1 10
#define WSP 0
#define OAUX 0
#define LNT 0
#define LSL 16
#define SAMP ((l & 7) + 16 + ((l >> 3) & 3) * 64)
constexpr int NWAVE = 10;

constexpr int WS_E = 0;
constexpr int WS_F = 2048;
constexpr int WS_QP = 4096;
constexpr int WS_HP = WS_QP + 8 * 8 * 2 * 512 * 8;
constexpr int WS_X = WS_HP + 7 * 8 * 2 * 256 * 8;
constexpr int WS_LSE = WS_X + 256 * 128;
constexpr int WS_END = WS_LSE + 256 * 128;
constexpr int WS_O = WS_QP;
constexpr int WS_P = WS_HP;
static_assert(WS_LSE - WS_P == 32 * 8 * 4 * 256, "the partials fill exactly the lines of HP and X");
constexpr int WS_ST = 8 << 20;
constexpr int L_WO = 0, L_WT = 16384, L_MRG = 49152, L_Q = L_MRG + 16384, L_X = L_Q + 1024, L_H = L_X + 8192, L_NIL = L_H + 8192, L_FF = L_NIL + 1024,
              L_CF = L_FF + 4096, L_G = L_CF + 128, L_FIN = L_G + 32,
              L_SYNC = L_FIN + 96,
              L_ML = L_SYNC, L_QRDY = L_ML + 512, L_TEND = L_QRDY + 4, L_HRDY = L_QRDY + 8, L_WRDY = L_QRDY + 12, L_ORG = L_QRDY + 16, L_DONE = L_QRDY + 32,
              L_END = L_SYNC + 1024;
constexpr int L_PG = L_WT;

__device__ __forceinline__ f32x4 mfma32(v4u a, v4u b, f32x4 c) {
  return __builtin_amdgcn_mfma_f32_16x16x32_bf16(__builtin_bit_cast(bf16x8, a), __builtin_bit_cast(bf16x8, b), c, 0, 0, 0);
}
__device__ __forceinline__ f32x4 mfma16(v2u a, v2u b, f32x4 c) {
  return __builtin_amdgcn_mfma_f32_16x16x16bf16_1k(__builtin_bit_cast(s16x4, a), __builtin_bit_cast(s16x4, b), c, 0, 0, 0);
}
__device__ __forceinline__ unsigned cvtpk(float lo, float hi) { bf16x2 r = {(__bf16)lo, (__bf16)hi}; return __builtin_bit_cast(unsigned, r); }
__device__ __forceinline__ float bf_lo(unsigned u) { return __builtin_bit_cast(float, u << 16); }
__device__ __forceinline__ float bf_hi(unsigned u) { return __builtin_bit_cast(float, u & 0xffff0000u); }
__device__ __forceinline__ float rbf(float x) { return (float)(__bf16)x; }
template <int CTRL> __device__ __forceinline__ float dpp(float x) { return __builtin_bit_cast(float, __builtin_amdgcn_mov_dpp(__builtin_bit_cast(int, x), CTRL, 0xf, 0xf, true)); }
template <int CTRL> __device__ __forceinline__ unsigned dppu(unsigned x) { return (unsigned)__builtin_amdgcn_mov_dpp((int)x, CTRL, 0xf, 0xf, true); }
__device__ __forceinline__ float xrow_max(float x) {
  auto s = __builtin_amdgcn_permlane16_swap(__float_as_uint(x), __float_as_uint(x), false, false);
  x = fmaxf(__uint_as_float(s[0]), __uint_as_float(s[1]));
  auto t = __builtin_amdgcn_permlane32_swap(__float_as_uint(x), __float_as_uint(x), false, false);
  return fmaxf(__uint_as_float(t[0]), __uint_as_float(t[1]));
}
__device__ __forceinline__ float xrow_sum(float x) {
  auto s = __builtin_amdgcn_permlane16_swap(__float_as_uint(x), __float_as_uint(x), false, false);
  x = __uint_as_float(s[0]) + __uint_as_float(s[1]);
  auto t = __builtin_amdgcn_permlane32_swap(__float_as_uint(x), __float_as_uint(x), false, false);
  return __uint_as_float(t[0]) + __uint_as_float(t[1]);
}
__device__ __forceinline__ v2u tr_read(const LAS unsigned char* p) { return __builtin_bit_cast(v2u, __builtin_amdgcn_ds_read_tr16_b64_v4i16((LAS s16x4*)p)); }
__device__ __forceinline__ v4u make_srd(const void* p, unsigned bytes) {
  const u64 a = (u64)p;
  return v4u{(unsigned)a, (unsigned)(a >> 32) & 0xffffu, bytes, 0x00020000u};
}
#define DMA_LOOP(POL, srd, voff, step, lds_addr, n) do { unsigned keep_, so_ = 0, n_ = (n); \
  asm volatile("s_cmp_eq_u32 %2, 0\n\ts_cbranch_scc1 2f\n\ts_mov_b32 %0, m0\n\ts_mov_b32 m0, %3\n\ts_nop 0\n1:\n\tbuffer_load_dwordx4 %4, %5, %1 offen " POL " lds\n\t" \
               "s_add_u32 m0, m0, 0x400\n\ts_add_u32 %1, %1, %6\n\ts_sub_u32 %2, %2, 1\n\ts_cmp_lg_u32 %2, 0\n\ts_cbranch_scc1 1b\n\ts_mov_b32 m0, %0\n2:" \
               : "=&s"(keep_), "+&s"(so_), "+&s"(n_) : "s"(lds_addr), "v"(voff), "s"(srd), "s"(step) : "scc", "memory"); } while (0)
__device__ __forceinline__ v4u tile_frag(const LAS unsigned char* tile, int l, int k, int ct) {
  const LAS unsigned char* p = tile + (8 * (l >> 4) + ((l & 15) >> 2)) * 128 + (l & 3) * 8 + k * 4096 + ct * 32;
  const v2u t0 = tr_read(p), t1 = tr_read(p + 512);
  return v4u{t0.x, t0.y, t1.x, t1.y};
}
#define DRAIN() asm volatile("s_waitcnt vmcnt(0)" ::: "memory")
#define LDSWAIT() asm volatile("s_waitcnt lgkmcnt(0)" ::: "memory")
#define WGBAR() do { __builtin_amdgcn_sched_barrier(0); __builtin_amdgcn_s_barrier(); __builtin_amdgcn_sched_barrier(0); asm volatile("" ::: "memory"); } while (0)
__device__ __forceinline__ u64 read_tag(const unsigned char* ws) {
  u64 t;
  asm volatile("s_load_dwordx2 %0, %1, 0x0\n\ts_waitcnt lgkmcnt(0)" : "=s"(t) : "s"(ws) : "memory");
  const unsigned old = (unsigned)t;
  t += 0x9E3779B97F4A7C15ull;
  t = (t ^ (t >> 30)) * 0xBF58476D1CE4E5B9ull; t = (t ^ (t >> 27)) * 0x94D049BB133111EBull;
  t ^= t >> 31;
  t |= 1u;
  return (unsigned)t == old ? t ^ 2u : t;
}
__device__ __forceinline__ unsigned now_ticks() {
  u64 t;
  asm volatile("s_memrealtime %0\n\ts_waitcnt lgkmcnt(0)" : "=s"(t) :: "memory");
  return (unsigned)t;
}
#define WAIT_TICK(base, delta) \
  asm volatile("s_branch 2f\n1:\n\ts_sleep 1\n2:\n\ts_memrealtime vcc\n\ts_waitcnt lgkmcnt(0)\n\ts_sub_u32 vcc_lo, vcc_lo, %0\n\ts_cmpk_lt_i32 vcc_lo, %1\n\ts_cbranch_scc1 1b" \
               :: "s"(base), "n"(delta) : "scc", "vcc", "memory")
#define WAIT_NEXT(acc, step) \
  asm volatile("s_branch 2f\n1:\n\ts_sleep 1\n2:\n\ts_memrealtime vcc\n\ts_waitcnt lgkmcnt(0)\n\ts_lshl_b32 vcc_lo, vcc_lo, 4\n\ts_sub_u32 vcc_lo, vcc_lo, %0\n\ts_cmp_lt_i32 vcc_lo, 0\n\ts_cbranch_scc1 1b\n\ts_add_u32 %0, %0, %1" \
               : "+s"(acc) : "s"(step) : "scc", "vcc", "memory")
template <int LO, int HI> __device__ __forceinline__ int sclamp(int x) {
  asm("s_max_i32 %0, %0, %1\n\ts_min_i32 %0, %0, %2" : "+s"(x) : "n"(LO), "n"(HI) : "scc");
  return x;
}
constexpr int SPIN_CAP = 1 << 18;
#define LD_SC1(off) __builtin_amdgcn_raw_buffer_load_b128(rws, (off), 0, AUX_SC1)
#define ST_SC1(v, off) __builtin_amdgcn_raw_buffer_store_b128((v), rws, (off), 0, AUX_SC1)
__device__ __forceinline__ v4u gran2(float a, float b, unsigned tg) { return v4u{__float_as_uint(a), tg, __float_as_uint(b), tg}; }
__device__ __forceinline__ unsigned gbad(v4u g, unsigned tg) { return (g.y ^ tg) | (g.w ^ tg); }
__device__ __forceinline__ f32x2 gval(v4u g) { return f32x2{__uint_as_float(g.x), __uint_as_float(g.z)}; }
__device__ __forceinline__ v4u gran_d(f32x4 d, int l, unsigned tg) { return gran2((l & 8) ? d[2] : d[0], (l & 8) ? d[3] : d[1], tg); }

#define PSTAMP(k) STAMP(k)
#define STAMP(k) do { if ((k) == PROBE && w >= PW0 && w < PW1) { const unsigned t_ = now_ticks() - t_in; if (l == 0) *(volatile LAS unsigned*)(L + L_END + w * 4) = t_; LDSWAIT(); } } while (0)
#define FLUSH() do { } while (0)

__device__ __forceinline__ void watch(__amdgpu_buffer_rsrc_t rws, int miss, unsigned tg, int& spin) {
#pragma nounroll
  while (miss >= 0 && spin < SPIN_CAP) {
    asm volatile("s_sleep 24" ::: "memory");
    if (gbad(__builtin_amdgcn_raw_buffer_load_b128(rws, miss, 0, AUX_E), tg) == 0) miss = -1;
    ++spin;
  }
  spin = __builtin_amdgcn_ballot_w64(spin >= SPIN_CAP) != 0 ? SPIN_CAP : __builtin_amdgcn_readfirstlane(spin) + 1;
}
template <int SLEEP> __device__ __forceinline__ void lds_wait(const LAS unsigned char* p) {
#pragma nounroll
  for (int spin = 0; spin < SPIN_CAP * 16; ++spin) {
    if (__builtin_amdgcn_ballot_w64(*(volatile const LAS unsigned*)p == 0u) == 0) break;
    if (SLEEP) asm volatile("s_sleep %0" :: "n"(SLEEP) : "memory");
  }
  asm volatile("" ::: "memory");
}
#define LDS_SET(off) (*(volatile LAS unsigned*)(L + (off)) = 1u)
__device__ __forceinline__ void poll2(__amdgpu_buffer_rsrc_t rws, int o0, int o1, unsigned tg, v4u& r0, v4u& r1) {
#pragma nounroll
  for (int spin = 0; spin < SPIN_CAP; ++spin) {
    r0 = LD_SC1(o0); r1 = LD_SC1(o1);
    if (__builtin_amdgcn_ballot_w64(min(min(r0.y ^ tg, r0.w ^ tg), min(r1.y ^ tg, r1.w ^ tg)) == 0u) == 0) break;
    asm volatile("" ::: "memory");
  }
}
__device__ __forceinline__ unsigned notag(unsigned x, unsigned tg) { return x == tg ? x ^ 1u : x; }
__device__ __forceinline__ float gate_of(v4u xv, v4u gv) {
  float gs = 0.f;
#pragma unroll
  for (int j = 0; j < 4; ++j) { gs += rbf(bf_lo(xv[j]) * bf_lo(gv[j])); gs += rbf(bf_hi(xv[j]) * bf_hi(gv[j])); }
  gs += dpp<0xB1>(gs); gs += dpp<0x4E>(gs); gs += dpp<0x124>(gs); gs += dpp<0x128>(gs); gs = xrow_sum(gs);
  return rbf(1.f / (1.f + __expf(-rbf(gs))));
}

struct Args {
  const unsigned short *x, *wq, *wo, *w1, *b1, *w2, *b2, *gw;
  const unsigned char *kc, *vc;
  unsigned char* ws;
  unsigned short* out;
};

typedef unsigned v16u __attribute__((ext_vector_type(16)));
typedef unsigned v8u __attribute__((ext_vector_type(8)));

__global__ void __launch_bounds__(NWAVE * 64) fused_decode(Args a_) {
  v16u ka0; v8u ka1; u64 t_in64;
  asm volatile("s_load_dwordx16 %0, %3, 0x0\n\ts_load_dwordx8 %1, %3, 0x40\n\ts_memrealtime %2" : "=&s"(ka0), "=&s"(ka1), "=&s"(t_in64) : "s"(__builtin_amdgcn_kernarg_segment_ptr()));
  __shared__ __attribute__((aligned(16))) unsigned char smem[L_END + 64];
  LAS unsigned char* L = (LAS unsigned char*)smem;
  const unsigned lbase = (unsigned)(uintptr_t)L;
  const int tid = threadIdx.x, l = tid & 63, w = __builtin_amdgcn_readfirstlane(tid >> 6);
  const int c = blockIdx.x, xr = c >> 3, sp = xr & 3, kvh = xr >> 2, b = (c - kvh) & 7, i32 = b * 4 + sp;
  {
    u64 pc;
    asm volatile("s_getpc_b64 %0" : "=s"(pc));
    const v4u rc = make_srd((const void*)(pc & ~255ull), CODEB);
    DMA_LOOP("", rc, ((xr * 2 + w) & 31) * 1024 + l * 16, 0, lbase + L_NIL, (12u >> w) & 1u);
  }
  const int g = l >> 4;
  const int oh = (c & 7) * 4 + (xr >> 3), on = xr & 7;
  const bool ffn2 = xr >= 24 && (c & 7) < 7;
  const bool isq = xr < 16, side = xr < 23 || ffn2;
  const int tl = isq ? ((xr >> 1) * 8 + ((c + xr) & 7)) * 2 + (xr & 1) : (c & 7) * 7 + xr - 16;
  const int cb = ffn2 ? on : tl >> 1, kh = ffn2 ? c & 7 : tl & 1;
#define KP(v, i) ((u64)v[i] | ((u64)v[(i) + 1] << 32))
  if (w == 1) *(LAS v4u*)(L + L_SYNC + l * 16) = v4u{0u, 0u, 0u, 0u};
  if (__builtin_expect(w == 0, 1)) {
    const unsigned ldb = isq ? 8192u : ffn2 ? 1024u : (unsigned)FF * 2u;
    unsigned woff = (unsigned)kh * 256u * ldb + cb * 128, wlen = 255u * ldb + 128u, wstep = 8u * ldb, wvo = (unsigned)(l >> 3) * ldb + (unsigned)(l & 7) * 16u;
    unsigned nw = side ? 32 : 0;
    asm volatile("s_waitcnt lgkmcnt(0)" EBAR0 : "+s"(ka0), "+s"(woff), "+s"(wlen), "+s"(wstep), "+s"(nw), "+v"(wvo) :: "memory");
    u64 pq = KP(ka0, 2), p1 = KP(ka0, 6), p2 = KP(ka0, 10);
    asm volatile("" : "+s"(pq), "+s"(p1), "+s"(p2));
    DMA_LOOP("nt", make_srd((const unsigned char*)(isq ? pq : ffn2 ? p2 : p1) + woff, wlen), wvo, wstep, lbase + L_WT, nw);
    DMA_LOOP("", make_srd((const void*)KP(ka0, 0), 8192), l * 16, 1024, lbase + L_X, nw >> 2);
  }
  asm volatile("s_waitcnt lgkmcnt(0)" : "+s"(t_in64), "+s"(ka0), "+s"(ka1) :: "memory");
  if (EBAR || w != 0) WGBAR();
  const unsigned t_in = (unsigned)t_in64;
  Args a;
  a.x = (const unsigned short*)KP(ka0, 0); a.wq = (const unsigned short*)KP(ka0, 2); a.wo = (const unsigned short*)KP(ka0, 4); a.w1 = (const unsigned short*)KP(ka0, 6);
  a.b1 = (const unsigned short*)KP(ka0, 8); a.w2 = (const unsigned short*)KP(ka0, 10); a.b2 = (const unsigned short*)KP(ka0, 12); a.gw = (const unsigned short*)KP(ka0, 14);
  a.kc = (const unsigned char*)KP(ka1, 0); a.vc = (const unsigned char*)KP(ka1, 2); a.ws = (unsigned char*)KP(ka1, 4); a.out = (unsigned short*)KP(ka1, 6);
  unsigned char* ws = a.ws;
  if (l == 0) *(volatile LAS unsigned*)(L + L_END + w * 4) = 0;
  PSTAMP(21);
  const __amdgpu_buffer_rsrc_t rws = __builtin_amdgcn_make_buffer_rsrc((void*)ws, 0, WS_END, 0x00020000);
  const int rr = c, rb = rr & 7, rc = rr >> 3;

  if (w == 0) {
    if (side) {
      const unsigned tg = (unsigned)read_tag(ws);
      float og = 0.f;
      if (ffn2) {
        const unsigned bb0 = *(const GAS unsigned*)(a.b1 + kh * 256 + l * 2), bb1 = *(const GAS unsigned*)(a.b1 + kh * 256 + 128 + l * 2);
        const v4u gv = *(const GAS v4u*)(a.gw + l * 8);
        DRAIN();
        const unsigned t_w = now_ticks();
        float gt = 0.f;
#pragma nounroll
        for (int j = 0; j < 8; ++j) { const float t = gate_of(*(const LAS v4u*)(L + L_X + j * 1024 + l * 16), gv); gt = (l & 7) == j ? t : gt; }
        if (l < 8) *(LAS float*)(L + L_G + l * 4) = gt;
        og = rbf(1.f - gt);
        WAIT_TICK(t_in, TH);
        if (RELQ) WAIT_TICK(t_w, 250);
        int spin = 0;
#pragma nounroll
        for (int hf = 0; hf < 2;) {
          v4u t[16];
#pragma unroll
          for (int i = 0; i < 16; ++i) {
            t[i] = __builtin_amdgcn_raw_buffer_load_b128(rws, WS_HP + kh * 32768 + hf * 16384 + l * 16, i * 1024, AUX_E);
            asm volatile("s_sleep %0" :: "n"(GH) : "memory");
          }
          int miss = -1;
#pragma unroll
          for (int j = 0; j < 8; ++j) {
            const int i0 = (j >> 1) * 4 + (j & 1);
            const unsigned bb = (j & 1) ? bb1 : bb0;
            miss = gbad(t[i0], tg) ? i0 : gbad(t[i0 + 2], tg) ? i0 + 2 : miss;
            const f32x2 z = gval(t[i0]) + gval(t[i0 + 2]);
            const float h0 = rbf(rbf(z.x) + bf_lo(bb)), h1 = rbf(rbf(z.y) + bf_hi(bb));
            const float r0 = h0 > 0.f ? h0 : 0.f, r1 = h1 > 0.f ? h1 : 0.f;
            *(LAS unsigned*)(L + L_H + (hf * 4 + (j >> 1)) * 1024 + (j & 1) * 256 + l * 4) = cvtpk(r0 * r0, r1 * r1);
          }
          if (__builtin_amdgcn_ballot_w64(miss >= 0) == 0 || spin >= SPIN_CAP) ++hf;
          else watch(rws, miss < 0 ? -1 : WS_HP + kh * 32768 + hf * 16384 + l * 16 + miss * 1024, tg, spin);
        }
        LDSWAIT();
        if (l == 0) LDS_SET(L_HRDY);
      }
      DRAIN();
      STAMP(1);
      PSTAMP(23);
      v4u xb[8];
      const LAS unsigned char* xp = L + (ffn2 ? L_H : L_X + kh * 512) + (l & 7) * 1024 + g * 16;
#pragma unroll
      for (int k = 0; k < 8; ++k) xb[k] = *(const LAS v4u*)(xp + k * 64);
      const int off = (isq ? WS_QP + (cb >> 3) * 65536 + (l & 7) * 8192 + kh * 4096 + (cb & 7) * 512 : WS_HP + (cb >> 2) * 32768 + (l & 7) * 4096 + kh * 2048 + (cb & 3) * 512)
                      + g * 32 + (l & 8) * 2;
#pragma nounroll
      for (int ct = 0; ct < 4; ++ct) {
        f32x4 d = {0.f, 0.f, 0.f, 0.f};
#pragma unroll
        for (int k = 0; k < 8; ++k) d = mfma32(tile_frag(L + L_WT, l, k, ct), xb[k], d);
        if (ffn2) *(LAS f32x4*)(L + L_FF + ct * 1024 + l * 16) = d * og;
        else { __builtin_amdgcn_raw_buffer_store_b128(gran_d(d, l, tg), rws, off + ct * 128, 0, AUX_E); if (WSP) asm volatile("s_sleep %0" :: "n"(WSP) : "memory"); }
      }
      LDSWAIT();
      PSTAMP(24);
    }
    STAMP(2);
    FLUSH();
    return;
  }
  if (w == 1) {
    const v4u xv = *(const GAS v4u*)(a.x + rb * DM + l * 8), gv = *(const GAS v4u*)(a.gw + l * 8);
    const v2u bb = *(const GAS v2u*)(a.b2 + rc * 16 + 4 * (l & 3));
    const u64 tag = read_tag(ws);
    const unsigned tg = (unsigned)tag, th = (unsigned)(tag >> 32);
    {
      const float gt = gate_of(xv, gv), og = rbf(1.f - gt);
      if (l < 4) *(LAS f32x4*)(L + L_FIN + l * 16) = f32x4{bf_lo(bb.x), bf_hi(bb.x), bf_lo(bb.y), bf_hi(bb.y)} * og;
      if (l == 0) *(LAS float*)(L + L_FIN + 64) = gt;
    }
    lds_wait<4>(L + L_ORG + 4);
    const unsigned org = *(volatile const LAS unsigned*)(L + L_ORG);
    if (l < 8) __builtin_amdgcn_raw_buffer_store_b128(v4u{org, tg, org, tg}, rws, WS_X + c * 128 + l * 16, 0, AUX_E);
    if (RELQ) { unsigned tq = __builtin_amdgcn_readfirstlane(org) + (TQ - T0) * 16; WAIT_NEXT(tq, 0); } else WAIT_TICK(t_in, TQ);
    v2u st;
#pragma nounroll
    for (int spin = 0; spin < SPIN_CAP;) {
      v4u t[8];
#pragma unroll
      for (int i = 0; i < 8; ++i) { t[i] = __builtin_amdgcn_raw_buffer_load_b128(rws, WS_QP + kvh * 65536 + b * 8192 + l * 16, i * 1024, AUX_E); asm volatile("s_sleep %0" :: "n"(GQ) : "memory"); }
      st = __builtin_amdgcn_raw_buffer_load_b64(rws, WS_X + (SAMP) * 128, 0, AUX_SC1);
      int miss = -1;
#pragma unroll
      for (int j = 0; j < 4; ++j) {
        miss = gbad(t[j], tg) ? j : gbad(t[4 + j], tg) ? 4 + j : miss;
        const f32x2 q = gval(t[j]) + gval(t[4 + j]);
        *(LAS unsigned*)(L + L_Q + j * 256 + l * 4) = cvtpk(q.x, q.y);
      }
      if (__builtin_amdgcn_ballot_w64(miss >= 0) == 0) break;
      watch(rws, miss < 0 ? -1 : WS_QP + kvh * 65536 + b * 8192 + l * 16 + miss * 1024, tg, spin);
    }
    {
      unsigned rel = (st.y == tg ? st.x : (now_ticks() << 4) - ((2 * WIN + 1) * DT0 + 2400)) - org;
      rel += dppu<0xB1>(rel); rel += dppu<0x4E>(rel); rel += dppu<0x141>(rel); rel += dppu<0x140>(rel);
      const int sum = __builtin_amdgcn_readlane((int)rel, 0) + __builtin_amdgcn_readlane((int)rel, 16);
      if (l == 0) *(LAS unsigned*)(L + L_TEND) = org + (sum >> 5) + (J1 + 1) * DT0 + (JA - J1) * DT + (31 - JA) * DT2 + 32 * DTV;
    }
    LDSWAIT();
    if (l == 0) LDS_SET(L_QRDY);
    STAMP(3);
    if (ffn2) lds_wait<8>(L + L_HRDY);
    DRAIN();
    if (l == 0) __builtin_amdgcn_raw_buffer_store_b64(v2u{tg, th}, rws, WS_F + c * 8, 0, AUX_SC1);
    STAMP(5);
    lds_wait<4>(L + L_ML + (l & 31) * 16 + 8);
    const v2u ml = *(volatile const LAS v2u*)(L + L_ML + (l & 31) * 16);
    const float mw = __uint_as_float(ml.x);
    float M = fmaxf(mw, dpp<0xB1>(mw)); M = fmaxf(M, dpp<0x4E>(M)); M = fmaxf(M, dpp<0x141>(M));
    float Ls = __builtin_amdgcn_exp2f(mw - M) * __uint_as_float(ml.y);
    Ls += dpp<0xB1>(Ls); Ls += dpp<0x4E>(Ls); Ls += dpp<0x141>(Ls);
    const float lse = M + __builtin_amdgcn_logf(Ls);
    const v4u lg = gran2(lse, dpp<0x128>(lse), tg);
    if (l < 32 && (l & 15) == 0) ST_SC1(lg, WS_LSE + ((b * 8 + kvh) * 4 + sp) * 128 + l);
    STAMP(6);
    v2u ls;
    asm volatile("s_sleep %0" :: "n"(LSL) : "memory");
#pragma nounroll
    for (int spin = 0; spin < SPIN_CAP; ++spin) {
      ls = __builtin_amdgcn_raw_buffer_load_b64(rws, WS_LSE + ((b * 8 + kvh) * 4 + (l & 3)) * 128 + ((l >> 3) & 3) * 8, 0, LNT ? AUX_E : AUX_SC1);
      if (__builtin_amdgcn_ballot_w64(ls.y != tg) == 0) break;
      asm volatile("s_sleep 8" ::: "memory");
    }
    const float lf = __uint_as_float(ls.x);
    float M2 = fmaxf(lf, dpp<0xB1>(lf)); M2 = fmaxf(M2, dpp<0x4E>(M2));
    float E = __builtin_amdgcn_exp2f(lf - M2);
    E += dpp<0xB1>(E); E += dpp<0x4E>(E);
    if (l < 32) *(LAS float*)(L + L_CF + l * 4) = __builtin_amdgcn_exp2f(mw - M2) * __builtin_amdgcn_rcpf(E);
    WAIT_TICK(t_in, TF);
#pragma nounroll
    for (int spin = 0; spin < SPIN_CAP; ++spin) {
      const v4u f0 = LD_SC1(WS_F + l * 16), f1 = LD_SC1(WS_F + 1024 + l * 16);
      if (__builtin_amdgcn_ballot_w64(((f0.x ^ tg) | (f0.y ^ th) | (f0.z ^ tg) | (f0.w ^ th) | (f1.x ^ tg) | (f1.y ^ th) | (f1.z ^ tg) | (f1.w ^ th)) != 0) == 0) break;
      asm volatile("s_sleep 16" ::: "memory");
    }
    LDSWAIT();
    STAMP(7);
    lds_wait<1>(L + L_DONE + (l & 7) * 4);
    asm volatile("s_sleep %0" :: "n"(WOS) : "memory");
    DMA_LOOP("nt", make_srd(a.wo + oh * 128 * DM + on * 64, 127 * 1024 + 128), (unsigned)(l >> 3) * 1024u + (unsigned)(l & 7) * 16u, 8192, lbase + L_WO, 16);
    WGBAR();
    DRAIN();
    if (l == 0) LDS_SET(L_WRDY);
    LDSWAIT();
    STAMP(8);
    FLUSH();
    return;
  }

  const int sw = w - 2;
  const unsigned stag = sw * (DT / 8);
  unsigned tnext = ((t_in + T0) << 4) + stag, dt = DT0;
#define PACE() do { asm volatile("s_waitcnt vmcnt(%0)" :: "n"(WIN - 1) : "memory"); WAIT_NEXT(tnext, dt); } while (0)
#define RESLIP() do { const unsigned t1_ = (now_ticks() - SLIP) << 4; if ((int)(t1_ - tnext) > 0) tnext = t1_ + (stag >> 1); } while (0)
#define REPLAN(done, nominal) do { RESLIP(); \
    const int ex_ = (int)(tnext + ((done) < 32 ? (32 - (done)) * DT2 + 32 * DTV : (64 - (done)) * DTV) - ((done) == 48 ? (stag >> SQZ) : stag) - tend); \
    dt = (nominal) - sclamp<-DN2, UP2>((ex_ * (65536 / (64 - (done)))) >> 16); } while (0)
  const size_t base = ((size_t)(b * HKV + kvh) * SK + sp * 1024 + sw * 128) * 256;
  const v4u rk = make_srd(a.kc + base, 32768), rv = make_srd(a.vc + base, 32768);
  const int ko = ((l >> 2) & 3) * 256 + g * 64 + (l & 3) * 16, vo = l * 16;
    const int rot = (16 * (sw & 1) + 8 * ((sw >> 1) & 1) + 4 * (sw >> 2) + 9 * (sp & 1) + 28 * (sp >> 1) + 30 * (b & 1)) & 31;
#define OFF(i) ((((i) + rot) & 31) * 1024)
#define LOAD_V(i) asm volatile("buffer_load_dwordx4 %0, %1, %2, %3 offen nt" : "+v"(kv[i]) : "v"(vo), "s"(rv), "s"(OFF(i)) : "memory")
  const u64 tag = read_tag(ws);
  const unsigned tg = (unsigned)tag;
  unsigned tend = 0;
  { const unsigned t1 = ((now_ticks() - SLIP0) << 4) + stag; if ((int)(t1 - tnext) > 0) tnext = t1; }
  v4u kv[32];
  __builtin_amdgcn_sched_barrier(0);
#pragma unroll
  for (int i = 0; i < 32; ++i) {
    PACE();
    asm volatile("buffer_load_dwordx4 %0, %1, %2, %3 offen nt" : "=v"(kv[i]) : "v"(ko), "s"(rk), "s"(OFF(i)) : "memory");
    PSTAMP(100 + i);
    if (i == WIN) RESLIP();
    if (i == 2 * WIN) {
      RESLIP();
      const unsigned org = tnext - stag - (2 * WIN + 1) * DT0;
      if (sw == 3 && l == 0) { *(volatile LAS unsigned*)(L + L_ORG) = org; LDS_SET(L_ORG + 4); }
    }
    if (i == J1) dt = DT;
    if (i == JA) {
      const unsigned f = __builtin_amdgcn_readfirstlane(*(volatile const LAS unsigned*)(L + L_QRDY));
      tend = __builtin_amdgcn_readfirstlane(*(volatile const LAS unsigned*)(L + L_TEND));
      if (f) REPLAN(JA + 1, DT2);
    }
    __builtin_amdgcn_sched_barrier(0);
  }
  STAMP(1);
  lds_wait<2>(L + L_QRDY);
  STAMP(2);
  const v4u qf = *(const LAS v4u*)(L + L_Q + ((l >> 2) & 3) * 256 + g * 64 + (l & 3) * 16);
  tend = __builtin_amdgcn_readfirstlane(*(const LAS unsigned*)(L + L_TEND));
  REPLAN(32, DTV);
  __builtin_amdgcn_sched_barrier(0);
  constexpr float C2 = 0.088388347648318440f * 1.4426950408889634f;
  float sc[8];
  const int s = l & 3;
#pragma unroll
  for (int m = 0; m < 8; ++m) {
    asm volatile("s_waitcnt vmcnt(%4)" : "+v"(kv[4 * m]), "+v"(kv[4 * m + 1]), "+v"(kv[4 * m + 2]), "+v"(kv[4 * m + 3]) : "n"(28 - 4 * m + m) : "memory");
    f32x4 d[4];
#pragma unroll
    for (int r = 0; r < 4; ++r) d[r] = mfma32(kv[4 * m + r], qf, f32x4{0.f, 0.f, 0.f, 0.f});
#pragma unroll
    for (int r = 0; r < 4; ++r) {
      float t = dpp<0x00>(d[r][0]); t = dpp<0x55>(d[r][1]) + t; t = dpp<0xAA>(d[r][2]) + t; t = dpp<0xFF>(d[r][3]) + t;
      asm volatile("" : "+v"(t));
      sc[m] = (r == 0 || s == r) ? t : sc[m];
    }
    asm volatile("" : "+v"(sc[m]));
    __builtin_amdgcn_sched_barrier(0);
    PACE();
    LOAD_V(m);
    __builtin_amdgcn_sched_barrier(0);
  }
  STAMP(3);
  float mx = sc[0];
#pragma unroll
  for (int m = 1; m < 8; ++m) mx = fmaxf(mx, sc[m]);
  mx = fmaxf(mx, dpp<0xB1>(mx)); mx = fmaxf(mx, dpp<0x4E>(mx)); mx = xrow_max(mx);
  const float mneg = -mx * C2;
  float lsum = 0.f;
  unsigned pk[8];
#pragma unroll
  for (int m = 0; m < 8; ++m) { const float p = __builtin_amdgcn_exp2f(fmaf(sc[m], C2, mneg)); pk[m] = cvtpk(p, p); lsum += bf_lo(pk[m]); }
  lsum += dpp<0xB1>(lsum); lsum += dpp<0x4E>(lsum); lsum = xrow_sum(lsum);
  if (l < 16 && (l & 3) == 0) {
    *(volatile LAS v2u*)(L + L_ML + ((l >> 2) * 8 + sw) * 16) = v2u{__float_as_uint(mx * C2), __float_as_uint(lsum)};
    LDS_SET(L_ML + ((l >> 2) * 8 + sw) * 16 + 8);
  }
  const unsigned half = (l & 1) ? 0xffff0000u : 0x0000ffffu;
  const unsigned m0 = (l & 2) ? 0u : half, m1 = (l & 2) ? half : 0u;
  f32x4 o0 = {0.f, 0.f, 0.f, 0.f}, o1 = {0.f, 0.f, 0.f, 0.f};
  const int s4 = sw & 3, pb8 = l & 7;
  unsigned a_mw, a_mr, a_g0, a_l0, a_l1, a_g1;
#pragma unroll
  for (int j = 8; j < 40; ++j) {
    if (j < 32) { PACE(); LOAD_V(j); if (j == 15) REPLAN(48, DTV); __builtin_amdgcn_sched_barrier(0); }
    if (j == 31) { if (l == 0) LDS_SET(L_DONE + sw * 4); STAMP(11); }
    if (j == 33) {
      a_mw = L_MRG + (sw * 4 + g) * 512 + (l & 15) * 32;
      a_mr = L_MRG + s4 * 512 + l * 8;
      if (sw < 4) {
        a_g0 = (l & 3) == 0 ? WS_P + (((kvh * 4 + s4) * 8 + b) * 4 + sp) * 256 + l * 4 : WS_END;
        a_l0 = L_WO + (8 * g + ((l & 15) >> 2)) * 128 + (l & 3) * 8 + s4 * 32;
        a_l1 = L_PG + (pb8 * 4 + ((l >> 3) & 1)) * 272 + g * 16;
        a_g1 = (l & 8) == 0 ? WS_O + (((on * 4 + s4) * 4 + (pb8 >> 1)) * 32 + oh) * 128 + (pb8 & 1) * 64 + g * 16 : WS_END;
      } else {
        a_g0 = WS_P + (oh * 8 + 2 * s4) * 1024 + l * 16;
        a_l0 = L_PG + (8 * s4 + g) * 272 + (l & 15) * 16;
        a_l1 = (l & 15) < 4 ? (rb * DM + rc * 16 + 4 * (l & 3) + g) * 2 : 8192;
        a_g1 = WS_O + (rr >> 1) * 4096 + (l >> 2) * 128 + (rr & 1) * 64 + (l & 3) * 16;
      }
      asm volatile("" : "+v"(a_mw), "+v"(a_mr), "+v"(a_g0), "+v"(a_l0), "+v"(a_l1), "+v"(a_g1));
      __builtin_amdgcn_sched_barrier(0);
    }
    const int i = j - 8;
    const unsigned pb = (i & 3) == 0 ? dppu<0x00>(pk[i >> 2]) : (i & 3) == 1 ? dppu<0x55>(pk[i >> 2]) : (i & 3) == 2 ? dppu<0xAA>(pk[i >> 2]) : dppu<0xFF>(pk[i >> 2]);
    const v2u av = {pb & m0, pb & m1};
    asm volatile("s_waitcnt vmcnt(%1)" : "+v"(kv[i]) : "n"((j < 32 ? j : 31) - i) : "memory");
    o0 = mfma16(av, v2u{kv[i].x, kv[i].y}, o0);
    o1 = mfma16(av, v2u{kv[i].z, kv[i].w}, o1);
    __builtin_amdgcn_sched_barrier(0);
  }
  *(LAS f32x4*)(L + a_mw) = o0;
  *(LAS f32x4*)(L + a_mw + 16) = o1;
  STAMP(4);
  if (sw < 4) {
    asm volatile("s_waitcnt lgkmcnt(0)\n\ts_barrier ; merge" ::: "memory");
    STAMP(5);
    const f32x4 c0 = *(const LAS f32x4*)(L + L_CF + sw * 32), c1 = *(const LAS f32x4*)(L + L_CF + sw * 32 + 16);
    f32x2 ac0 = {0.f, 0.f}, ac1 = {0.f, 0.f};
#pragma unroll
    for (int j = 0; j < 4; ++j) {
      ac0 += *(const LAS f32x2*)(L + a_mr + j * 2048) * c0[j];
      ac1 += *(const LAS f32x2*)(L + a_mr + (4 + j) * 2048) * c1[j];
    }
    ac0 += ac1;
    const unsigned pv = notag(cvtpk(ac0.x, ac0.y), tg);
    ST_SC1((v4u{pv, dppu<0x55>(pv), dppu<0xAA>(pv), dppu<0xFF>(pv)}), a_g0);
    STAMP(6);
    f32x4 ff = {0.f, 0.f, 0.f, 0.f};
    float gm = 1.f;
    if (ffn2) { ff = *(const LAS f32x4*)(L + L_FF + sw * 1024 + l * 16); gm = *(const LAS float*)(L + L_G + pb8 * 4); }
    lds_wait<0>(L + L_WRDY);
    v4u wf[4];
#pragma unroll
    for (int k = 0; k < 4; ++k) { const v2u t0 = tr_read(L + a_l0 + k * 4096), t1 = tr_read(L + a_l0 + k * 4096 + 512); wf[k] = v4u{t0.x, t0.y, t1.x, t1.y}; }
    asm volatile("" : "+v"(wf[0]), "+v"(wf[1]), "+v"(wf[2]), "+v"(wf[3]), "+v"(ff), "+v"(gm));
    WGBAR();
    v4u pa[8];
#pragma unroll
    for (int k = 0; k < 8; ++k) pa[k] = *(const LAS v4u*)(L + a_l1 + (k & 1) * 544 + (k >> 1) * 64);
    __builtin_amdgcn_sched_barrier(0);
    f32x4 da = {0.f, 0.f, 0.f, 0.f};
#pragma unroll
    for (int k = 0; k < 8; ++k) da = mfma32(wf[k >> 1], pa[k], da);
    v4u r;
#pragma unroll
    for (int j = 0; j < 4; ++j) r[j] = __float_as_uint(fmaf(da[j] + dpp<0x128>(da[j]), gm, ff[j]));
    r.y &= ~1u; r.w &= ~1u;
    ST_SC1(r, a_g1);
    STAMP(8);
  } else {
    v4u p0, p1;
    asm volatile("s_waitcnt lgkmcnt(0)\n\ts_barrier\n\ts_sleep %0" :: "n"(PDELAY1) : "memory");
    poll2(rws, a_g0, a_g0 + 1024, tg, p0, p1);
    *(LAS v4u*)(L + a_l0) = p0;
    *(LAS v4u*)(L + a_l0 + 4 * 272) = p1;
    STAMP(7);
    LDSWAIT();
    WGBAR();
    if (sw == 4) {
      float fin = *(const LAS float*)(L + L_FIN + (l & 3) * 16 + g * 4);
      const float gt = *(const LAS float*)(L + L_FIN + 64);
      float g0 = (l & 12) == 12 ? 1.f : gt, g1 = l < 48 ? g0 : gt;
      const __amdgpu_buffer_rsrc_t rout = __builtin_amdgcn_make_buffer_rsrc((void*)a.out, 0, 8192, 0x00020000);
      asm volatile("s_sleep %3" : "+v"(fin), "+v"(g0), "+v"(g1) : "n"(PDELAY2) : "memory");
      v4u r0, r1;
      poll2(rws, a_g1, a_g1 + 2048, tg, r0, r1);
      STAMP(9);
      f32x4 av = __builtin_bit_cast(f32x4, r0) * g0 + __builtin_bit_cast(f32x4, r1) * g1;
      auto s0 = __builtin_amdgcn_permlane16_swap(__float_as_uint(av[0]), __float_as_uint(av[1]), false, false);
      auto s1 = __builtin_amdgcn_permlane16_swap(__float_as_uint(av[2]), __float_as_uint(av[3]), false, false);
      const float u0 = __uint_as_float(s0[0]) + __uint_as_float(s0[1]), u1 = __uint_as_float(s1[0]) + __uint_as_float(s1[1]);
      auto s2 = __builtin_amdgcn_permlane32_swap(__float_as_uint(u0), __float_as_uint(u1), false, false);
      float v = __uint_as_float(s2[0]) + __uint_as_float(s2[1]);
      v += dpp<0x124>(v); v += dpp<0x128>(v);
      v += fin;
      __builtin_amdgcn_raw_buffer_store_b16((unsigned short)cvtpk(v, v), rout, a_l1, 0, OAUX);
      if (c == 0 && l == 0) __builtin_amdgcn_raw_buffer_store_b64(v2u{tg, (unsigned)(tag >> 32)}, rws, WS_E, 0, OAUX);
      STAMP(10);
      { unsigned m = 0;
        for (int i = PW0; i < PW1; ++i) m = max(m, *(volatile LAS unsigned*)(L + L_END + i * 4));
        m = __builtin_amdgcn_readfirstlane(m);
        unsigned tq = (t_in + 4000 + 4 * m) << 4; WAIT_NEXT(tq, dt); }
    }
  }
  FLUSH();
}

extern "C" void kernel_launch(void* const* d_in, const int* in_sizes, int n_in, void* d_out, int out_size, void* d_ws, size_t ws_size,
                              hipStream_t stream) {
  Args a;
  a.x = (const unsigned short*)d_in[0];
  a.kc = (const unsigned char*)d_in[1];
  a.vc = (const unsigned char*)d_in[2];
  a.wq = (const unsigned short*)d_in[3];
  a.wo = (const unsigned short*)d_in[4];
  a.w1 = (const unsigned short*)d_in[6];
  a.b1 = (const unsigned short*)d_in[7];
  a.w2 = (const unsigned short*)d_in[8];
  a.b2 = (const unsigned short*)d_in[9];
  a.gw = (const unsigned short*)d_in[10];
  a.ws = (unsigned char*)d_ws;
  a.out = (unsigned short*)d_out;
  fused_decode<<<256, NWAVE * 64, 0, stream>>>(a);
}
```

```cpp
#include <hip/hip_runtime.h>
#include <hip/hip_bf16.h>
#include <stdint.h>

typedef unsigned v16u __attribute__((ext_vector_type(16)));
typedef unsigned v8u __attribute__((ext_vector_type(8)));
typedef unsigned v4u __attribute__((ext_vector_type(4)));
typedef unsigned v2u __attribute__((ext_vector_type(2)));
typedef float f32x4 __attribute__((ext_vector_type(4)));
typedef float f32x2 __attribute__((ext_vector_type(2)));
typedef __bf16 bf16x8 __attribute__((ext_vector_type(8)));
typedef __bf16 bf16x2 __attribute__((ext_vector_type(2)));
typedef short s16x4 __attribute__((ext_vector_type(4)));
typedef unsigned long long u64;
#define GAS __attribute__((address_space(1)))
#define LAS __attribute__((address_space(3)))

constexpr int DM = 512, HKV = 8, SK = 4096, FF = 1792, NWAVE = 10;
constexpr int AUX_SC1 = 16;
constexpr int AUX_E = 18;
constexpr int SPIN_CAP = 1 << 18;
constexpr int T0 = 115;
constexpr int DT0 = 480, DT = 446, DTV = 435;
constexpr int WIN = 4;
constexpr int J1 = 2 * WIN, JA = 24;
constexpr int SLIP0 = 30, SLIP = 40;
constexpr int UP = 30, DN = 40;
constexpr int TQ = 560, TH = 480, TF = 1500;
constexpr int CODEB = 17408;

constexpr int WS_E = 0;
constexpr int WS_F = 2048;
constexpr int WS_QP = 4096;
constexpr int WS_HP = WS_QP + 8 * 8 * 2 * 512 * 8;
constexpr int WS_X = WS_HP + 7 * 8 * 2 * 256 * 8;
constexpr int WS_LSE = WS_X + 256 * 128;
constexpr int WS_END = WS_LSE + 256 * 128;
constexpr int WS_O = WS_QP;
constexpr int WS_P = WS_HP;
static_assert(WS_LSE - WS_P == 32 * 8 * 4 * 256, "the partials fill exactly the lines of HP and X");
constexpr int L_WO = 0, L_WT = 16384, L_MRG = 49152, L_Q = L_MRG + 16384, L_X = L_Q + 1024, L_H = L_X + 8192, L_NIL = L_H + 8192, L_FF = L_NIL + 1024,
              L_CF = L_FF + 4096, L_G = L_CF + 128, L_FIN = L_G + 32,
              L_SYNC = L_FIN + 96,
              L_ML = L_SYNC, L_QRDY = L_ML + 512, L_TEND = L_QRDY + 4, L_HRDY = L_QRDY + 8, L_WRDY = L_QRDY + 12, L_ORG = L_QRDY + 16, L_DONE = L_QRDY + 32,
              L_END = L_SYNC + 1024;
constexpr int L_PG = L_WT;

__device__ __forceinline__ f32x4 mfma32(v4u a, v4u b, f32x4 c) {
  return __builtin_amdgcn_mfma_f32_16x16x32_bf16(__builtin_bit_cast(bf16x8, a), __builtin_bit_cast(bf16x8, b), c, 0, 0, 0);
}
__device__ __forceinline__ f32x4 mfma16(v2u a, v2u b, f32x4 c) {
  return __builtin_amdgcn_mfma_f32_16x16x16bf16_1k(__builtin_bit_cast(s16x4, a), __builtin_bit_cast(s16x4, b), c, 0, 0, 0);
}
__device__ __forceinline__ unsigned cvtpk(float lo, float hi) { bf16x2 r = {(__bf16)lo, (__bf16)hi}; return __builtin_bit_cast(unsigned, r); }
__device__ __forceinline__ float bf_lo(unsigned u) { return __builtin_bit_cast(float, u << 16); }
__device__ __forceinline__ float bf_hi(unsigned u) { return __builtin_bit_cast(float, u & 0xffff0000u); }
__device__ __forceinline__ float rbf(float x) { return (float)(__bf16)x; }
template <int CTRL> __device__ __forceinline__ float dpp(float x) { return __builtin_bit_cast(float, __builtin_amdgcn_mov_dpp(__builtin_bit_cast(int, x), CTRL, 0xf, 0xf, true)); }
template <int CTRL> __device__ __forceinline__ unsigned dppu(unsigned x) { return (unsigned)__builtin_amdgcn_mov_dpp((int)x, CTRL, 0xf, 0xf, true); }
__device__ __forceinline__ float xrow_max(float x) {
  auto s = __builtin_amdgcn_permlane16_swap(__float_as_uint(x), __float_as_uint(x), false, false);
  x = fmaxf(__uint_as_float(s[0]), __uint_as_float(s[1]));
  auto t = __builtin_amdgcn_permlane32_swap(__float_as_uint(x), __float_as_uint(x), false, false);
  return fmaxf(__uint_as_float(t[0]), __uint_as_float(t[1]));
}
__device__ __forceinline__ float xrow_sum(float x) {
  auto s = __builtin_amdgcn_permlane16_swap(__float_as_uint(x), __float_as_uint(x), false, false);
  x = __uint_as_float(s[0]) + __uint_as_float(s[1]);
  auto t = __builtin_amdgcn_permlane32_swap(__float_as_uint(x), __float_as_uint(x), false, false);
  return __uint_as_float(t[0]) + __uint_as_float(t[1]);
}
__device__ __forceinline__ v2u tr_read(const LAS unsigned char* p) { return __builtin_bit_cast(v2u, __builtin_amdgcn_ds_read_tr16_b64_v4i16((LAS s16x4*)p)); }
__device__ __forceinline__ v4u make_srd(const void* p, unsigned bytes) {
  const u64 a = (u64)p;
  return v4u{(unsigned)a, (unsigned)(a >> 32) & 0xffffu, bytes, 0x00020000u};
}
#define DMA_LOOP(POL, srd, voff, step, lds_addr, n) do { unsigned keep_, so_ = 0, n_ = (n); \
  asm volatile("s_cmp_eq_u32 %2, 0\n\ts_cbranch_scc1 2f\n\ts_mov_b32 %0, m0\n\ts_mov_b32 m0, %3\n\ts_nop 0\n1:\n\tbuffer_load_dwordx4 %4, %5, %1 offen " POL " lds\n\t" \
               "s_add_u32 m0, m0, 0x400\n\ts_add_u32 %1, %1, %6\n\ts_sub_u32 %2, %2, 1\n\ts_cmp_lg_u32 %2, 0\n\ts_cbranch_scc1 1b\n\ts_mov_b32 m0, %0\n2:" \
               : "=&s"(keep_), "+&s"(so_), "+&s"(n_) : "s"(lds_addr), "v"(voff), "s"(srd), "s"(step) : "scc", "memory"); } while (0)
__device__ __forceinline__ v4u tile_frag(const LAS unsigned char* tile, int l, int k, int ct) {
  const LAS unsigned char* p = tile + (8 * (l >> 4) + ((l & 15) >> 2)) * 128 + (l & 3) * 8 + k * 4096 + ct * 32;
  const v2u t0 = tr_read(p), t1 = tr_read(p + 512);
  return v4u{t0.x, t0.y, t1.x, t1.y};
}
#define DRAIN() asm volatile("s_waitcnt vmcnt(0)" ::: "memory")
#define LDSWAIT() asm volatile("s_waitcnt lgkmcnt(0)" ::: "memory")
#define SLEEP(n) asm volatile("s_sleep %0" :: "n"(n) : "memory")
#define WGBAR() do { __builtin_amdgcn_sched_barrier(0); __builtin_amdgcn_s_barrier(); __builtin_amdgcn_sched_barrier(0); asm volatile("" ::: "memory"); } while (0)
__device__ __forceinline__ u64 read_tag(const unsigned char* ws) {
  u64 t;
  asm volatile("s_load_dwordx2 %0, %1, 0x0\n\ts_waitcnt lgkmcnt(0)" : "=s"(t) : "s"(ws) : "memory");
  const unsigned old = (unsigned)t;
  t += 0x9E3779B97F4A7C15ull;
  t = (t ^ (t >> 30)) * 0xBF58476D1CE4E5B9ull; t = (t ^ (t >> 27)) * 0x94D049BB133111EBull;
  t ^= t >> 31;
  t = (t | 0x7F81u) & ~0x40ull;
  return (unsigned)t == old ? t ^ 2u : t;
}
__device__ __forceinline__ unsigned now_ticks() {
  u64 t;
  asm volatile("s_memrealtime %0\n\ts_waitcnt lgkmcnt(0)" : "=s"(t) :: "memory");
  return (unsigned)t;
}
#define WAIT_TICK(base, delta) \
  asm volatile("s_branch 2f\n1:\n\ts_sleep 1\n2:\n\ts_memrealtime vcc\n\ts_waitcnt lgkmcnt(0)\n\ts_sub_u32 vcc_lo, vcc_lo, %0\n\ts_cmpk_lt_i32 vcc_lo, %1\n\ts_cbranch_scc1 1b" \
               :: "s"(base), "n"(delta) : "scc", "vcc", "memory")
#define WAIT_NEXT(acc, step) \
  asm volatile("s_branch 2f\n1:\n\ts_sleep 1\n2:\n\ts_memrealtime vcc\n\ts_waitcnt lgkmcnt(0)\n\ts_lshl_b32 vcc_lo, vcc_lo, 4\n\ts_sub_u32 vcc_lo, vcc_lo, %0\n\ts_cmp_lt_i32 vcc_lo, 0\n\ts_cbranch_scc1 1b\n\ts_add_u32 %0, %0, %1" \
               : "+s"(acc) : "s"(step) : "scc", "vcc", "memory")
template <int LO, int HI> __device__ __forceinline__ int sclamp(int x) {
  asm("s_max_i32 %0, %0, %1\n\ts_min_i32 %0, %0, %2" : "+s"(x) : "n"(LO), "n"(HI) : "scc");
  return x;
}
#define LD_SC1(off) __builtin_amdgcn_raw_buffer_load_b128(rws, (off), 0, AUX_SC1)
#define ST_SC1(v, off) __builtin_amdgcn_raw_buffer_store_b128((v), rws, (off), 0, AUX_SC1)
__device__ __forceinline__ v4u gran2(float a, float b, unsigned tg) { return v4u{__float_as_uint(a), tg, __float_as_uint(b), tg}; }
__device__ __forceinline__ unsigned gbad(v4u g, unsigned tg) { return (g.y ^ tg) | (g.w ^ tg); }
__device__ __forceinline__ f32x2 gval(v4u g) { return f32x2{__uint_as_float(g.x), __uint_as_float(g.z)}; }
__device__ __forceinline__ v4u gran_d(f32x4 d, int l, unsigned tg) { return gran2((l & 8) ? d[2] : d[0], (l & 8) ? d[3] : d[1], tg); }
__device__ __forceinline__ void watch(__amdgpu_buffer_rsrc_t rws, int miss, unsigned tg, int& spin) {
#pragma nounroll
  while (miss >= 0 && spin < SPIN_CAP) {
    SLEEP(24);
    if (gbad(__builtin_amdgcn_raw_buffer_load_b128(rws, miss, 0, AUX_E), tg) == 0) miss = -1;
    ++spin;
  }
  spin = __builtin_amdgcn_ballot_w64(spin >= SPIN_CAP) != 0 ? SPIN_CAP : __builtin_amdgcn_readfirstlane(spin) + 1;
}
template <int NAP> __device__ __forceinline__ void lds_wait(const LAS unsigned char* p) {
#pragma nounroll
  for (int spin = 0; spin < SPIN_CAP * 16; ++spin) {
    if (__builtin_amdgcn_ballot_w64(*(volatile const LAS unsigned*)p == 0u) == 0) break;
    if (NAP) SLEEP(NAP);
  }
  asm volatile("" ::: "memory");
}
#define LDS_SET(off) (*(volatile LAS unsigned*)(L + (off)) = 1u)
__device__ __forceinline__ void poll2(__amdgpu_buffer_rsrc_t rws, int o0, int o1, unsigned tg, v4u& r0, v4u& r1) {
#pragma nounroll
  for (int spin = 0; spin < SPIN_CAP; ++spin) {
    r0 = LD_SC1(o0); r1 = LD_SC1(o1);
    if (__builtin_amdgcn_ballot_w64(min(min(r0.y ^ tg, r0.w ^ tg), min(r1.y ^ tg, r1.w ^ tg)) == 0u) == 0) break;
    asm volatile("" ::: "memory");
  }
}
__device__ __forceinline__ float gate_of(v4u xv, v4u gv) {
  float gs = 0.f;
#pragma unroll
  for (int j = 0; j < 4; ++j) { gs += rbf(bf_lo(xv[j]) * bf_lo(gv[j])); gs += rbf(bf_hi(xv[j]) * bf_hi(gv[j])); }
  gs += dpp<0xB1>(gs); gs += dpp<0x4E>(gs); gs += dpp<0x124>(gs); gs += dpp<0x128>(gs); gs = xrow_sum(gs);
  return rbf(1.f / (1.f + __expf(-rbf(gs))));
}

struct Args {
  const unsigned short *x, *wq, *wo, *w1, *b1, *w2, *b2, *gw;
  const unsigned char *kc, *vc;
  unsigned char* ws;
  unsigned short* out;
};

__global__ void __launch_bounds__(NWAVE * 64, 3) fused_decode(Args a_) {
  v16u ka0; v8u ka1; u64 t_in64;
  asm volatile("s_load_dwordx16 %0, %3, 0x0\n\ts_load_dwordx8 %1, %3, 0x40\n\ts_memrealtime %2" : "=&s"(ka0), "=&s"(ka1), "=&s"(t_in64) : "s"(__builtin_amdgcn_kernarg_segment_ptr()));
  __shared__ __attribute__((aligned(16))) unsigned char smem[L_END];
  LAS unsigned char* L = (LAS unsigned char*)smem;
  const unsigned lbase = (unsigned)(uintptr_t)L;
  const int tid = threadIdx.x, l = tid & 63, w = __builtin_amdgcn_readfirstlane(tid >> 6);
  const int c = blockIdx.x, xr = c >> 3, sp = xr & 3, kvh = xr >> 2, b = (c - kvh) & 7;
  {
    u64 pc;
    asm volatile("s_getpc_b64 %0" : "=s"(pc));
    const v4u rc = make_srd((const void*)(pc & ~255ull), CODEB);
    DMA_LOOP("", rc, ((xr * 2 + w) & 31) * 1024 + l * 16, 0, lbase + L_NIL, (12u >> w) & 1u);
  }
  const int g = l >> 4;
  const int oh = (c & 7) * 4 + (xr >> 3), on = xr & 7;
  const bool ffn2 = xr >= 24 && (c & 7) < 7;
  const bool isq = xr < 16, side = xr < 23 || ffn2;
  const int tl = isq ? ((xr >> 1) * 8 + ((c + xr) & 7)) * 2 + (xr & 1) : (c & 7) * 7 + xr - 16;
  const int cb = ffn2 ? on : tl >> 1, kh = ffn2 ? c & 7 : tl & 1;
#define KP(v, i) ((u64)v[i] | ((u64)v[(i) + 1] << 32))
  if (w == 1) *(LAS v4u*)(L + L_SYNC + l * 16) = v4u{0u, 0u, 0u, 0u};
  if (__builtin_expect(w == 0, 1)) {
    const unsigned ldb = isq ? 8192u : ffn2 ? 1024u : (unsigned)FF * 2u;
    unsigned woff = (unsigned)kh * 256u * ldb + cb * 128, wlen = 255u * ldb + 128u, wstep = 8u * ldb, wvo = (unsigned)(l >> 3) * ldb + (unsigned)(l & 7) * 16u;
    unsigned nw = side ? 32 : 0;
    asm volatile("s_waitcnt lgkmcnt(0)" : "+s"(ka0), "+s"(woff), "+s"(wlen), "+s"(wstep), "+s"(nw), "+v"(wvo) :: "memory");
    u64 pq = KP(ka0, 2), p1 = KP(ka0, 6), p2 = KP(ka0, 10);
    asm volatile("" : "+s"(pq), "+s"(p1), "+s"(p2));
    DMA_LOOP("nt", make_srd((const unsigned char*)(isq ? pq : ffn2 ? p2 : p1) + woff, wlen), wvo, wstep, lbase + L_WT, nw);
    DMA_LOOP("", make_srd((const void*)KP(ka0, 0), 8192), l * 16, 1024, lbase + L_X, nw >> 2);
  }
  asm volatile("s_waitcnt lgkmcnt(0)" : "+s"(t_in64), "+s"(ka0), "+s"(ka1) :: "memory");
  u64 tag = 0;
  if (w != 0) tag = read_tag((const unsigned char*)KP(ka1, 4));
  WGBAR();
  const unsigned t_in = (unsigned)t_in64;
  Args a;
  a.x = (const unsigned short*)KP(ka0, 0); a.wq = (const unsigned short*)KP(ka0, 2); a.wo = (const unsigned short*)KP(ka0, 4); a.w1 = (const unsigned short*)KP(ka0, 6);
  a.b1 = (const unsigned short*)KP(ka0, 8); a.w2 = (const unsigned short*)KP(ka0, 10); a.b2 = (const unsigned short*)KP(ka0, 12); a.gw = (const unsigned short*)KP(ka0, 14);
  a.kc = (const unsigned char*)KP(ka1, 0); a.vc = (const unsigned char*)KP(ka1, 2); a.ws = (unsigned char*)KP(ka1, 4); a.out = (unsigned short*)KP(ka1, 6);
  const __amdgpu_buffer_rsrc_t rws = __builtin_amdgcn_make_buffer_rsrc((void*)a.ws, 0, WS_END, 0x00020000);
  const int rb = c & 7, rc = c >> 3;

  if (w == 0) {
    if (side) {
      const unsigned tg = (unsigned)read_tag(a.ws);
      float og = 0.f;
      if (ffn2) {
        const unsigned bb0 = *(const GAS unsigned*)(a.b1 + kh * 256 + l * 2), bb1 = *(const GAS unsigned*)(a.b1 + kh * 256 + 128 + l * 2);
        const v4u gv = *(const GAS v4u*)(a.gw + l * 8);
        DRAIN();
        const unsigned t_w = now_ticks();
        float gt = 0.f;
#pragma nounroll
        for (int j = 0; j < 8; ++j) { const float t = gate_of(*(const LAS v4u*)(L + L_X + j * 1024 + l * 16), gv); gt = (l & 7) == j ? t : gt; }
        if (l < 8) *(LAS float*)(L + L_G + l * 4) = gt;
        og = rbf(1.f - gt);
        WAIT_TICK(t_in, TH);
        WAIT_TICK(t_w, 250);
        int spin = 0;
#pragma nounroll
        for (int hf = 0; hf < 2;) {
          v4u t[16];
#pragma unroll
          for (int i = 0; i < 16; ++i) { t[i] = __builtin_amdgcn_raw_buffer_load_b128(rws, WS_HP + kh * 32768 + hf * 16384 + l * 16, i * 1024, AUX_E); SLEEP(3); }
          int miss = -1;
#pragma unroll
          for (int j = 0; j < 8; ++j) {
            const int i0 = (j >> 1) * 4 + (j & 1);
            const unsigned bb = (j & 1) ? bb1 : bb0;
            miss = gbad(t[i0], tg) ? i0 : gbad(t[i0 + 2], tg) ? i0 + 2 : miss;
            const f32x2 z = gval(t[i0]) + gval(t[i0 + 2]);
            const float h0 = rbf(rbf(z.x) + bf_lo(bb)), h1 = rbf(rbf(z.y) + bf_hi(bb));
            const float r0 = h0 > 0.f ? h0 : 0.f, r1 = h1 > 0.f ? h1 : 0.f;
            *(LAS unsigned*)(L + L_H + (hf * 4 + (j >> 1)) * 1024 + (j & 1) * 256 + l * 4) = cvtpk(r0 * r0, r1 * r1);
          }
          if (__builtin_amdgcn_ballot_w64(miss >= 0) == 0 || spin >= SPIN_CAP) ++hf;
          else watch(rws, miss < 0 ? -1 : WS_HP + kh * 32768 + hf * 16384 + l * 16 + miss * 1024, tg, spin);
        }
        LDSWAIT();
        if (l == 0) LDS_SET(L_HRDY);
      }
      DRAIN();
      v4u xb[8];
      const LAS unsigned char* xp = L + (ffn2 ? L_H : L_X + kh * 512) + (l & 7) * 1024 + g * 16;
#pragma unroll
      for (int k = 0; k < 8; ++k) xb[k] = *(const LAS v4u*)(xp + k * 64);
      const int off = (isq ? WS_QP + (cb >> 3) * 65536 + (l & 7) * 8192 + kh * 4096 + (cb & 7) * 512 : WS_HP + (cb >> 2) * 32768 + (l & 7) * 4096 + kh * 2048 + (cb & 3) * 512)
                      + g * 32 + (l & 8) * 2;
#pragma nounroll
      for (int ct = 0; ct < 4; ++ct) {
        f32x4 d = {0.f, 0.f, 0.f, 0.f};
#pragma unroll
        for (int k = 0; k < 8; ++k) d = mfma32(tile_frag(L + L_WT, l, k, ct), xb[k], d);
        if (ffn2) *(LAS f32x4*)(L + L_FF + ct * 1024 + l * 16) = d * og;
        else __builtin_amdgcn_raw_buffer_store_b128(gran_d(d, l, tg), rws, off + ct * 128, 0, AUX_E);
      }
      DRAIN();
      if (l == 0) LDS_SET(L_HRDY);
      LDSWAIT();
    }
    return;
  }
  if (w == 1) {
    const v4u xv = *(const GAS v4u*)(a.x + rb * DM + l * 8), gv = *(const GAS v4u*)(a.gw + l * 8);
    const v2u bb = *(const GAS v2u*)(a.b2 + rc * 16 + 4 * (l & 3));
    const unsigned tg = (unsigned)tag, th = (unsigned)(tag >> 32);
    {
      const float gt = gate_of(xv, gv), og = rbf(1.f - gt);
      if (l < 4) *(LAS f32x4*)(L + L_FIN + l * 16) = f32x4{bf_lo(bb.x), bf_hi(bb.x), bf_lo(bb.y), bf_hi(bb.y)} * og;
      if (l == 0) *(LAS float*)(L + L_FIN + 64) = gt;
    }
    lds_wait<4>(L + L_ORG + 4);
    const unsigned org = *(volatile const LAS unsigned*)(L + L_ORG);
    if (l < 8) __builtin_amdgcn_raw_buffer_store_b128(v4u{org, tg, org, tg}, rws, WS_X + c * 128 + l * 16, 0, AUX_E);
    { unsigned tq = __builtin_amdgcn_readfirstlane(org) + (TQ - T0) * 16; WAIT_NEXT(tq, 0); }
    v2u st;
#pragma nounroll
    for (int spin = 0; spin < SPIN_CAP;) {
      v4u t[8];
#pragma unroll
      for (int i = 0; i < 8; ++i) { t[i] = __builtin_amdgcn_raw_buffer_load_b128(rws, WS_QP + kvh * 65536 + b * 8192 + l * 16, i * 1024, AUX_E); SLEEP(6); }
      st = __builtin_amdgcn_raw_buffer_load_b64(rws, WS_X + ((l & 7) + 16 + ((l >> 3) & 3) * 64) * 128, 0, AUX_SC1);
      int miss = -1;
#pragma unroll
      for (int j = 0; j < 4; ++j) {
        miss = gbad(t[j], tg) ? j : gbad(t[4 + j], tg) ? 4 + j : miss;
        const f32x2 q = gval(t[j]) + gval(t[4 + j]);
        *(LAS unsigned*)(L + L_Q + j * 256 + l * 4) = cvtpk(q.x, q.y);
      }
      if (__builtin_amdgcn_ballot_w64(miss >= 0) == 0) break;
      watch(rws, miss < 0 ? -1 : WS_QP + kvh * 65536 + b * 8192 + l * 16 + miss * 1024, tg, spin);
    }
    {
      unsigned rel = (st.y == tg ? st.x : (now_ticks() << 4) - ((J1 + 1) * DT0 + 2400)) - org;
      rel += dppu<0xB1>(rel); rel += dppu<0x4E>(rel); rel += dppu<0x141>(rel); rel += dppu<0x140>(rel);
      const int sum = __builtin_amdgcn_readlane((int)rel, 0) + __builtin_amdgcn_readlane((int)rel, 16);
      if (l == 0) *(LAS unsigned*)(L + L_TEND) = org + (sum >> 5) + (J1 + 1) * DT0 + (31 - J1) * DT + 32 * DTV;
    }
    LDSWAIT();
    if (l == 0) LDS_SET(L_QRDY);
    if (side) lds_wait<8>(L + L_HRDY);
    DRAIN();
    if (l == 0) __builtin_amdgcn_raw_buffer_store_b64(v2u{tg, th}, rws, WS_F + c * 8, 0, AUX_SC1);
    lds_wait<4>(L + L_ML + (l & 31) * 16 + 8);
    const v2u ml = *(volatile const LAS v2u*)(L + L_ML + (l & 31) * 16);
    const float mw = __uint_as_float(ml.x);
    float M = fmaxf(mw, dpp<0xB1>(mw)); M = fmaxf(M, dpp<0x4E>(M)); M = fmaxf(M, dpp<0x141>(M));
    float Ls = __builtin_amdgcn_exp2f(mw - M) * __uint_as_float(ml.y);
    Ls += dpp<0xB1>(Ls); Ls += dpp<0x4E>(Ls); Ls += dpp<0x141>(Ls);
    const float lse = M + __builtin_amdgcn_logf(Ls);
    const v4u lg = gran2(lse, dpp<0x128>(lse), tg);
    if (l < 32 && (l & 15) == 0) ST_SC1(lg, WS_LSE + ((b * 8 + kvh) * 4 + sp) * 128 + l);
    v2u ls;
    SLEEP(16);
#pragma nounroll
    for (int spin = 0; spin < SPIN_CAP; ++spin) {
      ls = __builtin_amdgcn_raw_buffer_load_b64(rws, WS_LSE + ((b * 8 + kvh) * 4 + (l & 3)) * 128 + ((l >> 3) & 3) * 8, 0, AUX_SC1);
      if (__builtin_amdgcn_ballot_w64(ls.y != tg) == 0) break;
      SLEEP(8);
    }
    const float lf = __uint_as_float(ls.x);
    float M2 = fmaxf(lf, dpp<0xB1>(lf)); M2 = fmaxf(M2, dpp<0x4E>(M2));
    float E = __builtin_amdgcn_exp2f(lf - M2);
    E += dpp<0xB1>(E); E += dpp<0x4E>(E);
    if (l < 32) *(LAS float*)(L + L_CF + l * 4) = __builtin_amdgcn_exp2f(mw - M2) * __builtin_amdgcn_rcpf(E);
    WAIT_TICK(t_in, TF);
#pragma nounroll
    for (int spin = 0; spin < SPIN_CAP; ++spin) {
      const v4u f0 = LD_SC1(WS_F + l * 16), f1 = LD_SC1(WS_F + 1024 + l * 16);
      if (__builtin_amdgcn_ballot_w64(((f0.x ^ tg) | (f0.y ^ th) | (f0.z ^ tg) | (f0.w ^ th) | (f1.x ^ tg) | (f1.y ^ th) | (f1.z ^ tg) | (f1.w ^ th)) != 0) == 0) break;
      SLEEP(16);
    }
    LDSWAIT();
    lds_wait<1>(L + L_DONE + (l & 7) * 4);
    SLEEP(3);
    DMA_LOOP("nt", make_srd(a.wo + oh * 128 * DM + on * 64, 127 * 1024 + 128), (unsigned)(l >> 3) * 1024u + (unsigned)(l & 7) * 16u, 8192, lbase + L_WO, 16);
    WGBAR();
    DRAIN();
    if (l == 0) LDS_SET(L_WRDY);
    LDSWAIT();
    return;
  }

  const int sw = w - 2;
  const unsigned stag = sw * (DT / 8);
  unsigned tnext = ((t_in + T0) << 4) + stag, dt = DT0, tend = 0;
#define PACE() do { asm volatile("s_waitcnt vmcnt(%0)" :: "n"(WIN - 1) : "memory"); WAIT_NEXT(tnext, dt); } while (0)
#define RESLIP() do { const unsigned t1_ = (now_ticks() - SLIP) << 4; if ((int)(t1_ - tnext) > 0) tnext = t1_ + (stag >> 1); } while (0)
#define REPLAN(done, nominal) do { RESLIP(); \
    const int ex_ = (int)(tnext + ((done) < 32 ? (32 - (done)) * DT + 32 * DTV : (64 - (done)) * DTV) - stag - tend); \
    dt = (nominal) - sclamp<-DN, UP>((ex_ * (65536 / (64 - (done)))) >> 16); } while (0)
  const unsigned x0 = (((sw & 1) * 136) ^ (((sw >> 1) & 1) * 132) ^ ((sw >> 2) * 256) ^ ((sp & 1) * 4) ^ ((sp >> 1) * 88) ^ ((kvh & 1) * 48) ^
                       (((kvh >> 1) & 1) * 32) ^ ((kvh >> 2) * 2880) ^ ((b & 1) * 3624) ^ (((b >> 1) & 1) * 2516)) << 8;
#define OFF(i) (x0 ^ (((((i) & 1) ? 96u : 0u) ^ (((i) & 2) ? 68u : 0u) ^ (((i) & 4) ? 2048u : 0u) ^ (((i) & 8) ? 1280u : 0u) ^ (((i) & 16) ? 2560u : 0u)) << 8))
  const size_t base = (size_t)(b * HKV + kvh) * SK * 256;
  const v4u rk = make_srd(a.kc + base, SK * 256), rv = make_srd(a.vc + base, SK * 256);
  const int ko = ((l >> 2) & 3) * 256 + g * 64 + (l & 3) * 16, vo = l * 16;
#define LOAD_V(i) asm volatile("buffer_load_dwordx4 %0, %1, %2, %3 offen nt" : "+v"(kv[i]) : "v"(vo), "s"(rv), "s"(OFF(i)) : "memory")
  const unsigned tg = (unsigned)tag;
  { const unsigned t1 = ((now_ticks() - SLIP0) << 4) + stag; if ((int)(t1 - tnext) > 0) tnext = t1; }
  v4u kv[32];
  __builtin_amdgcn_sched_barrier(0);
#pragma unroll
  for (int i = 0; i < 32; ++i) {
    PACE();
    asm volatile("buffer_load_dwordx4 %0, %1, %2, %3 offen nt" : "=v"(kv[i]) : "v"(ko), "s"(rk), "s"(OFF(i)) : "memory");
    if (i == WIN) RESLIP();
    if (i == J1) {
      RESLIP();
      const unsigned org = tnext - stag - (J1 + 1) * DT0;
      if (sw == 3 && l == 0) { *(volatile LAS unsigned*)(L + L_ORG) = org; LDS_SET(L_ORG + 4); }
      dt = DT;
    }
    if (i == JA) {
      const unsigned f = __builtin_amdgcn_readfirstlane(*(volatile const LAS unsigned*)(L + L_QRDY));
      tend = __builtin_amdgcn_readfirstlane(*(volatile const LAS unsigned*)(L + L_TEND));
      if (f) REPLAN(JA + 1, DT);
    }
    __builtin_amdgcn_sched_barrier(0);
  }
  lds_wait<2>(L + L_QRDY);
  const v4u qf = *(const LAS v4u*)(L + L_Q + ((l >> 2) & 3) * 256 + g * 64 + (l & 3) * 16);
  tend = __builtin_amdgcn_readfirstlane(*(const LAS unsigned*)(L + L_TEND));
  REPLAN(32, DTV);
  __builtin_amdgcn_sched_barrier(0);
  constexpr float C2 = 0.088388347648318440f * 1.4426950408889634f;
  float sc[8];
  const int s = l & 3;
#pragma unroll
  for (int m = 0; m < 8; ++m) {
    asm volatile("s_waitcnt vmcnt(%4)" : "+v"(kv[4 * m]), "+v"(kv[4 * m + 1]), "+v"(kv[4 * m + 2]), "+v"(kv[4 * m + 3]) : "n"(28 - 4 * m + m) : "memory");
    f32x4 d[4];
#pragma unroll
    for (int r = 0; r < 4; ++r) d[r] = mfma32(kv[4 * m + r], qf, f32x4{0.f, 0.f, 0.f, 0.f});
#pragma unroll
    for (int r = 0; r < 4; ++r) {
      float t = dpp<0x00>(d[r][0]); t = dpp<0x55>(d[r][1]) + t; t = dpp<0xAA>(d[r][2]) + t; t = dpp<0xFF>(d[r][3]) + t;
      asm volatile("" : "+v"(t));
      sc[m] = (r == 0 || s == r) ? t : sc[m];
    }
    asm volatile("" : "+v"(sc[m]));
    __builtin_amdgcn_sched_barrier(0);
    PACE();
    LOAD_V(m);
    __builtin_amdgcn_sched_barrier(0);
  }
  float mx = sc[0];
#pragma unroll
  for (int m = 1; m < 8; ++m) mx = fmaxf(mx, sc[m]);
  mx = fmaxf(mx, dpp<0xB1>(mx)); mx = fmaxf(mx, dpp<0x4E>(mx)); mx = xrow_max(mx);
  const float mneg = -mx * C2;
  float lsum = 0.f;
  unsigned pk[8];
#pragma unroll
  for (int m = 0; m < 8; ++m) { const float p = __builtin_amdgcn_exp2f(fmaf(sc[m], C2, mneg)); pk[m] = cvtpk(p, p); lsum += bf_lo(pk[m]); }
  lsum += dpp<0xB1>(lsum); lsum += dpp<0x4E>(lsum); lsum = xrow_sum(lsum);
  if (l < 16 && (l & 3) == 0) {
    *(volatile LAS v2u*)(L + L_ML + ((l >> 2) * 8 + sw) * 16) = v2u{__float_as_uint(mx * C2), __float_as_uint(lsum)};
    LDS_SET(L_ML + ((l >> 2) * 8 + sw) * 16 + 8);
  }
  const unsigned half = (l & 1) ? 0xffff0000u : 0x0000ffffu;
  const unsigned m0 = (l & 2) ? 0u : half, m1 = (l & 2) ? half : 0u;
  f32x4 o0 = {0.f, 0.f, 0.f, 0.f}, o1 = {0.f, 0.f, 0.f, 0.f};
  const int s4 = sw & 3, pb8 = l & 7;
  unsigned a_mw, a_mr, a_g0, a_l0, a_l1, a_g1;
#pragma unroll
  for (int j = 8; j < 40; ++j) {
    if (j < 32) { PACE(); LOAD_V(j); if (j == 15) REPLAN(48, DTV); __builtin_amdgcn_sched_barrier(0); }
    if (j == 31) { if (l == 0) LDS_SET(L_DONE + sw * 4); }
    if (j == 33) {
      a_mw = L_MRG + (sw * 4 + g) * 512 + (l & 15) * 32;
      a_mr = L_MRG + s4 * 512 + l * 8;
      if (sw < 4) {
        a_g0 = (l & 3) == 0 ? WS_P + (((kvh * 4 + s4) * 8 + b) * 4 + sp) * 256 + l * 4 : WS_END;
        a_l0 = L_WO + (8 * g + ((l & 15) >> 2)) * 128 + (l & 3) * 8 + s4 * 32;
        a_l1 = L_PG + (pb8 * 4 + ((l >> 3) & 1)) * 272 + g * 16;
        a_g1 = (l & 8) == 0 ? WS_O + (((on * 4 + s4) * 4 + (pb8 >> 1)) * 32 + oh) * 128 + (pb8 & 1) * 64 + g * 16 : WS_END;
      } else {
        a_g0 = WS_P + (oh * 8 + 2 * s4) * 1024 + l * 16;
        a_l0 = L_PG + (8 * s4 + g) * 272 + (l & 15) * 16;
        a_l1 = (l & 15) < 4 ? (rb * DM + rc * 16 + 4 * (l & 3) + g) * 2 : 8192;
        a_g1 = WS_O + (c >> 1) * 4096 + (l >> 2) * 128 + (c & 1) * 64 + (l & 3) * 16;
      }
      asm volatile("" : "+v"(a_mw), "+v"(a_mr), "+v"(a_g0), "+v"(a_l0), "+v"(a_l1), "+v"(a_g1));
      __builtin_amdgcn_sched_barrier(0);
    }
    const int i = j - 8;
    const unsigned pb = (i & 3) == 0 ? dppu<0x00>(pk[i >> 2]) : (i & 3) == 1 ? dppu<0x55>(pk[i >> 2]) : (i & 3) == 2 ? dppu<0xAA>(pk[i >> 2]) : dppu<0xFF>(pk[i >> 2]);
    const v2u av = {pb & m0, pb & m1};
    asm volatile("s_waitcnt vmcnt(%1)" : "+v"(kv[i]) : "n"((j < 32 ? j : 31) - i) : "memory");
    o0 = mfma16(av, v2u{kv[i].x, kv[i].y}, o0);
    o1 = mfma16(av, v2u{kv[i].z, kv[i].w}, o1);
    __builtin_amdgcn_sched_barrier(0);
  }
  *(LAS f32x4*)(L + a_mw) = o0;
  *(LAS f32x4*)(L + a_mw + 16) = o1;
  if (sw < 4) {
    asm volatile("s_waitcnt lgkmcnt(0)\n\ts_barrier ; merge" ::: "memory");
    const f32x4 c0 = *(const LAS f32x4*)(L + L_CF + sw * 32), c1 = *(const LAS f32x4*)(L + L_CF + sw * 32 + 16);
    f32x2 ac0 = {0.f, 0.f}, ac1 = {0.f, 0.f};
#pragma unroll
    for (int j = 0; j < 4; ++j) {
      ac0 += *(const LAS f32x2*)(L + a_mr + j * 2048) * c0[j];
      ac1 += *(const LAS f32x2*)(L + a_mr + (4 + j) * 2048) * c1[j];
    }
    ac0 += ac1;
    const unsigned pv = cvtpk(ac0.x, ac0.y);
    ST_SC1((v4u{pv, dppu<0x55>(pv), dppu<0xAA>(pv), dppu<0xFF>(pv)}), a_g0);
    f32x4 ff = {0.f, 0.f, 0.f, 0.f};
    float gm = 1.f;
    if (ffn2) { ff = *(const LAS f32x4*)(L + L_FF + sw * 1024 + l * 16); gm = *(const LAS float*)(L + L_G + pb8 * 4); }
    lds_wait<0>(L + L_WRDY);
    v4u wf[4];
#pragma unroll
    for (int k = 0; k < 4; ++k) { const v2u t0 = tr_read(L + a_l0 + k * 4096), t1 = tr_read(L + a_l0 + k * 4096 + 512); wf[k] = v4u{t0.x, t0.y, t1.x, t1.y}; }
    asm volatile("" : "+v"(wf[0]), "+v"(wf[1]), "+v"(wf[2]), "+v"(wf[3]), "+v"(ff), "+v"(gm));
    WGBAR();
    v4u pa[8];
#pragma unroll
    for (int k = 0; k < 8; ++k) pa[k] = *(const LAS v4u*)(L + a_l1 + (k & 1) * 544 + (k >> 1) * 64);
    __builtin_amdgcn_sched_barrier(0);
    f32x4 da = {0.f, 0.f, 0.f, 0.f};
#pragma unroll
    for (int k = 0; k < 8; ++k) da = mfma32(wf[k >> 1], pa[k], da);
    v4u r;
#pragma unroll
    for (int j = 0; j < 4; ++j) r[j] = __float_as_uint(fmaf(da[j] + dpp<0x128>(da[j]), gm, ff[j]));
    r.y &= ~1u; r.w &= ~1u;
    ST_SC1(r, a_g1);
  } else {
    v4u p0, p1;
    asm volatile("s_waitcnt lgkmcnt(0)\n\ts_barrier\n\ts_sleep 18" ::: "memory");
    poll2(rws, a_g0, a_g0 + 1024, tg, p0, p1);
    *(LAS v4u*)(L + a_l0) = p0;
    *(LAS v4u*)(L + a_l0 + 4 * 272) = p1;
    LDSWAIT();
    WGBAR();
    if (sw == 4) {
      float fin = *(const LAS float*)(L + L_FIN + (l & 3) * 16 + g * 4);
      const float gt = *(const LAS float*)(L + L_FIN + 64);
      float g0 = (l & 12) == 12 ? 1.f : gt, g1 = l < 48 ? g0 : gt;
      const __amdgpu_buffer_rsrc_t rout = __builtin_amdgcn_make_buffer_rsrc((void*)a.out, 0, 8192, 0x00020000);
      asm volatile("s_sleep 17" : "+v"(fin), "+v"(g0), "+v"(g1) :: "memory");
      v4u r0, r1;
      poll2(rws, a_g1, a_g1 + 2048, tg, r0, r1);
      const f32x4 av = __builtin_bit_cast(f32x4, r0) * g0 + __builtin_bit_cast(f32x4, r1) * g1;
      auto s0 = __builtin_amdgcn_permlane16_swap(__float_as_uint(av[0]), __float_as_uint(av[1]), false, false);
      auto s1 = __builtin_amdgcn_permlane16_swap(__float_as_uint(av[2]), __float_as_uint(av[3]), false, false);
      const float u0 = __uint_as_float(s0[0]) + __uint_as_float(s0[1]), u1 = __uint_as_float(s1[0]) + __uint_as_float(s1[1]);
      auto s2 = __builtin_amdgcn_permlane32_swap(__float_as_uint(u0), __float_as_uint(u1), false, false);
      float v = __uint_as_float(s2[0]) + __uint_as_float(s2[1]);
      v += dpp<0x124>(v); v += dpp<0x128>(v);
      v += fin;
      __builtin_amdgcn_raw_buffer_store_b16((unsigned short)cvtpk(v, v), rout, a_l1, 0, 0);
      if (c == 0 && l == 0) __builtin_amdgcn_raw_buffer_store_b64(v2u{tg, (unsigned)(tag >> 32)}, rws, WS_E, 0, AUX_SC1);
    }
  }
}

extern "C" void kernel_launch(void* const* d_in, const int* in_sizes, int n_in, void* d_out, int out_size, void* d_ws, size_t ws_size,
                              hipStream_t stream) {
  Args a;
  a.x = (const unsigned short*)d_in[0];
  a.kc = (const unsigned char*)d_in[1];
  a.vc = (const unsigned char*)d_in[2];
  a.wq = (const unsigned short*)d_in[3];
  a.wo = (const unsigned short*)d_in[4];
  a.w1 = (const unsigned short*)d_in[6];
  a.b1 = (const unsigned short*)d_in[7];
  a.w2 = (const unsigned short*)d_in[8];
  a.b2 = (const unsigned short*)d_in[9];
  a.gw = (const unsigned short*)d_in[10];
  a.ws = (unsigned char*)d_ws;
  a.out = (unsigned short*)d_out;
  fused_decode<<<256, NWAVE * 64, 0, stream>>>(a);
}
```

```cpp
#define PROBE 30
#define PW0 0
#define PW1 1
#include <hip/hip_runtime.h>
#include <hip/hip_bf16.h>
#include <stdint.h>

typedef unsigned v4u __attribute__((ext_vector_type(4)));
typedef unsigned v2u __attribute__((ext_vector_type(2)));
typedef float f32x4 __attribute__((ext_vector_type(4)));
typedef float f32x2 __attribute__((ext_vector_type(2)));
typedef __bf16 bf16x8 __attribute__((ext_vector_type(8)));
typedef __bf16 bf16x2 __attribute__((ext_vector_type(2)));
typedef short s16x4 __attribute__((ext_vector_type(4)));
typedef unsigned long long u64;
#define GAS __attribute__((address_space(1)))
#define LAS __attribute__((address_space(3)))

constexpr int DM = 512, HKV = 8, SK = 4096, FF = 1792;
constexpr int AUX_SC1 = 16;
#define DT 452
#define DTV 439
#define T0 115
#define WIN 4
#define SLIP 50
#define UP1 0
#define UP2 40
#define DN2 40
#define JA 24
#define TQ 560
#define TH 480
#define TF 1500
#define GQ 6
#define GH 3
#define WOS 4
#define PDELAY1 18
#define PDELAY2 17
#define CODEB 17408
#define AUXE 18
constexpr int AUX_E = AUXE;
#define EBAR 1
#define EBAR0 ""
#define SLIP0 30
#define RELQ 1
#define SQZ 0
#define DT2 DT
#define DT0 485
#define J1 8
#define WSP 0
#define OAUX 0
#define LNT 0
#define LSL 32
#define SAMP ((l & 7) + 16 + ((l >> 3) & 3) * 64)
#define PD 1
#define PGAP 5
#define TOUCH 0
#define XAFT 0
#define MAP 4
#define E0 96
#define E1 68
#define E2 2048
#define E3 1280
#define E4 2560
#define TV0 1
#define TV1 2
#define XF (((sw&1)*136)^(((sw>>1)&1)*132)^((sw>>2)*256)^((sp&1)*4)^((sp>>1)*88)^((kvh&1)*48)^(((kvh>>1)&1)*32)^((kvh>>2)*2880)^((b&1)*3624)^(((b>>1)&1)*2516))
#define ROTF (16 * (sw & 1) + 8 * ((sw >> 1) & 1) + 4 * (sw >> 2) + 9 * (sp & 1) + 28 * (sp >> 1) + 30 * (b & 1))
#define BLKF (4 * (sw & 1) + (sw & 2) + (sw >> 2))
constexpr int NWAVE = 10;

constexpr int WS_E = 0;
constexpr int WS_F = 2048;
constexpr int WS_QP = 4096;
constexpr int WS_HP = WS_QP + 8 * 8 * 2 * 512 * 8;
constexpr int WS_X = WS_HP + 7 * 8 * 2 * 256 * 8;
constexpr int WS_LSE = WS_X + 256 * 128;
constexpr int WS_END = WS_LSE + 256 * 128;
constexpr int WS_O = WS_QP;
constexpr int WS_P = WS_HP;
static_assert(WS_LSE - WS_P == 32 * 8 * 4 * 256, "the partials fill exactly the lines of HP and X");
constexpr int WS_ST = 8 << 20;
constexpr int L_WO = 0, L_WT = 16384, L_MRG = 49152, L_Q = L_MRG + 16384, L_X = L_Q + 1024, L_H = L_X + 8192, L_NIL = L_H + 8192, L_FF = L_NIL + 1024,
              L_CF = L_FF + 4096, L_G = L_CF + 128, L_FIN = L_G + 32,
              L_SYNC = L_FIN + 96,
              L_ML = L_SYNC, L_QRDY = L_ML + 512, L_TEND = L_QRDY + 4, L_HRDY = L_QRDY + 8, L_WRDY = L_QRDY + 12, L_ORG = L_QRDY + 16, L_DONE = L_QRDY + 32,
              L_END = L_SYNC + 1024;
constexpr int L_PG = L_WT;

__device__ __forceinline__ f32x4 mfma32(v4u a, v4u b, f32x4 c) {
  return __builtin_amdgcn_mfma_f32_16x16x32_bf16(__builtin_bit_cast(bf16x8, a), __builtin_bit_cast(bf16x8, b), c, 0, 0, 0);
}
__device__ __forceinline__ f32x4 mfma16(v2u a, v2u b, f32x4 c) {
  return __builtin_amdgcn_mfma_f32_16x16x16bf16_1k(__builtin_bit_cast(s16x4, a), __builtin_bit_cast(s16x4, b), c, 0, 0, 0);
}
__device__ __forceinline__ unsigned cvtpk(float lo, float hi) { bf16x2 r = {(__bf16)lo, (__bf16)hi}; return __builtin_bit_cast(unsigned, r); }
__device__ __forceinline__ float bf_lo(unsigned u) { return __builtin_bit_cast(float, u << 16); }
__device__ __forceinline__ float bf_hi(unsigned u) { return __builtin_bit_cast(float, u & 0xffff0000u); }
__device__ __forceinline__ float rbf(float x) { return (float)(__bf16)x; }
template <int CTRL> __device__ __forceinline__ float dpp(float x) { return __builtin_bit_cast(float, __builtin_amdgcn_mov_dpp(__builtin_bit_cast(int, x), CTRL, 0xf, 0xf, true)); }
template <int CTRL> __device__ __forceinline__ unsigned dppu(unsigned x) { return (unsigned)__builtin_amdgcn_mov_dpp((int)x, CTRL, 0xf, 0xf, true); }
__device__ __forceinline__ float xrow_max(float x) {
  auto s = __builtin_amdgcn_permlane16_swap(__float_as_uint(x), __float_as_uint(x), false, false);
  x = fmaxf(__uint_as_float(s[0]), __uint_as_float(s[1]));
  auto t = __builtin_amdgcn_permlane32_swap(__float_as_uint(x), __float_as_uint(x), false, false);
  return fmaxf(__uint_as_float(t[0]), __uint_as_float(t[1]));
}
__device__ __forceinline__ float xrow_sum(float x) {
  auto s = __builtin_amdgcn_permlane16_swap(__float_as_uint(x), __float_as_uint(x), false, false);
  x = __uint_as_float(s[0]) + __uint_as_float(s[1]);
  auto t = __builtin_amdgcn_permlane32_swap(__float_as_uint(x), __float_as_uint(x), false, false);
  return __uint_as_float(t[0]) + __uint_as_float(t[1]);
}
__device__ __forceinline__ v2u tr_read(const LAS unsigned char* p) { return __builtin_bit_cast(v2u, __builtin_amdgcn_ds_read_tr16_b64_v4i16((LAS s16x4*)p)); }
__device__ __forceinline__ v4u make_srd(const void* p, unsigned bytes) {
  const u64 a = (u64)p;
  return v4u{(unsigned)a, (unsigned)(a >> 32) & 0xffffu, bytes, 0x00020000u};
}
#define DMA_LOOP(POL, srd, voff, step, lds_addr, n) do { unsigned keep_, so_ = 0, n_ = (n); \
  asm volatile("s_cmp_eq_u32 %2, 0\n\ts_cbranch_scc1 2f\n\ts_mov_b32 %0, m0\n\ts_mov_b32 m0, %3\n\ts_nop 0\n1:\n\tbuffer_load_dwordx4 %4, %5, %1 offen " POL " lds\n\t" \
               "s_add_u32 m0, m0, 0x400\n\ts_add_u32 %1, %1, %6\n\ts_sub_u32 %2, %2, 1\n\ts_cmp_lg_u32 %2, 0\n\ts_cbranch_scc1 1b\n\ts_mov_b32 m0, %0\n2:" \
               : "=&s"(keep_), "+&s"(so_), "+&s"(n_) : "s"(lds_addr), "v"(voff), "s"(srd), "s"(step) : "scc", "memory"); } while (0)
__device__ __forceinline__ v4u tile_frag(const LAS unsigned char* tile, int l, int k, int ct) {
  const LAS unsigned char* p = tile + (8 * (l >> 4) + ((l & 15) >> 2)) * 128 + (l & 3) * 8 + k * 4096 + ct * 32;
  const v2u t0 = tr_read(p), t1 = tr_read(p + 512);
  return v4u{t0.x, t0.y, t1.x, t1.y};
}
#define DRAIN() asm volatile("s_waitcnt vmcnt(0)" ::: "memory")
#define LDSWAIT() asm volatile("s_waitcnt lgkmcnt(0)" ::: "memory")
#define WGBAR() do { __builtin_amdgcn_sched_barrier(0); __builtin_amdgcn_s_barrier(); __builtin_amdgcn_sched_barrier(0); asm volatile("" ::: "memory"); } while (0)
__device__ __forceinline__ u64 read_tag(const unsigned char* ws) {
  u64 t;
  asm volatile("s_load_dwordx2 %0, %1, 0x0\n\ts_waitcnt lgkmcnt(0)" : "=s"(t) : "s"(ws) : "memory");
  const unsigned old = (unsigned)t;
  t += 0x9E3779B97F4A7C15ull;
  t = (t ^ (t >> 30)) * 0xBF58476D1CE4E5B9ull; t = (t ^ (t >> 27)) * 0x94D049BB133111EBull;
  t ^= t >> 31;
  t = (t | 0x7F81u) & ~0x40ull;
  return (unsigned)t == old ? t ^ 2u : t;
}
__device__ __forceinline__ unsigned now_ticks() {
  u64 t;
  asm volatile("s_memrealtime %0\n\ts_waitcnt lgkmcnt(0)" : "=s"(t) :: "memory");
  return (unsigned)t;
}
#define WAIT_TICK(base, delta) \
  asm volatile("s_branch 2f\n1:\n\ts_sleep 1\n2:\n\ts_memrealtime vcc\n\ts_waitcnt lgkmcnt(0)\n\ts_sub_u32 vcc_lo, vcc_lo, %0\n\ts_cmpk_lt_i32 vcc_lo, %1\n\ts_cbranch_scc1 1b" \
               :: "s"(base), "n"(delta) : "scc", "vcc", "memory")
#define WSL "s_branch 2f\n1:\n\ts_sleep 1\n2:\n\t"
#define WAIT_NEXT(acc, step) \
  asm volatile(WSL "s_memrealtime vcc\n\ts_waitcnt lgkmcnt(0)\n\ts_lshl_b32 vcc_lo, vcc_lo, 4\n\ts_sub_u32 vcc_lo, vcc_lo, %0\n\ts_cmp_lt_i32 vcc_lo, 0\n\ts_cbranch_scc1 1b\n\ts_add_u32 %0, %0, %1" \
               : "+s"(acc) : "s"(step) : "scc", "vcc", "memory")
template <int LO, int HI> __device__ __forceinline__ int sclamp(int x) {
  asm("s_max_i32 %0, %0, %1\n\ts_min_i32 %0, %0, %2" : "+s"(x) : "n"(LO), "n"(HI) : "scc");
  return x;
}
constexpr int SPIN_CAP = 1 << 18;
#define LD_SC1(off) __builtin_amdgcn_raw_buffer_load_b128(rws, (off), 0, AUX_SC1)
#define ST_SC1(v, off) __builtin_amdgcn_raw_buffer_store_b128((v), rws, (off), 0, AUX_SC1)
__device__ __forceinline__ v4u gran2(float a, float b, unsigned tg) { return v4u{__float_as_uint(a), tg, __float_as_uint(b), tg}; }
__device__ __forceinline__ unsigned gbad(v4u g, unsigned tg) { return (g.y ^ tg) | (g.w ^ tg); }
__device__ __forceinline__ f32x2 gval(v4u g) { return f32x2{__uint_as_float(g.x), __uint_as_float(g.z)}; }
__device__ __forceinline__ v4u gran_d(f32x4 d, int l, unsigned tg) { return gran2((l & 8) ? d[2] : d[0], (l & 8) ? d[3] : d[1], tg); }

#define PSTAMP(k) STAMP(k)
#define STAMP(k) do { if ((k) == PROBE && w >= PW0 && w < PW1) { const unsigned t_ = now_ticks() - t_in; if (l == 0) *(volatile LAS unsigned*)(L + L_END + w * 4) = t_; LDSWAIT(); } } while (0)
#define FLUSH() do { } while (0)

__device__ __forceinline__ void watch(__amdgpu_buffer_rsrc_t rws, int miss, unsigned tg, int& spin) {
#pragma nounroll
  while (miss >= 0 && spin < SPIN_CAP) {
    asm volatile("s_sleep 24" ::: "memory");
    if (gbad(__builtin_amdgcn_raw_buffer_load_b128(rws, miss, 0, AUX_E), tg) == 0) miss = -1;
    ++spin;
  }
  spin = __builtin_amdgcn_ballot_w64(spin >= SPIN_CAP) != 0 ? SPIN_CAP : __builtin_amdgcn_readfirstlane(spin) + 1;
}
template <int SLEEP> __device__ __forceinline__ void lds_wait(const LAS unsigned char* p) {
#pragma nounroll
  for (int spin = 0; spin < SPIN_CAP * 16; ++spin) {
    if (__builtin_amdgcn_ballot_w64(*(volatile const LAS unsigned*)p == 0u) == 0) break;
    if (SLEEP) asm volatile("s_sleep %0" :: "n"(SLEEP) : "memory");
  }
  asm volatile("" ::: "memory");
}
#define LDS_SET(off) (*(volatile LAS unsigned*)(L + (off)) = 1u)
__device__ __forceinline__ void poll2(__amdgpu_buffer_rsrc_t rws, int o0, int o1, unsigned tg, v4u& r0, v4u& r1) {
#pragma nounroll
  for (int spin = 0; spin < SPIN_CAP; ++spin) {
    r0 = LD_SC1(o0); r1 = LD_SC1(o1);
    if (__builtin_amdgcn_ballot_w64(min(min(r0.y ^ tg, r0.w ^ tg), min(r1.y ^ tg, r1.w ^ tg)) == 0u) == 0) break;
    asm volatile("" ::: "memory");
  }
}
__device__ __forceinline__ unsigned notag(unsigned x, unsigned tg) { return x == tg ? x ^ 1u : x; }
__device__ __forceinline__ float gate_of(v4u xv, v4u gv) {
  float gs = 0.f;
#pragma unroll
  for (int j = 0; j < 4; ++j) { gs += rbf(bf_lo(xv[j]) * bf_lo(gv[j])); gs += rbf(bf_hi(xv[j]) * bf_hi(gv[j])); }
  gs += dpp<0xB1>(gs); gs += dpp<0x4E>(gs); gs += dpp<0x124>(gs); gs += dpp<0x128>(gs); gs = xrow_sum(gs);
  return rbf(1.f / (1.f + __expf(-rbf(gs))));
}

struct Args {
  const unsigned short *x, *wq, *wo, *w1, *b1, *w2, *b2, *gw;
  const unsigned char *kc, *vc;
  unsigned char* ws;
  unsigned short* out;
};

typedef unsigned v16u __attribute__((ext_vector_type(16)));
typedef unsigned v8u __attribute__((ext_vector_type(8)));

__global__ void __launch_bounds__(NWAVE * 64, 3) fused_decode(Args a_) {
  v16u ka0; v8u ka1; u64 t_in64;
  asm volatile("s_load_dwordx16 %0, %3, 0x0\n\ts_load_dwordx8 %1, %3, 0x40\n\ts_memrealtime %2" : "=&s"(ka0), "=&s"(ka1), "=&s"(t_in64) : "s"(__builtin_amdgcn_kernarg_segment_ptr()));
  asm volatile("s_waitcnt lgkmcnt(0)" : "+s"(t_in64), "+s"(ka0), "+s"(ka1) :: "memory");
  const unsigned early = now_ticks();
  __shared__ __attribute__((aligned(16))) unsigned char smem[L_END + 64];
  LAS unsigned char* L = (LAS unsigned char*)smem;
  const unsigned lbase = (unsigned)(uintptr_t)L;
  const int tid = threadIdx.x, l = tid & 63, w = __builtin_amdgcn_readfirstlane(tid >> 6);
  const int c = blockIdx.x, xr = c >> 3, sp = xr & 3, kvh = xr >> 2, b = (c - kvh) & 7, i32 = b * 4 + sp;
  {
    u64 pc;
    asm volatile("s_getpc_b64 %0" : "=s"(pc));
    const v4u rc = make_srd((const void*)(pc & ~255ull), CODEB);
    DMA_LOOP("", rc, ((xr * 2 + w) & 31) * 1024 + l * 16, 0, lbase + L_NIL, (12u >> w) & 1u);
  }
  const int g = l >> 4;
  const int oh = (c & 7) * 4 + (xr >> 3), on = xr & 7;
  const bool ffn2 = xr >= 24 && (c & 7) < 7;
  const bool isq = xr < 16, side = xr < 23 || ffn2;
  const int tl = isq ? ((xr >> 1) * 8 + ((c + xr) & 7)) * 2 + (xr & 1) : (c & 7) * 7 + xr - 16;
  const int cb = ffn2 ? on : tl >> 1, kh = ffn2 ? c & 7 : tl & 1;
#define KP(v, i) ((u64)v[i] | ((u64)v[(i) + 1] << 32))
  if (w == 1) *(LAS v4u*)(L + L_SYNC + l * 16) = v4u{0u, 0u, 0u, 0u};
  if (__builtin_expect(w == 0, 1)) {
    const unsigned ldb = isq ? 8192u : ffn2 ? 1024u : (unsigned)FF * 2u;
    unsigned woff = (unsigned)kh * 256u * ldb + cb * 128, wlen = 255u * ldb + 128u, wstep = 8u * ldb, wvo = (unsigned)(l >> 3) * ldb + (unsigned)(l & 7) * 16u;
    unsigned nw = side ? 32 : 0;
    asm volatile("s_waitcnt lgkmcnt(0)" EBAR0 : "+s"(ka0), "+s"(woff), "+s"(wlen), "+s"(wstep), "+s"(nw), "+v"(wvo) :: "memory");
    u64 pq = KP(ka0, 2), p1 = KP(ka0, 6), p2 = KP(ka0, 10);
    asm volatile("" : "+s"(pq), "+s"(p1), "+s"(p2));
    DMA_LOOP("nt", make_srd((const unsigned char*)(isq ? pq : ffn2 ? p2 : p1) + woff, wlen), wvo, wstep, lbase + L_WT, nw);
    if (XAFT) asm volatile("s_barrier" ::: "memory");
    DMA_LOOP("", make_srd((const void*)KP(ka0, 0), 8192), l * 16, 1024, lbase + L_X, nw >> 2);
  }
  asm volatile("s_waitcnt lgkmcnt(0)" : "+s"(t_in64), "+s"(ka0), "+s"(ka1) :: "memory");
  u64 tag = 0;
  if (w != 0) tag = read_tag((const unsigned char*)KP(ka1, 4));
  if ((EBAR && !XAFT) || w != 0) WGBAR();
  const unsigned t_in = (unsigned)t_in64;
  Args a;
  a.x = (const unsigned short*)KP(ka0, 0); a.wq = (const unsigned short*)KP(ka0, 2); a.wo = (const unsigned short*)KP(ka0, 4); a.w1 = (const unsigned short*)KP(ka0, 6);
  a.b1 = (const unsigned short*)KP(ka0, 8); a.w2 = (const unsigned short*)KP(ka0, 10); a.b2 = (const unsigned short*)KP(ka0, 12); a.gw = (const unsigned short*)KP(ka0, 14);
  a.kc = (const unsigned char*)KP(ka1, 0); a.vc = (const unsigned char*)KP(ka1, 2); a.ws = (unsigned char*)KP(ka1, 4); a.out = (unsigned short*)KP(ka1, 6);
  unsigned char* ws = a.ws;
  if (l == 0) *(volatile LAS unsigned*)(L + L_END + w * 4) = (PROBE >= 30 && PROBE < 40) ? early - t_in : 0;
  PSTAMP(21);
  const __amdgpu_buffer_rsrc_t rws = __builtin_amdgcn_make_buffer_rsrc((void*)ws, 0, WS_END, 0x00020000);
  const int rr = c, rb = rr & 7, rc = rr >> 3;

  if (w == 0) {
    if (side) {
      const unsigned tg = (unsigned)read_tag(ws);
      float og = 0.f;
      if (ffn2) {
        const unsigned bb0 = *(const GAS unsigned*)(a.b1 + kh * 256 + l * 2), bb1 = *(const GAS unsigned*)(a.b1 + kh * 256 + 128 + l * 2);
        const v4u gv = *(const GAS v4u*)(a.gw + l * 8);
        DRAIN();
        const unsigned t_w = now_ticks();
        float gt = 0.f;
#pragma nounroll
        for (int j = 0; j < 8; ++j) { const float t = gate_of(*(const LAS v4u*)(L + L_X + j * 1024 + l * 16), gv); gt = (l & 7) == j ? t : gt; }
        if (l < 8) *(LAS float*)(L + L_G + l * 4) = gt;
        og = rbf(1.f - gt);
        WAIT_TICK(t_in, TH);
        if (RELQ) WAIT_TICK(t_w, 250);
        int spin = 0;
#pragma nounroll
        for (int hf = 0; hf < 2;) {
          v4u t[16];
#pragma unroll
          for (int i = 0; i < 16; ++i) {
            t[i] = __builtin_amdgcn_raw_buffer_load_b128(rws, WS_HP + kh * 32768 + hf * 16384 + l * 16, i * 1024, AUX_E);
            asm volatile("s_sleep %0" :: "n"(GH) : "memory");
          }
          int miss = -1;
#pragma unroll
          for (int j = 0; j < 8; ++j) {
            const int i0 = (j >> 1) * 4 + (j & 1);
            const unsigned bb = (j & 1) ? bb1 : bb0;
            miss = gbad(t[i0], tg) ? i0 : gbad(t[i0 + 2], tg) ? i0 + 2 : miss;
            const f32x2 z = gval(t[i0]) + gval(t[i0 + 2]);
            const float h0 = rbf(rbf(z.x) + bf_lo(bb)), h1 = rbf(rbf(z.y) + bf_hi(bb));
            const float r0 = h0 > 0.f ? h0 : 0.f, r1 = h1 > 0.f ? h1 : 0.f;
            *(LAS unsigned*)(L + L_H + (hf * 4 + (j >> 1)) * 1024 + (j & 1) * 256 + l * 4) = cvtpk(r0 * r0, r1 * r1);
          }
          if (__builtin_amdgcn_ballot_w64(miss >= 0) == 0 || spin >= SPIN_CAP) ++hf;
          else watch(rws, miss < 0 ? -1 : WS_HP + kh * 32768 + hf * 16384 + l * 16 + miss * 1024, tg, spin);
        }
        LDSWAIT();
        if (l == 0) LDS_SET(L_HRDY);
      }
      DRAIN();
      STAMP(1);
      PSTAMP(23);
      v4u xb[8];
      const LAS unsigned char* xp = L + (ffn2 ? L_H : L_X + kh * 512) + (l & 7) * 1024 + g * 16;
#pragma unroll
      for (int k = 0; k < 8; ++k) xb[k] = *(const LAS v4u*)(xp + k * 64);
      const int off = (isq ? WS_QP + (cb >> 3) * 65536 + (l & 7) * 8192 + kh * 4096 + (cb & 7) * 512 : WS_HP + (cb >> 2) * 32768 + (l & 7) * 4096 + kh * 2048 + (cb & 3) * 512)
                      + g * 32 + (l & 8) * 2;
#pragma nounroll
      for (int ct = 0; ct < 4; ++ct) {
        f32x4 d = {0.f, 0.f, 0.f, 0.f};
#pragma unroll
        for (int k = 0; k < 8; ++k) d = mfma32(tile_frag(L + L_WT, l, k, ct), xb[k], d);
        if (ffn2) *(LAS f32x4*)(L + L_FF + ct * 1024 + l * 16) = d * og;
        else { __builtin_amdgcn_raw_buffer_store_b128(gran_d(d, l, tg), rws, off + ct * 128, 0, AUX_E); if (WSP) asm volatile("s_sleep %0" :: "n"(WSP) : "memory"); }
      }
      DRAIN();
      if (l == 0) LDS_SET(L_HRDY);
      LDSWAIT();
      PSTAMP(24);
    }
    STAMP(2);
    FLUSH();
    return;
  }
  if (w == 1) {
    const v4u xv = *(const GAS v4u*)(a.x + rb * DM + l * 8), gv = *(const GAS v4u*)(a.gw + l * 8);
    const v2u bb = *(const GAS v2u*)(a.b2 + rc * 16 + 4 * (l & 3));
    const unsigned tg = (unsigned)tag, th = (unsigned)(tag >> 32);
    {
      const float gt = gate_of(xv, gv), og = rbf(1.f - gt);
      if (l < 4) *(LAS f32x4*)(L + L_FIN + l * 16) = f32x4{bf_lo(bb.x), bf_hi(bb.x), bf_lo(bb.y), bf_hi(bb.y)} * og;
      if (l == 0) *(LAS float*)(L + L_FIN + 64) = gt;
    }
    lds_wait<4>(L + L_ORG + 4);
    const unsigned org = *(volatile const LAS unsigned*)(L + L_ORG);
    if (l < 8) __builtin_amdgcn_raw_buffer_store_b128(v4u{org, tg, org, tg}, rws, WS_X + c * 128 + l * 16, 0, AUX_E);
    if (RELQ) { unsigned tq = __builtin_amdgcn_readfirstlane(org) + (TQ - T0) * 16; WAIT_NEXT(tq, 0); } else WAIT_TICK(t_in, TQ);
    v2u st;
#pragma nounroll
    for (int spin = 0; spin < SPIN_CAP;) {
      v4u t[8];
#pragma unroll
      for (int i = 0; i < 8; ++i) { t[i] = __builtin_amdgcn_raw_buffer_load_b128(rws, WS_QP + kvh * 65536 + b * 8192 + l * 16, i * 1024, AUX_E); asm volatile("s_sleep %0" :: "n"(GQ) : "memory"); }
      st = __builtin_amdgcn_raw_buffer_load_b64(rws, WS_X + (SAMP) * 128, 0, AUX_SC1);
      int miss = -1;
#pragma unroll
      for (int j = 0; j < 4; ++j) {
        miss = gbad(t[j], tg) ? j : gbad(t[4 + j], tg) ? 4 + j : miss;
        const f32x2 q = gval(t[j]) + gval(t[4 + j]);
        *(LAS unsigned*)(L + L_Q + j * 256 + l * 4) = cvtpk(q.x, q.y);
      }
      if (__builtin_amdgcn_ballot_w64(miss >= 0) == 0) break;
      watch(rws, miss < 0 ? -1 : WS_QP + kvh * 65536 + b * 8192 + l * 16 + miss * 1024, tg, spin);
    }
    {
      unsigned rel = (st.y == tg ? st.x : (now_ticks() << 4) - ((2 * WIN + 1) * DT0 + 2400)) - org;
      rel += dppu<0xB1>(rel); rel += dppu<0x4E>(rel); rel += dppu<0x141>(rel); rel += dppu<0x140>(rel);
      const int sum = __builtin_amdgcn_readlane((int)rel, 0) + __builtin_amdgcn_readlane((int)rel, 16);
      if (l == 0) *(LAS unsigned*)(L + L_TEND) = org + (sum >> 5) + (J1 + 1) * DT0 + (JA - J1) * DT + (31 - JA) * DT2 + 32 * DTV;
    }
    LDSWAIT();
    if (l == 0) LDS_SET(L_QRDY);
    STAMP(3);
    if (side) lds_wait<8>(L + L_HRDY);
    DRAIN();
    if (l == 0) __builtin_amdgcn_raw_buffer_store_b64(v2u{tg, th}, rws, WS_F + c * 8, 0, AUX_SC1);
    STAMP(5);
    lds_wait<4>(L + L_ML + (l & 31) * 16 + 8);
    const v2u ml = *(volatile const LAS v2u*)(L + L_ML + (l & 31) * 16);
    const float mw = __uint_as_float(ml.x);
    float M = fmaxf(mw, dpp<0xB1>(mw)); M = fmaxf(M, dpp<0x4E>(M)); M = fmaxf(M, dpp<0x141>(M));
    float Ls = __builtin_amdgcn_exp2f(mw - M) * __uint_as_float(ml.y);
    Ls += dpp<0xB1>(Ls); Ls += dpp<0x4E>(Ls); Ls += dpp<0x141>(Ls);
    const float lse = M + __builtin_amdgcn_logf(Ls);
    const v4u lg = gran2(lse, dpp<0x128>(lse), tg);
    if (l < 32 && (l & 15) == 0) ST_SC1(lg, WS_LSE + ((b * 8 + kvh) * 4 + sp) * 128 + l);
    STAMP(6);
    v2u ls;
    asm volatile("s_sleep %0" :: "n"(LSL) : "memory");
#pragma nounroll
    for (int spin = 0; spin < SPIN_CAP; ++spin) {
      ls = __builtin_amdgcn_raw_buffer_load_b64(rws, WS_LSE + ((b * 8 + kvh) * 4 + (l & 3)) * 128 + ((l >> 3) & 3) * 8, 0, LNT ? AUX_E : AUX_SC1);
      if (__builtin_amdgcn_ballot_w64(ls.y != tg) == 0) break;
      asm volatile("s_sleep 8" ::: "memory");
    }
    const float lf = __uint_as_float(ls.x);
    float M2 = fmaxf(lf, dpp<0xB1>(lf)); M2 = fmaxf(M2, dpp<0x4E>(M2));
    float E = __builtin_amdgcn_exp2f(lf - M2);
    E += dpp<0xB1>(E); E += dpp<0x4E>(E);
    if (l < 32) *(LAS float*)(L + L_CF + l * 4) = __builtin_amdgcn_exp2f(mw - M2) * __builtin_amdgcn_rcpf(E);
    WAIT_TICK(t_in, TF);
#pragma nounroll
    for (int spin = 0; spin < SPIN_CAP; ++spin) {
      const v4u f0 = LD_SC1(WS_F + l * 16), f1 = LD_SC1(WS_F + 1024 + l * 16);
      if (__builtin_amdgcn_ballot_w64(((f0.x ^ tg) | (f0.y ^ th) | (f0.z ^ tg) | (f0.w ^ th) | (f1.x ^ tg) | (f1.y ^ th) | (f1.z ^ tg) | (f1.w ^ th)) != 0) == 0) break;
      asm volatile("s_sleep 16" ::: "memory");
    }
    if (c == 0 && l == 0) __builtin_amdgcn_raw_buffer_store_b64(v2u{tg, th}, rws, WS_E, 0, AUX_SC1);
    LDSWAIT();
    STAMP(7);
    lds_wait<1>(L + L_DONE + (l & 7) * 4);
    asm volatile("s_sleep %0" :: "n"(WOS) : "memory");
    DMA_LOOP("nt", make_srd(a.wo + oh * 128 * DM + on * 64, 127 * 1024 + 128), (unsigned)(l >> 3) * 1024u + (unsigned)(l & 7) * 16u, 8192, lbase + L_WO, 16);
    WGBAR();
    DRAIN();
    if (l == 0) LDS_SET(L_WRDY);
    LDSWAIT();
    STAMP(8);
    FLUSH();
    return;
  }

  const int sw = w - 2;
  const unsigned stag = sw * (DT / 8);
  unsigned tnext = ((t_in + T0) << 4) + stag, dt = DT0;
#define PACE() do { asm volatile("s_waitcnt vmcnt(%0)" :: "n"(WIN - 1) : "memory"); WAIT_NEXT(tnext, dt); } while (0)
#define RESLIP() do { const unsigned t1_ = (now_ticks() - SLIP) << 4; if ((int)(t1_ - tnext) > 0) tnext = t1_ + (stag >> 1); } while (0)
#define REPLAN(done, nominal) do { RESLIP(); \
    const int ex_ = (int)(tnext + ((done) < 32 ? (32 - (done)) * DT2 + 32 * DTV : (64 - (done)) * DTV) - ((done) == 48 ? (stag >> SQZ) : stag) - tend); \
    dt = (nominal) - sclamp<-DN2, UP2>((ex_ * (65536 / (64 - (done)))) >> 16); } while (0)
  const int rot = (ROTF) & 31, blk = (BLKF) & 7;
  constexpr unsigned e0 = E0 << 8, e1 = E1 << 8, e2 = E2 << 8, e3 = E3 << 8, e4 = E4 << 8, tv0 = TV0 << 8, tv1 = TV1 << 8;
  const unsigned x0 = (XF) << 8;
  const size_t base = (size_t)(b * HKV + kvh) * SK * 256;
  constexpr unsigned KVLEN = 1048576;
#define OFF(i) (x0 ^ (((i) & 1) ? e0 : 0u) ^ (((i) & 2) ? e1 : 0u) ^ (((i) & 4) ? e2 : 0u) ^ (((i) & 8) ? e3 : 0u) ^ (((i) & 16) ? e4 : 0u))
  const v4u rk = make_srd(a.kc + base, KVLEN), rv = make_srd(a.vc + base, KVLEN);
  const int ko = ((l >> 2) & 1) * tv0 + ((l >> 3) & 1) * tv1 + g * 64 + (l & 3) * 16, vo = (g & 1) * tv0 + (g >> 1) * tv1 + (l & 15) * 16;
#define LOAD_V(i) asm volatile("buffer_load_dwordx4 %0, %1, %2, %3 offen nt" : "+v"(kv[i]) : "v"(vo), "s"(rv), "s"(OFF(i)) : "memory")
  const unsigned tg = (unsigned)tag;
  unsigned tend = 0;
  { const unsigned t1 = ((now_ticks() - SLIP0) << 4) + stag; if ((int)(t1 - tnext) > 0) tnext = t1; }
  v4u kv[32];
  __builtin_amdgcn_sched_barrier(0);
#pragma unroll
  for (int i = 0; i < 32; ++i) {
    PACE();
    asm volatile("buffer_load_dwordx4 %0, %1, %2, %3 offen nt" : "=v"(kv[i]) : "v"(ko), "s"(rk), "s"(OFF(i)) : "memory");
    PSTAMP(100 + i);
    if (i == WIN) RESLIP();
    if (i == 2 * WIN) {
      RESLIP();
      const unsigned org = tnext - stag - (2 * WIN + 1) * DT0;
      if (sw == 3 && l == 0) { *(volatile LAS unsigned*)(L + L_ORG) = org; LDS_SET(L_ORG + 4); }
    }
    if (i == J1) dt = DT;
    if (i == JA) {
      const unsigned f = __builtin_amdgcn_readfirstlane(*(volatile const LAS unsigned*)(L + L_QRDY));
      tend = __builtin_amdgcn_readfirstlane(*(volatile const LAS unsigned*)(L + L_TEND));
      if (f) REPLAN(JA + 1, DT2);
    }
    __builtin_amdgcn_sched_barrier(0);
  }
  STAMP(1);
  lds_wait<2>(L + L_QRDY);
  STAMP(2);
  const v4u qf = *(const LAS v4u*)(L + L_Q + ((l >> 2) & 3) * 256 + g * 64 + (l & 3) * 16);
  tend = __builtin_amdgcn_readfirstlane(*(const LAS unsigned*)(L + L_TEND));
  REPLAN(32, DTV);
  __builtin_amdgcn_sched_barrier(0);
  constexpr float C2 = 0.088388347648318440f * 1.4426950408889634f;
  float sc[8];
  const int s = l & 3;
#pragma unroll
  for (int m = 0; m < 8; ++m) {
    asm volatile("s_waitcnt vmcnt(%4)" : "+v"(kv[4 * m]), "+v"(kv[4 * m + 1]), "+v"(kv[4 * m + 2]), "+v"(kv[4 * m + 3]) : "n"(28 - 4 * m + m) : "memory");
    f32x4 d[4];
#pragma unroll
    for (int r = 0; r < 4; ++r) d[r] = mfma32(kv[4 * m + r], qf, f32x4{0.f, 0.f, 0.f, 0.f});
#pragma unroll
    for (int r = 0; r < 4; ++r) {
      float t = dpp<0x00>(d[r][0]); t = dpp<0x55>(d[r][1]) + t; t = dpp<0xAA>(d[r][2]) + t; t = dpp<0xFF>(d[r][3]) + t;
      asm volatile("" : "+v"(t));
      sc[m] = (r == 0 || s == r) ? t : sc[m];
    }
    asm volatile("" : "+v"(sc[m]));
    __builtin_amdgcn_sched_barrier(0);
    PACE();
    LOAD_V(m);
    __builtin_amdgcn_sched_barrier(0);
  }
  STAMP(3);
  float mx = sc[0];
#pragma unroll
  for (int m = 1; m < 8; ++m) mx = fmaxf(mx, sc[m]);
  mx = fmaxf(mx, dpp<0xB1>(mx)); mx = fmaxf(mx, dpp<0x4E>(mx)); mx = xrow_max(mx);
  const float mneg = -mx * C2;
  float lsum = 0.f;
  unsigned pk[8];
#pragma unroll
  for (int m = 0; m < 8; ++m) { const float p = __builtin_amdgcn_exp2f(fmaf(sc[m], C2, mneg)); pk[m] = cvtpk(p, p); lsum += bf_lo(pk[m]); }
  lsum += dpp<0xB1>(lsum); lsum += dpp<0x4E>(lsum); lsum = xrow_sum(lsum);
  if (l < 16 && (l & 3) == 0) {
    *(volatile LAS v2u*)(L + L_ML + ((l >> 2) * 8 + sw) * 16) = v2u{__float_as_uint(mx * C2), __float_as_uint(lsum)};
    LDS_SET(L_ML + ((l >> 2) * 8 + sw) * 16 + 8);
  }
  const unsigned half = (l & 1) ? 0xffff0000u : 0x0000ffffu;
  const unsigned m0 = (l & 2) ? 0u : half, m1 = (l & 2) ? half : 0u;
  f32x4 o0 = {0.f, 0.f, 0.f, 0.f}, o1 = {0.f, 0.f, 0.f, 0.f};
  const int s4 = sw & 3, pb8 = l & 7;
  unsigned a_mw, a_mr, a_g0, a_l0, a_l1, a_g1;
#pragma unroll
  for (int j = 8; j < 40; ++j) {
    if (j < 32) { PACE(); LOAD_V(j); if (j == 15) REPLAN(48, DTV); __builtin_amdgcn_sched_barrier(0); }
    if (j == 31) { if (l == 0) LDS_SET(L_DONE + sw * 4); STAMP(11); }
    if (j == 33) {
      a_mw = L_MRG + (sw * 4 + g) * 512 + (l & 15) * 32;
      a_mr = L_MRG + s4 * 512 + l * 8;
      if (sw < 4) {
        a_g0 = (l & 3) == 0 ? WS_P + (((kvh * 4 + s4) * 8 + b) * 4 + sp) * 256 + l * 4 : WS_END;
        a_l0 = L_WO + (8 * g + ((l & 15) >> 2)) * 128 + (l & 3) * 8 + s4 * 32;
        a_l1 = L_PG + (pb8 * 4 + ((l >> 3) & 1)) * 272 + g * 16;
        a_g1 = (l & 8) == 0 ? WS_O + (((on * 4 + s4) * 4 + (pb8 >> 1)) * 32 + oh) * 128 + (pb8 & 1) * 64 + g * 16 : WS_END;
      } else {
        a_g0 = WS_P + (oh * 8 + 2 * s4) * 1024 + l * 16;
        a_l0 = L_PG + (8 * s4 + g) * 272 + (l & 15) * 16;
        a_l1 = (l & 15) < 4 ? (rb * DM + rc * 16 + 4 * (l & 3) + g) * 2 : 8192;
        a_g1 = WS_O + (rr >> 1) * 4096 + (l >> 2) * 128 + (rr & 1) * 64 + (l & 3) * 16;
      }
      asm volatile("" : "+v"(a_mw), "+v"(a_mr), "+v"(a_g0), "+v"(a_l0), "+v"(a_l1), "+v"(a_g1));
      __builtin_amdgcn_sched_barrier(0);
    }
    const int i = j - 8;
    const unsigned pb = (i & 3) == 0 ? dppu<0x00>(pk[i >> 2]) : (i & 3) == 1 ? dppu<0x55>(pk[i >> 2]) : (i & 3) == 2 ? dppu<0xAA>(pk[i >> 2]) : dppu<0xFF>(pk[i >> 2]);
    const v2u av = {pb & m0, pb & m1};
    asm volatile("s_waitcnt vmcnt(%1)" : "+v"(kv[i]) : "n"((j < 32 ? j : 31) - i) : "memory");
    o0 = mfma16(av, v2u{kv[i].x, kv[i].y}, o0);
    o1 = mfma16(av, v2u{kv[i].z, kv[i].w}, o1);
    __builtin_amdgcn_sched_barrier(0);
  }
  *(LAS f32x4*)(L + a_mw) = o0;
  *(LAS f32x4*)(L + a_mw + 16) = o1;
  STAMP(4);
  if (sw < 4) {
    asm volatile("s_waitcnt lgkmcnt(0)\n\ts_barrier ; merge" ::: "memory");
    STAMP(5);
    const f32x4 c0 = *(const LAS f32x4*)(L + L_CF + sw * 32), c1 = *(const LAS f32x4*)(L + L_CF + sw * 32 + 16);
    f32x2 ac0 = {0.f, 0.f}, ac1 = {0.f, 0.f};
#pragma unroll
    for (int j = 0; j < 4; ++j) {
      ac0 += *(const LAS f32x2*)(L + a_mr + j * 2048) * c0[j];
      ac1 += *(const LAS f32x2*)(L + a_mr + (4 + j) * 2048) * c1[j];
    }
    ac0 += ac1;
    const unsigned pv = cvtpk(ac0.x, ac0.y);
    ST_SC1((v4u{pv, dppu<0x55>(pv), dppu<0xAA>(pv), dppu<0xFF>(pv)}), a_g0);
    STAMP(6);
    f32x4 ff = {0.f, 0.f, 0.f, 0.f};
    float gm = 1.f;
    if (ffn2) { ff = *(const LAS f32x4*)(L + L_FF + sw * 1024 + l * 16); gm = *(const LAS float*)(L + L_G + pb8 * 4); }
    lds_wait<0>(L + L_WRDY);
    v4u wf[4];
#pragma unroll
    for (int k = 0; k < 4; ++k) { const v2u t0 = tr_read(L + a_l0 + k * 4096), t1 = tr_read(L + a_l0 + k * 4096 + 512); wf[k] = v4u{t0.x, t0.y, t1.x, t1.y}; }
    asm volatile("" : "+v"(wf[0]), "+v"(wf[1]), "+v"(wf[2]), "+v"(wf[3]), "+v"(ff), "+v"(gm));
    WGBAR();
    v4u pa[8];
#pragma unroll
    for (int k = 0; k < 8; ++k) pa[k] = *(const LAS v4u*)(L + a_l1 + (k & 1) * 544 + (k >> 1) * 64);
    __builtin_amdgcn_sched_barrier(0);
    f32x4 da = {0.f, 0.f, 0.f, 0.f};
#pragma unroll
    for (int k = 0; k < 8; ++k) da = mfma32(wf[k >> 1], pa[k], da);
    v4u r;
#pragma unroll
    for (int j = 0; j < 4; ++j) r[j] = __float_as_uint(fmaf(da[j] + dpp<0x128>(da[j]), gm, ff[j]));
    r.y &= ~1u; r.w &= ~1u;
    ST_SC1(r, a_g1);
    STAMP(8);
  } else {
    v4u p0, p1;
    asm volatile("s_waitcnt lgkmcnt(0)\n\ts_barrier\n\ts_sleep %0" :: "n"(PDELAY1) : "memory");
    poll2(rws, a_g0, a_g0 + 1024, tg, p0, p1);
    *(LAS v4u*)(L + a_l0) = p0;
    *(LAS v4u*)(L + a_l0 + 4 * 272) = p1;
    STAMP(7);
    LDSWAIT();
    WGBAR();
    asm volatile("s_waitcnt vmcnt(0)" :: "v"(p0), "v"(p1) : "memory");
    if (sw == 4) {
      float fin = *(const LAS float*)(L + L_FIN + (l & 3) * 16 + g * 4);
      const float gt = *(const LAS float*)(L + L_FIN + 64);
      float g0 = (l & 12) == 12 ? 1.f : gt, g1 = l < 48 ? g0 : gt;
      const __amdgpu_buffer_rsrc_t rout = __builtin_amdgcn_make_buffer_rsrc((void*)a.out, 0, 8192, 0x00020000);
      asm volatile("s_sleep %3" : "+v"(fin), "+v"(g0), "+v"(g1) : "n"(PDELAY2) : "memory");
      v4u r0, r1;
      poll2(rws, a_g1, a_g1 + 2048, tg, r0, r1);
      STAMP(9);
      f32x4 av = __builtin_bit_cast(f32x4, r0) * g0 + __builtin_bit_cast(f32x4, r1) * g1;
      auto s0 = __builtin_amdgcn_permlane16_swap(__float_as_uint(av[0]), __float_as_uint(av[1]), false, false);
      auto s1 = __builtin_amdgcn_permlane16_swap(__float_as_uint(av[2]), __float_as_uint(av[3]), false, false);
      const float u0 = __uint_as_float(s0[0]) + __uint_as_float(s0[1]), u1 = __uint_as_float(s1[0]) + __uint_as_float(s1[1]);
      auto s2 = __builtin_amdgcn_permlane32_swap(__float_as_uint(u0), __float_as_uint(u1), false, false);
      float v = __uint_as_float(s2[0]) + __uint_as_float(s2[1]);
      v += dpp<0x124>(v); v += dpp<0x128>(v);
      v += fin;
      __builtin_amdgcn_raw_buffer_store_b16((unsigned short)cvtpk(v, v), rout, a_l1, 0, OAUX);
      STAMP(10);
      asm volatile("s_waitcnt vmcnt(0)" :: "v"(r0), "v"(r1) : "memory");
      { unsigned m = 0;
        for (int i = PW0; i < PW1; ++i) m = max(m, *(volatile LAS unsigned*)(L + L_END + i * 4));
        m = __builtin_amdgcn_readfirstlane(m);
        unsigned tq = (t_in + 4000 + 4 * m) << 4; WAIT_NEXT(tq, dt); }
    }
  }
  FLUSH();
}

extern "C" void kernel_launch(void* const* d_in, const int* in_sizes, int n_in, void* d_out, int out_size, void* d_ws, size_t ws_size,
                              hipStream_t stream) {
  Args a;
  a.x = (const unsigned short*)d_in[0];
  a.kc = (const unsigned char*)d_in[1];
  a.vc = (const unsigned char*)d_in[2];
  a.wq = (const unsigned short*)d_in[3];
  a.wo = (const unsigned short*)d_in[4];
  a.w1 = (const unsigned short*)d_in[6];
  a.b1 = (const unsigned short*)d_in[7];
  a.w2 = (const unsigned short*)d_in[8];
  a.b2 = (const unsigned short*)d_in[9];
  a.gw = (const unsigned short*)d_in[10];
  a.ws = (unsigned char*)d_ws;
  a.out = (unsigned short*)d_out;
  fused_decode<<<256, NWAVE * 64, 0, stream>>>(a);
}
```

```cpp
#include <hip/hip_runtime.h>
#include <hip/hip_bf16.h>
#include <stdint.h>

typedef unsigned v16u __attribute__((ext_vector_type(16)));
typedef unsigned v8u __attribute__((ext_vector_type(8)));
typedef unsigned v4u __attribute__((ext_vector_type(4)));
typedef unsigned v2u __attribute__((ext_vector_type(2)));
typedef float f32x4 __attribute__((ext_vector_type(4)));
typedef float f32x2 __attribute__((ext_vector_type(2)));
typedef __bf16 bf16x8 __attribute__((ext_vector_type(8)));
typedef __bf16 bf16x2 __attribute__((ext_vector_type(2)));
typedef short s16x4 __attribute__((ext_vector_type(4)));
typedef unsigned long long u64;
#define GAS __attribute__((address_space(1)))
#define LAS __attribute__((address_space(3)))

constexpr int DM = 512, HKV = 8, SK = 4096, FF = 1792, NWAVE = 10;
constexpr int AUX_SC1 = 16;
constexpr int AUX_E = 18;
constexpr int SPIN_CAP = 1 << 18;
constexpr int T0 = 115;
constexpr int DT0 = 485, DT = 452, DTV = 439;
constexpr int WIN = 4;
constexpr int J1 = 2 * WIN, JA = 24;
constexpr int SLIP0 = 30, SLIP = 50;
constexpr int UP = 40, DN = 40;
constexpr int TQ = 560, TH = 480, TF = 1500;
constexpr int CODEB = 17408;

constexpr int WS_E = 0;
constexpr int WS_F = 2048;
constexpr int WS_QP = 4096;
constexpr int WS_HP = WS_QP + 8 * 8 * 2 * 512 * 8;
constexpr int WS_X = WS_HP + 7 * 8 * 2 * 256 * 8;
constexpr int WS_LSE = WS_X + 256 * 128;
constexpr int WS_END = WS_LSE + 256 * 128;
constexpr int WS_O = WS_QP;
constexpr int WS_P = WS_HP;
static_assert(WS_LSE - WS_P == 32 * 8 * 4 * 256, "the partials fill exactly the lines of HP and X");
constexpr int L_WO = 0, L_WT = 16384, L_MRG = 49152, L_Q = L_MRG + 16384, L_X = L_Q + 1024, L_H = L_X + 8192, L_NIL = L_H + 8192, L_FF = L_NIL + 1024,
              L_CF = L_FF + 4096, L_G = L_CF + 128, L_FIN = L_G + 32,
              L_SYNC = L_FIN + 96,
              L_ML = L_SYNC, L_QRDY = L_ML + 512, L_TEND = L_QRDY + 4, L_HRDY = L_QRDY + 8, L_WRDY = L_QRDY + 12, L_ORG = L_QRDY + 16, L_DONE = L_QRDY + 32,
              L_END = L_SYNC + 1024;
constexpr int L_PG = L_WT;

__device__ __forceinline__ f32x4 mfma32(v4u a, v4u b, f32x4 c) {
  return __builtin_amdgcn_mfma_f32_16x16x32_bf16(__builtin_bit_cast(bf16x8, a), __builtin_bit_cast(bf16x8, b), c, 0, 0, 0);
}
__device__ __forceinline__ f32x4 mfma16(v2u a, v2u b, f32x4 c) {
  return __builtin_amdgcn_mfma_f32_16x16x16bf16_1k(__builtin_bit_cast(s16x4, a), __builtin_bit_cast(s16x4, b), c, 0, 0, 0);
}
__device__ __forceinline__ unsigned cvtpk(float lo, float hi) { bf16x2 r = {(__bf16)lo, (__bf16)hi}; return __builtin_bit_cast(unsigned, r); }
__device__ __forceinline__ float bf_lo(unsigned u) { return __builtin_bit_cast(float, u << 16); }
__device__ __forceinline__ float bf_hi(unsigned u) { return __builtin_bit_cast(float, u & 0xffff0000u); }
__device__ __forceinline__ float rbf(float x) { return (float)(__bf16)x; }
template <int CTRL> __device__ __forceinline__ float dpp(float x) { return __builtin_bit_cast(float, __builtin_amdgcn_mov_dpp(__builtin_bit_cast(int, x), CTRL, 0xf, 0xf, true)); }
template <int CTRL> __device__ __forceinline__ unsigned dppu(unsigned x) { return (unsigned)__builtin_amdgcn_mov_dpp((int)x, CTRL, 0xf, 0xf, true); }
__device__ __forceinline__ float xrow_max(float x) {
  auto s = __builtin_amdgcn_permlane16_swap(__float_as_uint(x), __float_as_uint(x), false, false);
  x = fmaxf(__uint_as_float(s[0]), __uint_as_float(s[1]));
  auto t = __builtin_amdgcn_permlane32_swap(__float_as_uint(x), __float_as_uint(x), false, false);
  return fmaxf(__uint_as_float(t[0]), __uint_as_float(t[1]));
}
__device__ __forceinline__ float xrow_sum(float x) {
  auto s = __builtin_amdgcn_permlane16_swap(__float_as_uint(x), __float_as_uint(x), false, false);
  x = __uint_as_float(s[0]) + __uint_as_float(s[1]);
  auto t = __builtin_amdgcn_permlane32_swap(__float_as_uint(x), __float_as_uint(x), false, false);
  return __uint_as_float(t[0]) + __uint_as_float(t[1]);
}
__device__ __forceinline__ v2u tr_read(const LAS unsigned char* p) { return __builtin_bit_cast(v2u, __builtin_amdgcn_ds_read_tr16_b64_v4i16((LAS s16x4*)p)); }
__device__ __forceinline__ v4u make_srd(const void* p, unsigned bytes) {
  const u64 a = (u64)p;
  return v4u{(unsigned)a, (unsigned)(a >> 32) & 0xffffu, bytes, 0x00020000u};
}
#define DMA_LOOP(POL, srd, voff, step, lds_addr, n) do { unsigned keep_, so_ = 0, n_ = (n); \
  asm volatile("s_cmp_eq_u32 %2, 0\n\ts_cbranch_scc1 2f\n\ts_mov_b32 %0, m0\n\ts_mov_b32 m0, %3\n\ts_nop 0\n1:\n\tbuffer_load_dwordx4 %4, %5, %1 offen " POL " lds\n\t" \
               "s_add_u32 m0, m0, 0x400\n\ts_add_u32 %1, %1, %6\n\ts_sub_u32 %2, %2, 1\n\ts_cmp_lg_u32 %2, 0\n\ts_cbranch_scc1 1b\n\ts_mov_b32 m0, %0\n2:" \
               : "=&s"(keep_), "+&s"(so_), "+&s"(n_) : "s"(lds_addr), "v"(voff), "s"(srd), "s"(step) : "scc", "memory"); } while (0)
__device__ __forceinline__ v4u tile_frag(const LAS unsigned char* tile, int l, int k, int ct) {
  const LAS unsigned char* p = tile + (8 * (l >> 4) + ((l & 15) >> 2)) * 128 + (l & 3) * 8 + k * 4096 + ct * 32;
  const v2u t0 = tr_read(p), t1 = tr_read(p + 512);
  return v4u{t0.x, t0.y, t1.x, t1.y};
}
#define DRAIN() asm volatile("s_waitcnt vmcnt(0)" ::: "memory")
#define LDSWAIT() asm volatile("s_waitcnt lgkmcnt(0)" ::: "memory")
#define SLEEP(n) asm volatile("s_sleep %0" :: "n"(n) : "memory")
#define WGBAR() do { __builtin_amdgcn_sched_barrier(0); __builtin_amdgcn_s_barrier(); __builtin_amdgcn_sched_barrier(0); asm volatile("" ::: "memory"); } while (0)
__device__ __forceinline__ u64 read_tag(const unsigned char* ws) {
  u64 t;
  asm volatile("s_load_dwordx2 %0, %1, 0x0\n\ts_waitcnt lgkmcnt(0)" : "=s"(t) : "s"(ws) : "memory");
  const unsigned old = (unsigned)t;
  t += 0x9E3779B97F4A7C15ull;
  t = (t ^ (t >> 30)) * 0xBF58476D1CE4E5B9ull; t = (t ^ (t >> 27)) * 0x94D049BB133111EBull;
  t ^= t >> 31;
  t = (t | 0x7F81u) & ~0x40ull;
  return (unsigned)t == old ? t ^ 2u : t;
}
__device__ __forceinline__ unsigned now_ticks() {
  u64 t;
  asm volatile("s_memrealtime %0\n\ts_waitcnt lgkmcnt(0)" : "=s"(t) :: "memory");
  return (unsigned)t;
}
#define WAIT_TICK(base, delta) \
  asm volatile("s_branch 2f\n1:\n\ts_sleep 1\n2:\n\ts_memrealtime vcc\n\ts_waitcnt lgkmcnt(0)\n\ts_sub_u32 vcc_lo, vcc_lo, %0\n\ts_cmpk_lt_i32 vcc_lo, %1\n\ts_cbranch_scc1 1b" \
               :: "s"(base), "n"(delta) : "scc", "vcc", "memory")
#define WAIT_NEXT(acc, step) \
  asm volatile("s_branch 2f\n1:\n\ts_sleep 1\n2:\n\ts_memrealtime vcc\n\ts_waitcnt lgkmcnt(0)\n\ts_lshl_b32 vcc_lo, vcc_lo, 4\n\ts_sub_u32 vcc_lo, vcc_lo, %0\n\ts_cmp_lt_i32 vcc_lo, 0\n\ts_cbranch_scc1 1b\n\ts_add_u32 %0, %0, %1" \
               : "+s"(acc) : "s"(step) : "scc", "vcc", "memory")
template <int LO, int HI> __device__ __forceinline__ int sclamp(int x) {
  asm("s_max_i32 %0, %0, %1\n\ts_min_i32 %0, %0, %2" : "+s"(x) : "n"(LO), "n"(HI) : "scc");
  return x;
}
#define LD_SC1(off) __builtin_amdgcn_raw_buffer_load_b128(rws, (off), 0, AUX_SC1)
#define ST_SC1(v, off) __builtin_amdgcn_raw_buffer_store_b128((v), rws, (off), 0, AUX_SC1)
__device__ __forceinline__ v4u gran2(float a, float b, unsigned tg) { return v4u{__float_as_uint(a), tg, __float_as_uint(b), tg}; }
__device__ __forceinline__ unsigned gbad(v4u g, unsigned tg) { return (g.y ^ tg) | (g.w ^ tg); }
__device__ __forceinline__ f32x2 gval(v4u g) { return f32x2{__uint_as_float(g.x), __uint_as_float(g.z)}; }
__device__ __forceinline__ v4u gran_d(f32x4 d, int l, unsigned tg) { return gran2((l & 8) ? d[2] : d[0], (l & 8) ? d[3] : d[1], tg); }
__device__ __forceinline__ void watch(__amdgpu_buffer_rsrc_t rws, int miss, unsigned tg, int& spin) {
#pragma nounroll
  while (miss >= 0 && spin < SPIN_CAP) {
    SLEEP(24);
    if (gbad(__builtin_amdgcn_raw_buffer_load_b128(rws, miss, 0, AUX_E), tg) == 0) miss = -1;
    ++spin;
  }
  spin = __builtin_amdgcn_ballot_w64(spin >= SPIN_CAP) != 0 ? SPIN_CAP : __builtin_amdgcn_readfirstlane(spin) + 1;
}
template <int NAP> __device__ __forceinline__ void lds_wait(const LAS unsigned char* p) {
#pragma nounroll
  for (int spin = 0; spin < SPIN_CAP * 16; ++spin) {
    if (__builtin_amdgcn_ballot_w64(*(volatile const LAS unsigned*)p == 0u) == 0) break;
    if (NAP) SLEEP(NAP);
  }
  asm volatile("" ::: "memory");
}
#define LDS_SET(off) (*(volatile LAS unsigned*)(L + (off)) = 1u)
__device__ __forceinline__ void poll2(__amdgpu_buffer_rsrc_t rws, int o0, int o1, unsigned tg, v4u& r0, v4u& r1) {
#pragma nounroll
  for (int spin = 0; spin < SPIN_CAP; ++spin) {
    r0 = LD_SC1(o0); r1 = LD_SC1(o1);
    if (__builtin_amdgcn_ballot_w64(min(min(r0.y ^ tg, r0.w ^ tg), min(r1.y ^ tg, r1.w ^ tg)) == 0u) == 0) break;
    asm volatile("" ::: "memory");
  }
}
__device__ __forceinline__ float gate_of(v4u xv, v4u gv) {
  float gs = 0.f;
#pragma unroll
  for (int j = 0; j < 4; ++j) { gs += rbf(bf_lo(xv[j]) * bf_lo(gv[j])); gs += rbf(bf_hi(xv[j]) * bf_hi(gv[j])); }
  gs += dpp<0xB1>(gs); gs += dpp<0x4E>(gs); gs += dpp<0x124>(gs); gs += dpp<0x128>(gs); gs = xrow_sum(gs);
  return rbf(1.f / (1.f + __expf(-rbf(gs))));
}

struct Args {
  const unsigned short *x, *wq, *wo, *w1, *b1, *w2, *b2, *gw;
  const unsigned char *kc, *vc;
  unsigned char* ws;
  unsigned short* out;
};

__global__ void __launch_bounds__(NWAVE * 64, 3) fused_decode(Args a_) {
  v16u ka0; v8u ka1; u64 t_in64;
  asm volatile("s_load_dwordx16 %0, %3, 0x0\n\ts_load_dwordx8 %1, %3, 0x40\n\ts_memrealtime %2" : "=&s"(ka0), "=&s"(ka1), "=&s"(t_in64) : "s"(__builtin_amdgcn_kernarg_segment_ptr()));
  __shared__ __attribute__((aligned(16))) unsigned char smem[L_END];
  LAS unsigned char* L = (LAS unsigned char*)smem;
  const unsigned lbase = (unsigned)(uintptr_t)L;
  const int tid = threadIdx.x, l = tid & 63, w = __builtin_amdgcn_readfirstlane(tid >> 6);
  const int c = blockIdx.x, xr = c >> 3, sp = xr & 3, kvh = xr >> 2, b = (c - kvh) & 7;
  {
    u64 pc;
    asm volatile("s_getpc_b64 %0" : "=s"(pc));
    const v4u rc = make_srd((const void*)(pc & ~255ull), CODEB);
    DMA_LOOP("", rc, ((xr * 2 + w) & 31) * 1024 + l * 16, 0, lbase + L_NIL, (12u >> w) & 1u);
  }
  const int g = l >> 4;
  const int oh = (c & 7) * 4 + (xr >> 3), on = xr & 7;
  const bool ffn2 = xr >= 24 && (c & 7) < 7;
  const bool isq = xr < 16, side = xr < 23 || ffn2;
  const int tl = isq ? ((xr >> 1) * 8 + ((c + xr) & 7)) * 2 + (xr & 1) : (c & 7) * 7 + xr - 16;
  const int cb = ffn2 ? on : tl >> 1, kh = ffn2 ? c & 7 : tl & 1;
#define KP(v, i) ((u64)v[i] | ((u64)v[(i) + 1] << 32))
  if (w == 1) *(LAS v4u*)(L + L_SYNC + l * 16) = v4u{0u, 0u, 0u, 0u};
  if (__builtin_expect(w == 0, 1)) {
    const unsigned ldb = isq ? 8192u : ffn2 ? 1024u : (unsigned)FF * 2u;
    unsigned woff = (unsigned)kh * 256u * ldb + cb * 128, wlen = 255u * ldb + 128u, wstep = 8u * ldb, wvo = (unsigned)(l >> 3) * ldb + (unsigned)(l & 7) * 16u;
    unsigned nw = side ? 32 : 0;
    asm volatile("s_waitcnt lgkmcnt(0)" : "+s"(ka0), "+s"(woff), "+s"(wlen), "+s"(wstep), "+s"(nw), "+v"(wvo) :: "memory");
    u64 pq = KP(ka0, 2), p1 = KP(ka0, 6), p2 = KP(ka0, 10);
    asm volatile("" : "+s"(pq), "+s"(p1), "+s"(p2));
    DMA_LOOP("nt", make_srd((const unsigned char*)(isq ? pq : ffn2 ? p2 : p1) + woff, wlen), wvo, wstep, lbase + L_WT, nw);
    DMA_LOOP("", make_srd((const void*)KP(ka0, 0), 8192), l * 16, 1024, lbase + L_X, nw >> 2);
  }
  asm volatile("s_waitcnt lgkmcnt(0)" : "+s"(t_in64), "+s"(ka0), "+s"(ka1) :: "memory");
  u64 tag = 0;
  if (w != 0) tag = read_tag((const unsigned char*)KP(ka1, 4));
  WGBAR();
  const unsigned t_in = (unsigned)t_in64;
  Args a;
  a.x = (const unsigned short*)KP(ka0, 0); a.wq = (const unsigned short*)KP(ka0, 2); a.wo = (const unsigned short*)KP(ka0, 4); a.w1 = (const unsigned short*)KP(ka0, 6);
  a.b1 = (const unsigned short*)KP(ka0, 8); a.w2 = (const unsigned short*)KP(ka0, 10); a.b2 = (const unsigned short*)KP(ka0, 12); a.gw = (const unsigned short*)KP(ka0, 14);
  a.kc = (const unsigned char*)KP(ka1, 0); a.vc = (const unsigned char*)KP(ka1, 2); a.ws = (unsigned char*)KP(ka1, 4); a.out = (unsigned short*)KP(ka1, 6);
  const __amdgpu_buffer_rsrc_t rws = __builtin_amdgcn_make_buffer_rsrc((void*)a.ws, 0, WS_END, 0x00020000);
  const int rb = c & 7, rc = c >> 3;

  if (w == 0) {
    if (side) {
      const unsigned tg = (unsigned)read_tag(a.ws);
      float og = 0.f;
      if (ffn2) {
        const unsigned bb0 = *(const GAS unsigned*)(a.b1 + kh * 256 + l * 2), bb1 = *(const GAS unsigned*)(a.b1 + kh * 256 + 128 + l * 2);
        const v4u gv = *(const GAS v4u*)(a.gw + l * 8);
        DRAIN();
        const unsigned t_w = now_ticks();
        float gt = 0.f;
#pragma nounroll
        for (int j = 0; j < 8; ++j) { const float t = gate_of(*(const LAS v4u*)(L + L_X + j * 1024 + l * 16), gv); gt = (l & 7) == j ? t : gt; }
        if (l < 8) *(LAS float*)(L + L_G + l * 4) = gt;
        og = rbf(1.f - gt);
        WAIT_TICK(t_in, TH);
        WAIT_TICK(t_w, 250);
        int spin = 0;
#pragma nounroll
        for (int hf = 0; hf < 2;) {
          v4u t[16];
#pragma unroll
          for (int i = 0; i < 16; ++i) { t[i] = __builtin_amdgcn_raw_buffer_load_b128(rws, WS_HP + kh * 32768 + hf * 16384 + l * 16, i * 1024, AUX_E); SLEEP(3); }
          int miss = -1;
#pragma unroll
          for (int j = 0; j < 8; ++j) {
            const int i0 = (j >> 1) * 4 + (j & 1);
            const unsigned bb = (j & 1) ? bb1 : bb0;
            miss = gbad(t[i0], tg) ? i0 : gbad(t[i0 + 2], tg) ? i0 + 2 : miss;
            const f32x2 z = gval(t[i0]) + gval(t[i0 + 2]);
            const float h0 = rbf(rbf(z.x) + bf_lo(bb)), h1 = rbf(rbf(z.y) + bf_hi(bb));
            const float r0 = h0 > 0.f ? h0 : 0.f, r1 = h1 > 0.f ? h1 : 0.f;
            *(LAS unsigned*)(L + L_H + (hf * 4 + (j >> 1)) * 1024 + (j & 1) * 256 + l * 4) = cvtpk(r0 * r0, r1 * r1);
          }
          if (__builtin_amdgcn_ballot_w64(miss >= 0) == 0 || spin >= SPIN_CAP) ++hf;
          else watch(rws, miss < 0 ? -1 : WS_HP + kh * 32768 + hf * 16384 + l * 16 + miss * 1024, tg, spin);
        }
        LDSWAIT();
        if (l == 0) LDS_SET(L_HRDY);
      }
      DRAIN();
      v4u xb[8];
      const LAS unsigned char* xp = L + (ffn2 ? L_H : L_X + kh * 512) + (l & 7) * 1024 + g * 16;
#pragma unroll
      for (int k = 0; k < 8; ++k) xb[k] = *(const LAS v4u*)(xp + k * 64);
      const int off = (isq ? WS_QP + (cb >> 3) * 65536 + (l & 7) * 8192 + kh * 4096 + (cb & 7) * 512 : WS_HP + (cb >> 2) * 32768 + (l & 7) * 4096 + kh * 2048 + (cb & 3) * 512)
                      + g * 32 + (l & 8) * 2;
#pragma nounroll
      for (int ct = 0; ct < 4; ++ct) {
        f32x4 d = {0.f, 0.f, 0.f, 0.f};
#pragma unroll
        for (int k = 0; k < 8; ++k) d = mfma32(tile_frag(L + L_WT, l, k, ct), xb[k], d);
        if (ffn2) *(LAS f32x4*)(L + L_FF + ct * 1024 + l * 16) = d * og;
        else __builtin_amdgcn_raw_buffer_store_b128(gran_d(d, l, tg), rws, off + ct * 128, 0, AUX_E);
      }
      DRAIN();
      if (l == 0) LDS_SET(L_HRDY);
      LDSWAIT();
    }
    return;
  }
  if (w == 1) {
    const v4u xv = *(const GAS v4u*)(a.x + rb * DM + l * 8), gv = *(const GAS v4u*)(a.gw + l * 8);
    const v2u bb = *(const GAS v2u*)(a.b2 + rc * 16 + 4 * (l & 3));
    const unsigned tg = (unsigned)tag, th = (unsigned)(tag >> 32);
    {
      const float gt = gate_of(xv, gv), og = rbf(1.f - gt);
      if (l < 4) *(LAS f32x4*)(L + L_FIN + l * 16) = f32x4{bf_lo(bb.x), bf_hi(bb.x), bf_lo(bb.y), bf_hi(bb.y)} * og;
      if (l == 0) *(LAS float*)(L + L_FIN + 64) = gt;
    }
    lds_wait<4>(L + L_ORG + 4);
    const unsigned org = *(volatile const LAS unsigned*)(L + L_ORG);
    if (l < 8) __builtin_amdgcn_raw_buffer_store_b128(v4u{org, tg, org, tg}, rws, WS_X + c * 128 + l * 16, 0, AUX_E);
    { unsigned tq = __builtin_amdgcn_readfirstlane(org) + (TQ - T0) * 16; WAIT_NEXT(tq, 0); }
    v2u st;
#pragma nounroll
    for (int spin = 0; spin < SPIN_CAP;) {
      v4u t[8];
#pragma unroll
      for (int i = 0; i < 8; ++i) { t[i] = __builtin_amdgcn_raw_buffer_load_b128(rws, WS_QP + kvh * 65536 + b * 8192 + l * 16, i * 1024, AUX_E); SLEEP(6); }
      st = __builtin_amdgcn_raw_buffer_load_b64(rws, WS_X + ((l & 7) + 16 + ((l >> 3) & 3) * 64) * 128, 0, AUX_SC1);
      int miss = -1;
#pragma unroll
      for (int j = 0; j < 4; ++j) {
        miss = gbad(t[j], tg) ? j : gbad(t[4 + j], tg) ? 4 + j : miss;
        const f32x2 q = gval(t[j]) + gval(t[4 + j]);
        *(LAS unsigned*)(L + L_Q + j * 256 + l * 4) = cvtpk(q.x, q.y);
      }
      if (__builtin_amdgcn_ballot_w64(miss >= 0) == 0) break;
      watch(rws, miss < 0 ? -1 : WS_QP + kvh * 65536 + b * 8192 + l * 16 + miss * 1024, tg, spin);
    }
    {
      unsigned rel = (st.y == tg ? st.x : (now_ticks() << 4) - ((J1 + 1) * DT0 + 2400)) - org;
      rel += dppu<0xB1>(rel); rel += dppu<0x4E>(rel); rel += dppu<0x141>(rel); rel += dppu<0x140>(rel);
      const int sum = __builtin_amdgcn_readlane((int)rel, 0) + __builtin_amdgcn_readlane((int)rel, 16);
      if (l == 0) *(LAS unsigned*)(L + L_TEND) = org + (sum >> 5) + (J1 + 1) * DT0 + (31 - J1) * DT + 32 * DTV;
    }
    LDSWAIT();
    if (l == 0) LDS_SET(L_QRDY);
    if (side) lds_wait<8>(L + L_HRDY);
    DRAIN();
    if (l == 0) __builtin_amdgcn_raw_buffer_store_b64(v2u{tg, th}, rws, WS_F + c * 8, 0, AUX_SC1);
    lds_wait<4>(L + L_ML + (l & 31) * 16 + 8);
    const v2u ml = *(volatile const LAS v2u*)(L + L_ML + (l & 31) * 16);
    const float mw = __uint_as_float(ml.x);
    float M = fmaxf(mw, dpp<0xB1>(mw)); M = fmaxf(M, dpp<0x4E>(M)); M = fmaxf(M, dpp<0x141>(M));
    float Ls = __builtin_amdgcn_exp2f(mw - M) * __uint_as_float(ml.y);
    Ls += dpp<0xB1>(Ls); Ls += dpp<0x4E>(Ls); Ls += dpp<0x141>(Ls);
    const float lse = M + __builtin_amdgcn_logf(Ls);
    const v4u lg = gran2(lse, dpp<0x128>(lse), tg);
    if (l < 32 && (l & 15) == 0) ST_SC1(lg, WS_LSE + ((b * 8 + kvh) * 4 + sp) * 128 + l);
    v2u ls;
    SLEEP(32);
#pragma nounroll
    for (int spin = 0; spin < SPIN_CAP; ++spin) {
      ls = __builtin_amdgcn_raw_buffer_load_b64(rws, WS_LSE + ((b * 8 + kvh) * 4 + (l & 3)) * 128 + ((l >> 3) & 3) * 8, 0, AUX_SC1);
      if (__builtin_amdgcn_ballot_w64(ls.y != tg) == 0) break;
      SLEEP(8);
    }
    const float lf = __uint_as_float(ls.x);
    float M2 = fmaxf(lf, dpp<0xB1>(lf)); M2 = fmaxf(M2, dpp<0x4E>(M2));
    float E = __builtin_amdgcn_exp2f(lf - M2);
    E += dpp<0xB1>(E); E += dpp<0x4E>(E);
    if (l < 32) *(LAS float*)(L + L_CF + l * 4) = __builtin_amdgcn_exp2f(mw - M2) * __builtin_amdgcn_rcpf(E);
    WAIT_TICK(t_in, TF);
#pragma nounroll
    for (int spin = 0; spin < SPIN_CAP; ++spin) {
      const v4u f0 = LD_SC1(WS_F + l * 16), f1 = LD_SC1(WS_F + 1024 + l * 16);
      if (__builtin_amdgcn_ballot_w64(((f0.x ^ tg) | (f0.y ^ th) | (f0.z ^ tg) | (f0.w ^ th) | (f1.x ^ tg) | (f1.y ^ th) | (f1.z ^ tg) | (f1.w ^ th)) != 0) == 0) break;
      SLEEP(16);
    }
    if (c == 0 && l == 0) __builtin_amdgcn_raw_buffer_store_b64(v2u{tg, th}, rws, WS_E, 0, AUX_SC1);
    LDSWAIT();
    lds_wait<1>(L + L_DONE + (l & 7) * 4);
    SLEEP(4);
    DMA_LOOP("nt", make_srd(a.wo + oh * 128 * DM + on * 64, 127 * 1024 + 128), (unsigned)(l >> 3) * 1024u + (unsigned)(l & 7) * 16u, 8192, lbase + L_WO, 16);
    WGBAR();
    DRAIN();
    if (l == 0) LDS_SET(L_WRDY);
    LDSWAIT();
    return;
  }

  const int sw = w - 2;
  const unsigned stag = sw * (DT / 8);
  unsigned tnext = ((t_in + T0) << 4) + stag, dt = DT0, tend = 0;
#define PACE() do { asm volatile("s_waitcnt vmcnt(%0)" :: "n"(WIN - 1) : "memory"); WAIT_NEXT(tnext, dt); } while (0)
#define RESLIP() do { const unsigned t1_ = (now_ticks() - SLIP) << 4; if ((int)(t1_ - tnext) > 0) tnext = t1_ + (stag >> 1); } while (0)
#define REPLAN(done, nominal) do { RESLIP(); \
    const int ex_ = (int)(tnext + ((done) < 32 ? (32 - (done)) * DT + 32 * DTV : (64 - (done)) * DTV) - stag - tend); \
    dt = (nominal) - sclamp<-DN, UP>((ex_ * (65536 / (64 - (done)))) >> 16); } while (0)
  const unsigned x0 = (((sw & 1) * 136) ^ (((sw >> 1) & 1) * 132) ^ ((sw >> 2) * 256) ^ ((sp & 1) * 4) ^ ((sp >> 1) * 88) ^ ((kvh & 1) * 48) ^
                       (((kvh >> 1) & 1) * 32) ^ ((kvh >> 2) * 2880) ^ ((b & 1) * 3624) ^ (((b >> 1) & 1) * 2516)) << 8;
#define OFF(i) (x0 ^ (((((i) & 1) ? 96u : 0u) ^ (((i) & 2) ? 68u : 0u) ^ (((i) & 4) ? 2048u : 0u) ^ (((i) & 8) ? 1280u : 0u) ^ (((i) & 16) ? 2560u : 0u)) << 8))
  const size_t base = (size_t)(b * HKV + kvh) * SK * 256;
  const v4u rk = make_srd(a.kc + base, SK * 256), rv = make_srd(a.vc + base, SK * 256);
  const int ko = ((l >> 2) & 3) * 256 + g * 64 + (l & 3) * 16, vo = l * 16;
#define LOAD_V(i) asm volatile("buffer_load_dwordx4 %0, %1, %2, %3 offen nt" : "+v"(kv[i]) : "v"(vo), "s"(rv), "s"(OFF(i)) : "memory")
  const unsigned tg = (unsigned)tag;
  { const unsigned t1 = ((now_ticks() - SLIP0) << 4) + stag; if ((int)(t1 - tnext) > 0) tnext = t1; }
  v4u kv[32];
  __builtin_amdgcn_sched_barrier(0);
#pragma unroll
  for (int i = 0; i < 32; ++i) {
    PACE();
    asm volatile("buffer_load_dwordx4 %0, %1, %2, %3 offen nt" : "=v"(kv[i]) : "v"(ko), "s"(rk), "s"(OFF(i)) : "memory");
    if (i == WIN) RESLIP();
    if (i == J1) {
      RESLIP();
      const unsigned org = tnext - stag - (J1 + 1) * DT0;
      if (sw == 3 && l == 0) { *(volatile LAS unsigned*)(L + L_ORG) = org; LDS_SET(L_ORG + 4); }
      dt = DT;
    }
    if (i == JA) {
      const unsigned f = __builtin_amdgcn_readfirstlane(*(volatile const LAS unsigned*)(L + L_QRDY));
      tend = __builtin_amdgcn_readfirstlane(*(volatile const LAS unsigned*)(L + L_TEND));
      if (f) REPLAN(JA + 1, DT);
    }
    __builtin_amdgcn_sched_barrier(0);
  }
  lds_wait<2>(L + L_QRDY);
  const v4u qf = *(const LAS v4u*)(L + L_Q + ((l >> 2) & 3) * 256 + g * 64 + (l & 3) * 16);
  tend = __builtin_amdgcn_readfirstlane(*(const LAS unsigned*)(L + L_TEND));
  REPLAN(32, DTV);
  __builtin_amdgcn_sched_barrier(0);
  constexpr float C2 = 0.088388347648318440f * 1.4426950408889634f;
  float sc[8];
  const int s = l & 3;
#pragma unroll
  for (int m = 0; m < 8; ++m) {
    asm volatile("s_waitcnt vmcnt(%4)" : "+v"(kv[4 * m]), "+v"(kv[4 * m + 1]), "+v"(kv[4 * m + 2]), "+v"(kv[4 * m + 3]) : "n"(28 - 4 * m + m) : "memory");
    f32x4 d[4];
#pragma unroll
    for (int r = 0; r < 4; ++r) d[r] = mfma32(kv[4 * m + r], qf, f32x4{0.f, 0.f, 0.f, 0.f});
#pragma unroll
    for (int r = 0; r < 4; ++r) {
      float t = dpp<0x00>(d[r][0]); t = dpp<0x55>(d[r][1]) + t; t = dpp<0xAA>(d[r][2]) + t; t = dpp<0xFF>(d[r][3]) + t;
      asm volatile("" : "+v"(t));
      sc[m] = (r == 0 || s == r) ? t : sc[m];
    }
    asm volatile("" : "+v"(sc[m]));
    __builtin_amdgcn_sched_barrier(0);
    PACE();
    LOAD_V(m);
    __builtin_amdgcn_sched_barrier(0);
  }
  float mx = sc[0];
#pragma unroll
  for (int m = 1; m < 8; ++m) mx = fmaxf(mx, sc[m]);
  mx = fmaxf(mx, dpp<0xB1>(mx)); mx = fmaxf(mx, dpp<0x4E>(mx)); mx = xrow_max(mx);
  const float mneg = -mx * C2;
  float lsum = 0.f;
  unsigned pk[8];
#pragma unroll
  for (int m = 0; m < 8; ++m) { const float p = __builtin_amdgcn_exp2f(fmaf(sc[m], C2, mneg)); pk[m] = cvtpk(p, p); lsum += bf_lo(pk[m]); }
  lsum += dpp<0xB1>(lsum); lsum += dpp<0x4E>(lsum); lsum = xrow_sum(lsum);
  if (l < 16 && (l & 3) == 0) {
    *(volatile LAS v2u*)(L + L_ML + ((l >> 2) * 8 + sw) * 16) = v2u{__float_as_uint(mx * C2), __float_as_uint(lsum)};
    LDS_SET(L_ML + ((l >> 2) * 8 + sw) * 16 + 8);
  }
  const unsigned half = (l & 1) ? 0xffff0000u : 0x0000ffffu;
  const unsigned m0 = (l & 2) ? 0u : half, m1 = (l & 2) ? half : 0u;
  f32x4 o0 = {0.f, 0.f, 0.f, 0.f}, o1 = {0.f, 0.f, 0.f, 0.f};
  const int s4 = sw & 3, pb8 = l & 7;
  unsigned a_mw, a_mr, a_g0, a_l0, a_l1, a_g1;
#pragma unroll
  for (int j = 8; j < 40; ++j) {
    if (j < 32) { PACE(); LOAD_V(j); if (j == 15) REPLAN(48, DTV); __builtin_amdgcn_sched_barrier(0); }
    if (j == 31) { if (l == 0) LDS_SET(L_DONE + sw * 4); }
    if (j == 33) {
      a_mw = L_MRG + (sw * 4 + g) * 512 + (l & 15) * 32;
      a_mr = L_MRG + s4 * 512 + l * 8;
      if (sw < 4) {
        a_g0 = (l & 3) == 0 ? WS_P + (((kvh * 4 + s4) * 8 + b) * 4 + sp) * 256 + l * 4 : WS_END;
        a_l0 = L_WO + (8 * g + ((l & 15) >> 2)) * 128 + (l & 3) * 8 + s4 * 32;
        a_l1 = L_PG + (pb8 * 4 + ((l >> 3) & 1)) * 272 + g * 16;
        a_g1 = (l & 8) == 0 ? WS_O + (((on * 4 + s4) * 4 + (pb8 >> 1)) * 32 + oh) * 128 + (pb8 & 1) * 64 + g * 16 : WS_END;
      } else {
        a_g0 = WS_P + (oh * 8 + 2 * s4) * 1024 + l * 16;
        a_l0 = L_PG + (8 * s4 + g) * 272 + (l & 15) * 16;
        a_l1 = (l & 15) < 4 ? (rb * DM + rc * 16 + 4 * (l & 3) + g) * 2 : 8192;
        a_g1 = WS_O + (c >> 1) * 4096 + (l >> 2) * 128 + (c & 1) * 64 + (l & 3) * 16;
      }
      asm volatile("" : "+v"(a_mw), "+v"(a_mr), "+v"(a_g0), "+v"(a_l0), "+v"(a_l1), "+v"(a_g1));
      __builtin_amdgcn_sched_barrier(0);
    }
    const int i = j - 8;
    const unsigned pb = (i & 3) == 0 ? dppu<0x00>(pk[i >> 2]) : (i & 3) == 1 ? dppu<0x55>(pk[i >> 2]) : (i & 3) == 2 ? dppu<0xAA>(pk[i >> 2]) : dppu<0xFF>(pk[i >> 2]);
    const v2u av = {pb & m0, pb & m1};
    asm volatile("s_waitcnt vmcnt(%1)" : "+v"(kv[i]) : "n"((j < 32 ? j : 31) - i) : "memory");
    o0 = mfma16(av, v2u{kv[i].x, kv[i].y}, o0);
    o1 = mfma16(av, v2u{kv[i].z, kv[i].w}, o1);
    __builtin_amdgcn_sched_barrier(0);
  }
  *(LAS f32x4*)(L + a_mw) = o0;
  *(LAS f32x4*)(L + a_mw + 16) = o1;
  if (sw < 4) {
    asm volatile("s_waitcnt lgkmcnt(0)\n\ts_barrier ; merge" ::: "memory");
    const f32x4 c0 = *(const LAS f32x4*)(L + L_CF + sw * 32), c1 = *(const LAS f32x4*)(L + L_CF + sw * 32 + 16);
    f32x2 ac0 = {0.f, 0.f}, ac1 = {0.f, 0.f};
#pragma unroll
    for (int j = 0; j < 4; ++j) {
      ac0 += *(const LAS f32x2*)(L + a_mr + j * 2048) * c0[j];
      ac1 += *(const LAS f32x2*)(L + a_mr + (4 + j) * 2048) * c1[j];
    }
    ac0 += ac1;
    const unsigned pv = cvtpk(ac0.x, ac0.y);
    ST_SC1((v4u{pv, dppu<0x55>(pv), dppu<0xAA>(pv), dppu<0xFF>(pv)}), a_g0);
    f32x4 ff = {0.f, 0.f, 0.f, 0.f};
    float gm = 1.f;
    if (ffn2) { ff = *(const LAS f32x4*)(L + L_FF + sw * 1024 + l * 16); gm = *(const LAS float*)(L + L_G + pb8 * 4); }
    lds_wait<0>(L + L_WRDY);
    v4u wf[4];
#pragma unroll
    for (int k = 0; k < 4; ++k) { const v2u t0 = tr_read(L + a_l0 + k * 4096), t1 = tr_read(L + a_l0 + k * 4096 + 512); wf[k] = v4u{t0.x, t0.y, t1.x, t1.y}; }
    asm volatile("" : "+v"(wf[0]), "+v"(wf[1]), "+v"(wf[2]), "+v"(wf[3]), "+v"(ff), "+v"(gm));
    WGBAR();
    v4u pa[8];
#pragma unroll
    for (int k = 0; k < 8; ++k) pa[k] = *(const LAS v4u*)(L + a_l1 + (k & 1) * 544 + (k >> 1) * 64);
    __builtin_amdgcn_sched_barrier(0);
    f32x4 da = {0.f, 0.f, 0.f, 0.f};
#pragma unroll
    for (int k = 0; k < 8; ++k) da = mfma32(wf[k >> 1], pa[k], da);
    v4u r;
#pragma unroll
    for (int j = 0; j < 4; ++j) r[j] = __float_as_uint(fmaf(da[j] + dpp<0x128>(da[j]), gm, ff[j]));
    r.y &= ~1u; r.w &= ~1u;
    ST_SC1(r, a_g1);
  } else {
    v4u p0, p1;
    asm volatile("s_waitcnt lgkmcnt(0)\n\ts_barrier\n\ts_sleep 18" ::: "memory");
    poll2(rws, a_g0, a_g0 + 1024, tg, p0, p1);
    *(LAS v4u*)(L + a_l0) = p0;
    *(LAS v4u*)(L + a_l0 + 4 * 272) = p1;
    LDSWAIT();
    WGBAR();
    if (sw == 4) {
      float fin = *(const LAS float*)(L + L_FIN + (l & 3) * 16 + g * 4);
      const float gt = *(const LAS float*)(L + L_FIN + 64);
      float g0 = (l & 12) == 12 ? 1.f : gt, g1 = l < 48 ? g0 : gt;
      const __amdgpu_buffer_rsrc_t rout = __builtin_amdgcn_make_buffer_rsrc((void*)a.out, 0, 8192, 0x00020000);
      asm volatile("s_sleep 17" : "+v"(fin), "+v"(g0), "+v"(g1) :: "memory");
      v4u r0, r1;
      poll2(rws, a_g1, a_g1 + 2048, tg, r0, r1);
      const f32x4 av = __builtin_bit_cast(f32x4, r0) * g0 + __builtin_bit_cast(f32x4, r1) * g1;
      auto s0 = __builtin_amdgcn_permlane16_swap(__float_as_uint(av[0]), __float_as_uint(av[1]), false, false);
      auto s1 = __builtin_amdgcn_permlane16_swap(__float_as_uint(av[2]), __float_as_uint(av[3]), false, false);
      const float u0 = __uint_as_float(s0[0]) + __uint_as_float(s0[1]), u1 = __uint_as_float(s1[0]) + __uint_as_float(s1[1]);
      auto s2 = __builtin_amdgcn_permlane32_swap(__float_as_uint(u0), __float_as_uint(u1), false, false);
      float v = __uint_as_float(s2[0]) + __uint_as_float(s2[1]);
      v += dpp<0x124>(v); v += dpp<0x128>(v);
      v += fin;
      __builtin_amdgcn_raw_buffer_store_b16((unsigned short)cvtpk(v, v), rout, a_l1, 0, 0);
    }
  }
}

extern "C" void kernel_launch(void* const* d_in, const int* in_sizes, int n_in, void* d_out, int out_size, void* d_ws, size_t ws_size,
                              hipStream_t stream) {
  Args a;
  a.x = (const unsigned short*)d_in[0];
  a.kc = (const unsigned char*)d_in[1];
  a.vc = (const unsigned char*)d_in[2];
  a.wq = (const unsigned short*)d_in[3];
  a.wo = (const unsigned short*)d_in[4];
  a.w1 = (const unsigned short*)d_in[6];
  a.b1 = (const unsigned short*)d_in[7];
  a.w2 = (const unsigned short*)d_in[8];
  a.b2 = (const unsigned short*)d_in[9];
  a.gw = (const unsigned short*)d_in[10];
  a.ws = (unsigned char*)d_ws;
  a.out = (unsigned short*)d_out;
  fused_decode<<<256, NWAVE * 64, 0, stream>>>(a);
}
```
